```python
import torch

def _softmax(x, axis=-1):
    return torch.softmax(x, dim=axis)

def _gelu(x):
    return torch.nn.functional.gelu(x, approximate='tanh')

def _silu(x):
    return torch.nn.functional.silu(x)

def _sigmoid(x):
    return torch.sigmoid(x)

def _layernorm(x, axis=-1, eps=1e-05):
    m = x.mean(dim=axis, keepdim=True)
    v = x.var(dim=axis, keepdim=True, unbiased=False)
    return (x - m) / torch.sqrt(v + eps)

def _scatter_add(tgt, idx, src, axis=0):
    return tgt.clone().index_add_(axis, idx, src)

def _segment_sum(src, seg, n):
    out = torch.zeros((n,) + src.shape[1:], dtype=src.dtype)
    return out.index_add_(0, seg, src)

def _pad(x, pw):
    flat = tuple((v for pair in reversed(pw) for v in pair))
    return torch.nn.functional.pad(x, flat)
kernel_name = 'adaLN_cond__gqa_block_cached__swiglu_ffn__prompt'

def setup_inputs():

    _w = lambda a: (lambda t: t.to(torch.bfloat16) if t.is_floating_point() else t)(torch.as_tensor(a))

    def _A_setup():
        B, Sq, D = (1, 2048, 448)
        Hq, Hkv, Sk, Dh = (16, 8, 2048, 256)
        assert Hq % Hkv == 0, (Hq, Hkv)
        return {'x': _w((lambda *s: torch.randn(*s, dtype=torch.bfloat16) * 0.05)(B, Sq, D)), 'k_cache': _w((lambda *s: torch.randn(*s, dtype=torch.bfloat16) * 3.2)(B, Hkv, Sk, Dh)), 'v_cache': _w((lambda *s: torch.randn(*s, dtype=torch.bfloat16) * 0.4)(B, Hkv, Sk, Dh)), 'wq': _w((lambda *s: torch.randn(*s, dtype=torch.bfloat16) * 3.2)(D, Hq * Dh) * D ** (-0.5)), 'wo': _w((lambda *s: torch.randn(*s, dtype=torch.bfloat16) * 0.4)(Hq * Dh, D) * (Hq * Dh) ** (-0.5))}
    _A_in = _A_setup()

    def _B_setup():
        B, S, D, F = (1, 2048, 448, 1792)
        return {'x': _w((lambda *s: torch.randn(*s, dtype=torch.bfloat16) * 0.05)(B, S, D)), 'w_gate': _w((lambda *s: torch.randn(*s, dtype=torch.bfloat16) * 0.05)(D, F)), 'w_up': _w((lambda *s: torch.randn(*s, dtype=torch.bfloat16) * 0.05)(D, F)), 'w_down': _w((lambda *s: torch.randn(*s, dtype=torch.bfloat16) * 0.05)(F, D))}
    _B_in = _B_setup()
    return {'A_x': _A_in['x'], 'A_k_cache': _A_in['k_cache'], 'A_v_cache': _A_in['v_cache'], 'A_wq': _A_in['wq'], 'A_wo': _A_in['wo'], 'B_x': _B_in['x'], 'B_w_gate': _B_in['w_gate'], 'B_w_up': _B_in['w_up'], 'B_w_down': _B_in['w_down'], 'c': _w((lambda *s: torch.randn(*s, dtype=torch.bfloat16) * 0.2)(1, 448) * 8.0), 'w_mod': _w((lambda *s: torch.randn(*s, dtype=torch.bfloat16) * 0.2)(448, 6 * 448) * 448 ** (-0.5) * 8.0)}

def reference(A_x, A_k_cache, A_v_cache, A_wq, A_wo, B_x, B_w_gate, B_w_up, B_w_down, c, w_mod):

    def _A_ref(x, k_cache, v_cache, wq, wo):
        B, Sq, D = x.shape
        Hq, Dh = (16, 256)
        Hkv = k_cache.shape[1]
        rep = Hq // Hkv
        q = (lambda x, *d: x.permute(*d))((x @ wq).reshape(B, Sq, Hq, Dh), 0, 2, 1, 3)
        k = (lambda x, n, axis: torch.repeat_interleave(x, n, dim=axis))(k_cache, rep, axis=1)
        v = (lambda x, n, axis: torch.repeat_interleave(x, n, dim=axis))(v_cache, rep, axis=1)
        scores = torch.einsum('bhqd,bhkd->bhqk', q, k) * Dh ** (-0.5)
        p = _softmax(scores, axis=-1)
        o = torch.einsum('bhqk,bhkd->bhqd', p, v)
        return (lambda x, *d: x.permute(*d))(o, 0, 2, 1, 3).reshape(B, Sq, Hq * Dh) @ wo

    def _B_ref(x, w_gate, w_up, w_down):
        g = _silu(x @ w_gate)
        u = x @ w_up
        return g * u @ w_down
    x = A_x
    mod = (_silu(c) @ w_mod).reshape(c.shape[0], 6, 1, -1)
    sa, ca, ga, sb, cb, gb = (mod[:, 0].clone(), mod[:, 1].clone(), mod[:, 2].clone(), mod[:, 3].clone(), mod[:, 4].clone(), mod[:, 5].clone())
    h = _layernorm(x, axis=-1) * (1 + ca) + sa
    x = x + ga * _A_ref(h, A_k_cache, A_v_cache, A_wq, A_wo)
    h = _layernorm(x, axis=-1) * (1 + cb) + sb
    x = x + gb * _B_ref(h, B_w_gate, B_w_up, B_w_down)
    return x
```

```cpp
#include <hip/hip_runtime.h>
#include <hip/hip_bf16.h>
#include <stdint.h>

#define MIMIC_S 0
#define MIMIC_LN 1

typedef unsigned short u16;
typedef short bf16x8 __attribute__((ext_vector_type(8)));
typedef short s16x4 __attribute__((ext_vector_type(4)));
typedef float f32x16 __attribute__((ext_vector_type(16)));
typedef float f32x2 __attribute__((ext_vector_type(2)));
typedef __bf16 bf16x2_t __attribute__((ext_vector_type(2)));
typedef unsigned u32x4 __attribute__((ext_vector_type(4)));
typedef unsigned u32x2 __attribute__((ext_vector_type(2)));
#define DI __device__ __forceinline__
#define LDSP(T, p) ((__attribute__((address_space(3))) T*)(p))

constexpr int SEQ = 2048, DM = 448, HQ = 16, HKV = 8, DH = 256, FF = 1792, QD = HQ * DH;

DI float bf2f(u16 x) { return __uint_as_float((unsigned)x << 16); }
DI unsigned cvtpk(float lo, float hi) { f32x2 v = {lo, hi}; bf16x2_t b = __builtin_convertvector(v, bf16x2_t); return __builtin_bit_cast(unsigned, b); }
DI u16 f2bf(float x) { return (u16)(cvtpk(x, x) & 0xffffu); }
DI float rbf(float x) { return bf2f(f2bf(x)); }
DI float siluf(float x) { return x / (1.f + __expf(-x)); }

__global__ __launch_bounds__(256) void k_mod(const u16* __restrict__ c, const u16* __restrict__ wmod, float* __restrict__ mod) {
  __shared__ float sc[DM];
  __shared__ float red[4][64];
  const int tid = threadIdx.x;
  for (int i = tid; i < DM; i += 256) sc[i] = rbf(siluf(bf2f(c[i])));
  __syncthreads();
  const int col = blockIdx.x * 64 + (tid & 63), kg = tid >> 6;
  float acc = 0.f;
  for (int k = kg * 112; k < kg * 112 + 112; ++k) acc += sc[k] * bf2f(wmod[(size_t)k * (6 * DM) + col]);
  red[kg][tid & 63] = acc;
  __syncthreads();
  if (tid < 64) mod[col] = rbf(red[0][tid] + red[1][tid] + red[2][tid] + red[3][tid]);
}

__global__ __launch_bounds__(256) void k_ln(const u16* __restrict__ x, const float* __restrict__ shift, const float* __restrict__ scale, u16* __restrict__ h) {
  const int row = blockIdx.x * 4 + (threadIdx.x >> 6), lane = threadIdx.x & 63;
  float v[7]; float s = 0.f;
  for (int i = 0; i < 7; ++i) { v[i] = bf2f(x[(size_t)row * DM + lane + 64 * i]); s += v[i]; }
  for (int o = 32; o; o >>= 1) s += __shfl_xor(s, o);
  const float mean = s * (1.f / DM);
  float q = 0.f;
  for (int i = 0; i < 7; ++i) { const float d = v[i] - mean; q += d * d; }
  for (int o = 32; o; o >>= 1) q += __shfl_xor(q, o);
  const float var = q * (1.f / DM);
  const float mb = rbf(mean), vb = rbf(var);
  const float sd = rbf(sqrtf(rbf(vb + 1e-5f)));
  for (int i = 0; i < 7; ++i) {
    const int cidx = lane + 64 * i;
    const float n = rbf(rbf(v[i] - mb) / sd);
    const float t = rbf(n * rbf(1.f + scale[cidx]));
    h[(size_t)row * DM + cidx] = f2bf(t + shift[cidx]);
  }
}

template <int MODE>
__global__ __launch_bounds__(256) void k_gemm(const u16* __restrict__ X, const u16* __restrict__ W, const u16* __restrict__ W2, int N, int K,
                                              const u16* __restrict__ res, const float* __restrict__ gate, u16* __restrict__ out) {
  constexpr int NW = MODE == 2 ? 2 : 1;
  __shared__ __attribute__((aligned(16))) char smem[128 * 128 + 2 * 64 * 128];
  char* Xs = smem; char* Ws = smem + 128 * 128;
  const int tid = threadIdx.x, wid = tid >> 6, lane = tid & 63, r = lane & 31, h = lane >> 5;
  const int m0 = blockIdx.y * 128, n0 = blockIdx.x * 64;
  f32x16 acc[NW][2];
  for (int w = 0; w < NW; ++w) for (int i = 0; i < 2; ++i) for (int j = 0; j < 16; ++j) acc[w][i][j] = 0.f;
  const int i16 = lane & 15, q = i16 >> 2, p = i16 & 3, ch = (lane >> 4) & 1;
  for (int k0 = 0; k0 < K; k0 += 64) {
    __syncthreads();
    for (int i = 0; i < 4; ++i) {
      const int id = tid + 256 * i, row = id >> 3, c = id & 7;
      const u32x4 v = *(const u32x4*)(X + (size_t)(m0 + row) * K + k0 + c * 8);
      *(u32x4*)(Xs + row * 128 + ((c ^ ((row >> 1) & 7)) << 4)) = v;
    }
    for (int w = 0; w < NW; ++w)
      for (int i = 0; i < 2; ++i) {
        const int id = tid + 256 * i, row = id >> 3, c = id & 7;
        const u32x4 v = *(const u32x4*)((w ? W2 : W) + (size_t)(k0 + row) * N + n0 + c * 8);
        *(u32x4*)(Ws + w * 8192 + row * 128 + ((c ^ (((row >> 1) & 1) << 2)) << 4)) = v;
      }
    __syncthreads();
#pragma unroll
    for (int s = 0; s < 4; ++s) {
      const int row = wid * 32 + r;
      const bf16x8 b = *(const bf16x8*)(Xs + row * 128 + (((2 * s + h) ^ ((row >> 1) & 7)) << 4));
#pragma unroll
      for (int w = 0; w < NW; ++w)
#pragma unroll
        for (int nb = 0; nb < 2; ++nb) {
          s16x4 lo, hi;
          {
            const int kr = 16 * s + 8 * h + q; const int c = 4 * nb + 2 * ch + (p >> 1);
            lo = __builtin_amdgcn_ds_read_tr16_b64_v4i16(LDSP(s16x4, Ws + w * 8192 + kr * 128 + ((c ^ (((kr >> 1) & 1) << 2)) << 4) + 8 * (p & 1)));
          }
          {
            const int kr = 16 * s + 8 * h + 4 + q; const int c = 4 * nb + 2 * ch + (p >> 1);
            hi = __builtin_amdgcn_ds_read_tr16_b64_v4i16(LDSP(s16x4, Ws + w * 8192 + kr * 128 + ((c ^ (((kr >> 1) & 1) << 2)) << 4) + 8 * (p & 1)));
          }
          const bf16x8 a = __builtin_shufflevector(lo, hi, 0, 1, 2, 3, 4, 5, 6, 7);
          acc[w][nb] = __builtin_amdgcn_mfma_f32_32x32x16_bf16(a, b, acc[w][nb], 0, 0, 0);
        }
    }
  }
  const int m = m0 + wid * 32 + r;
#pragma unroll
  for (int nb = 0; nb < 2; ++nb)
#pragma unroll
    for (int g = 0; g < 4; ++g) {
      const int n = n0 + nb * 32 + 8 * g + 4 * h;
      float o[4];
#pragma unroll
      for (int j = 0; j < 4; ++j) {
        const float c1 = acc[0][nb][4 * g + j];
        if (MODE == 0) o[j] = c1;
        else if (MODE == 1) o[j] = bf2f(res[(size_t)m * N + n + j]) + rbf(gate[n + j] * rbf(c1));
        else { const float c2 = acc[NW - 1][nb][4 * g + j]; o[j] = rbf(siluf(rbf(c1))) * rbf(c2); }
      }
      u32x2 w2 = {cvtpk(o[0], o[1]), cvtpk(o[2], o[3])};
      *(u32x2*)(out + (size_t)m * N + n) = w2;
    }
}

__global__ __launch_bounds__(256) void k_attn(const u16* __restrict__ Q, const u16* __restrict__ Kc, const u16* __restrict__ Vc, u16* __restrict__ O) {
  __shared__ __attribute__((aligned(16))) char smem[2 * 32 * 512];
  char* Ks = smem; char* Vs = smem + 32 * 512;
  const int tid = threadIdx.x, wid = tid >> 6, lane = tid & 63, r = lane & 31, h = lane >> 5;
  const int bid = blockIdx.x, kvh = bid & 7, rest = bid >> 3, head = kvh * 2 + (rest & 1), qb = rest >> 1;
  const int qrow = qb * 128 + wid * 32 + r;
  bf16x8 qf[16];
#pragma unroll
  for (int s = 0; s < 16; ++s) qf[s] = *(const bf16x8*)(Q + (size_t)qrow * QD + head * DH + 16 * s + 8 * h);
  f32x16 o[8];
#pragma unroll
  for (int d = 0; d < 8; ++d) for (int j = 0; j < 16; ++j) o[d][j] = 0.f;
  float mrun = -1e30f, lrun = 0.f;
  const int i16 = lane & 15, q4 = i16 >> 2, p = i16 & 3, ch = (lane >> 4) & 1;
  const u16* Kh = Kc + (size_t)kvh * SEQ * DH; const u16* Vh = Vc + (size_t)kvh * SEQ * DH;
  constexpr float C = 1.4426950408889634f / 16.f;
  for (int t = 0; t < SEQ / 32; ++t) {
    __syncthreads();
    for (int i = 0; i < 4; ++i) {
      const int id = tid + 256 * i, row = id >> 5, c = id & 31;
      const u32x4 kv = *(const u32x4*)(Kh + (size_t)(t * 32 + row) * DH + c * 8);
      const u32x4 vv = *(const u32x4*)(Vh + (size_t)(t * 32 + row) * DH + c * 8);
      *(u32x4*)(Ks + row * 512 + ((c ^ (row & 15)) << 4)) = kv;
      *(u32x4*)(Vs + row * 512 + ((c ^ ((row & 3) << 2)) << 4)) = vv;
    }
    __syncthreads();
    f32x16 s = {};
#pragma unroll
    for (int ks = 0; ks < 16; ++ks) {
      const bf16x8 a = *(const bf16x8*)(Ks + r * 512 + (((2 * ks + h) ^ (r & 15)) << 4));
      s = __builtin_amdgcn_mfma_f32_32x32x16_bf16(a, qf[ks], s, 0, 0, 0);
    }
    float mx = s[0];
#pragma unroll
    for (int j = 1; j < 16; ++j) mx = fmaxf(mx, s[j]);
    mx = fmaxf(mx, __shfl_xor(mx, 32));
    const float mn = fmaxf(mrun, mx);
    const float alpha = __builtin_amdgcn_exp2f((mrun - mn) * C);
    mrun = mn;
    float ps = 0.f;
#pragma unroll
    for (int j = 0; j < 16; ++j) { s[j] = __builtin_amdgcn_exp2f((s[j] - mn) * C); ps += s[j]; }
    ps += __shfl_xor(ps, 32);
    lrun = lrun * alpha + ps;
#pragma unroll
    for (int d = 0; d < 8; ++d) for (int j = 0; j < 16; ++j) o[d][j] *= alpha;
    bf16x8 pf[2];
#pragma unroll
    for (int ks = 0; ks < 2; ++ks) {
      u32x4 w = {cvtpk(s[8 * ks], s[8 * ks + 1]), cvtpk(s[8 * ks + 2], s[8 * ks + 3]), cvtpk(s[8 * ks + 4], s[8 * ks + 5]), cvtpk(s[8 * ks + 6], s[8 * ks + 7])};
      pf[ks] = __builtin_bit_cast(bf16x8, w);
    }
#pragma unroll
    for (int d = 0; d < 8; ++d)
#pragma unroll
      for (int ks = 0; ks < 2; ++ks) {
        const int c = 4 * d + 2 * ch + (p >> 1);
        const int k0 = 16 * ks + 4 * h + q4, k1 = k0 + 8;
        const s16x4 lo = __builtin_amdgcn_ds_read_tr16_b64_v4i16(LDSP(s16x4, Vs + k0 * 512 + ((c ^ ((k0 & 3) << 2)) << 4) + 8 * (p & 1)));
        const s16x4 hi = __builtin_amdgcn_ds_read_tr16_b64_v4i16(LDSP(s16x4, Vs + k1 * 512 + ((c ^ ((k1 & 3) << 2)) << 4) + 8 * (p & 1)));
        const bf16x8 a = __builtin_shufflevector(lo, hi, 0, 1, 2, 3, 4, 5, 6, 7);
        o[d] = __builtin_amdgcn_mfma_f32_32x32x16_bf16(a, pf[ks], o[d], 0, 0, 0);
      }
  }
  const float il = 1.f / lrun;
#pragma unroll
  for (int d = 0; d < 8; ++d)
#pragma unroll
    for (int g = 0; g < 4; ++g) {
      u32x2 w2 = {cvtpk(o[d][4 * g] * il, o[d][4 * g + 1] * il), cvtpk(o[d][4 * g + 2] * il, o[d][4 * g + 3] * il)};
      *(u32x2*)(O + (size_t)qrow * QD + head * DH + 32 * d + 8 * g + 4 * h) = w2;
    }
}

extern "C" void kernel_launch(void* const* d_in, const int* in_sizes, int n_in, void* d_out, int out_size, void* d_ws, size_t ws_size, hipStream_t stream) {
  const u16* x = (const u16*)d_in[0]; const u16* kc = (const u16*)d_in[1]; const u16* vc = (const u16*)d_in[2];
  const u16* wq = (const u16*)d_in[3]; const u16* wo = (const u16*)d_in[4];
  const u16* wg = (const u16*)d_in[6]; const u16* wu = (const u16*)d_in[7]; const u16* wd = (const u16*)d_in[8];
  const u16* c = (const u16*)d_in[9]; const u16* wmod = (const u16*)d_in[10];
  u16* out = (u16*)d_out;
  char* ws = (char*)d_ws;
  float* mod = (float*)ws;
  u16* h = (u16*)(ws + (1 << 16));
  u16* qb = (u16*)(ws + (4 << 20));
  u16* ob = (u16*)(ws + (24 << 20));
  u16* x1 = (u16*)(ws + (44 << 20));
  u16* h2 = (u16*)(ws + (48 << 20));
  u16* act = (u16*)(ws + (52 << 20));
  k_mod<<<42, 256, 0, stream>>>(c, wmod, mod);
  k_ln<<<SEQ / 4, 256, 0, stream>>>(x, mod + 0 * DM, mod + 1 * DM, h);
  k_gemm<0><<<dim3(QD / 64, SEQ / 128), 256, 0, stream>>>(h, wq, nullptr, QD, DM, nullptr, nullptr, qb);
  k_attn<<<256, 256, 0, stream>>>(qb, kc, vc, ob);
  k_gemm<1><<<dim3(DM / 64, SEQ / 128), 256, 0, stream>>>(ob, wo, nullptr, DM, QD, x, mod + 2 * DM, x1);
  k_ln<<<SEQ / 4, 256, 0, stream>>>(x1, mod + 3 * DM, mod + 4 * DM, h2);
  k_gemm<2><<<dim3(FF / 64, SEQ / 128), 256, 0, stream>>>(h2, wg, wu, FF, DM, nullptr, nullptr, act);
  k_gemm<1><<<dim3(DM / 64, SEQ / 128), 256, 0, stream>>>(act, wd, nullptr, DM, FF, x1, mod + 5 * DM, out);
}
```

```cpp
#define PF0 32
#define STM " sc1"
#define FS1 16
#define FS3 16
#include <hip/hip_runtime.h>
#include <stdint.h>

typedef unsigned short u16;
typedef float f32x2 __attribute__((ext_vector_type(2)));
typedef float f32x4 __attribute__((ext_vector_type(4)));
typedef __bf16 bf16x2_t __attribute__((ext_vector_type(2)));
typedef unsigned u32x4 __attribute__((ext_vector_type(4)));
typedef unsigned u32x2 __attribute__((ext_vector_type(2)));
typedef unsigned u32x16 __attribute__((ext_vector_type(16)));
#define DI __device__ __forceinline__

constexpr int DM = 448;

DI float bf2f(u16 x) { return __uint_as_float((unsigned)x << 16); }
DI unsigned cvtpk(float lo, float hi) { f32x2 v = {lo, hi}; bf16x2_t b = __builtin_convertvector(v, bf16x2_t); return __builtin_bit_cast(unsigned, b); }
DI u16 f2bf(float x) { return (u16)(cvtpk(x, x) & 0xffffu); }
DI float rbf(float x) { return bf2f(f2bf(x)); }
DI float siluf(float x) { return x / (1.f + __expf(-x)); }
DI void st16(void* p, u32x4 v) { asm volatile("global_store_dwordx4 %0, %1, off" STM "\ns_nop 1" :: "v"(p), "v"(v) : "memory"); }
DI void st4(void* p, float v) { asm volatile("global_store_dword %0, %1, off" STM :: "v"(p), "v"(v) : "memory"); }
template <int C> DI float dppadd(float v) { return v + __uint_as_float(__builtin_amdgcn_update_dpp(0u, __float_as_uint(v), C, 0xf, 0xf, false)); }
DI float add16(float v) { auto r = __builtin_amdgcn_permlane16_swap(__float_as_uint(v), __float_as_uint(v), false, false); return __uint_as_float(r[0]) + __uint_as_float(r[1]); }
DI float add32(float v) { auto r = __builtin_amdgcn_permlane32_swap(__float_as_uint(v), __float_as_uint(v), false, false); return __uint_as_float(r[0]) + __uint_as_float(r[1]); }
DI float red8up(float v) { return add32(add16(dppadd<0x128>(v))); }
DI float redall(float v) { return add32(add16(dppadd<0x124>(dppadd<0x128>(dppadd<0x4e>(dppadd<0xb1>(v)))))); }

#define NGB 84
__global__ __launch_bounds__(256) void k0(const u16* __restrict__ c, const u16* __restrict__ wmod, const u16* __restrict__ x,
                                          float* __restrict__ tab, float* __restrict__ mod, u16* __restrict__ nrm, const char* __restrict__ wq) {
  __shared__ float sc[DM];
  __shared__ __attribute__((aligned(16))) float red[4][32];
  const int tid = threadIdx.x, lane = tid & 63;
  if (blockIdx.x < NGB) {
    const int cg = tid & 3, kg = tid >> 2;
    const u16* wp = wmod + (size_t)kg * (6 * DM) + blockIdx.x * 32 + cg * 8;
#define NI 7
    const unsigned cc = tid < 224 ? ((const unsigned*)c)[tid] : 0u;
    u32x4 w[7];
#pragma unroll
    for (int i = 0; i < NI; ++i) w[i] = *(const u32x4*)(wp + (size_t)i * 64 * (6 * DM));
    if (tid < 224) {
      f32x2 sv = {rbf(siluf(__uint_as_float(cc << 16))), rbf(siluf(__uint_as_float(cc & 0xffff0000u)))};
      *(f32x2*)(sc + 2 * tid) = sv;
    }
    __syncthreads();
    float a[8] = {0, 0, 0, 0, 0, 0, 0, 0};
#pragma unroll
    for (int i = 0; i < NI; ++i) {
      const float s = sc[kg + 64 * i];
#pragma unroll
      for (int j = 0; j < 4; ++j) { a[2 * j] += s * __uint_as_float(w[i][j] << 16); a[2 * j + 1] += s * __uint_as_float(w[i][j] & 0xffff0000u); }
    }
#pragma unroll
    for (int j = 0; j < 8; ++j) a[j] = add32(add16(dppadd<0x124>(dppadd<0x128>(a[j]))));
    if (lane < 4) { *(f32x4*)&red[tid >> 6][cg * 8] = f32x4{a[0], a[1], a[2], a[3]}; *(f32x4*)&red[tid >> 6][cg * 8 + 4] = f32x4{a[4], a[5], a[6], a[7]}; }
    __syncthreads();
    if (tid < 32) {
      const float t = rbf((red[0][tid] + red[1][tid]) + (red[2][tid] + red[3][tid]));
      const int col = (blockIdx.x % 84) * 32 + tid;
      st4(mod + col, t);
      constexpr float C = 1.4426950408889634f / 16.f;
      if (col < DM) st4(tab + DM + col, t * C);
      else if (col < 2 * DM) st4(tab + col - DM, rbf(1.f + t) * C);
    }
  } else {
    const int b = blockIdx.x - NGB;
    const int row = b * 4 + (tid >> 6);
    const bool act = lane < 56;
    if (b < 4 && tid < 128) { const int i = (b & 1) * 128 + tid; st4((char*)tab + (b < 2 ? 0x1F80000 + (i >> 4) * 2048 + (i & 15) * FS3 : 32768 + (i >> 3) * 1024 + (i & 7) * FS1), 0.f); }
    u32x4 v = {0, 0, 0, 0};
    if (act) v = *(const u32x4*)(x + (size_t)row * DM + lane * 8);
    unsigned d1 = 0;
    if (b >= 8 && b < 11) {
      unsigned long long pc; unsigned o0, sz;
      asm volatile("s_getpc_b64 %0\nL_pc0:\ns_mov_b32 %1, L_K1-L_pc0\ns_mov_b32 %2, F_end-L_K1-4" : "=s"(pc), "=s"(o0), "=s"(sz));
      const unsigned o = (b - 8) * 16384 + tid * 64;
      const char* p = (const char*)pc + o0 + (o < sz ? o : sz);
      asm volatile("global_load_dword %0, %1, off" : "=v"(d1) : "v"(p));
    }
    float f[8];
#pragma unroll
    for (int j = 0; j < 4; ++j) { f[2 * j] = __uint_as_float(v[j] << 16); f[2 * j + 1] = __uint_as_float(v[j] & 0xffff0000u); }
    float s = 0.f;
#pragma unroll
    for (int j = 0; j < 8; ++j) s += f[j];
    s = redall(s);
    const float mean = s * (1.f / DM);
    unsigned d0 = 0;
    if (tid < PF0) {
      const char* p = wq + (size_t)(b * PF0 + tid) * 128;
      asm volatile("global_load_dword %0, %1, off" : "=v"(d0) : "v"(p), "v"(mean));
    }
    float q = 0.f;
#pragma unroll
    for (int j = 0; j < 8; ++j) { f[j] -= mean; q += act ? f[j] * f[j] : 0.f; }
    q = redall(q);
    const float rs = 1.f / sqrtf(q * (1.f / DM) + 1e-5f);
    if (act) {
      u32x4 o4 = {cvtpk(f[0] * rs, f[1] * rs), cvtpk(f[2] * rs, f[3] * rs), cvtpk(f[4] * rs, f[5] * rs), cvtpk(f[6] * rs, f[7] * rs)};
      st16(nrm + (size_t)row * DM + lane * 8, o4);
    }
    asm volatile("s_waitcnt vmcnt(0)" :: "v"(d0));
    asm volatile("" :: "v"(d1));
  }
}
#define PLIM1 400
#define LBAR asm volatile("s_waitcnt lgkmcnt(0)\ns_barrier" ::: "memory");

__global__ __launch_bounds__(256, 1) void k1(const u16* __restrict__ wq, const u16* __restrict__ Kc, const u16* __restrict__ Vc, const u16* __restrict__ wo, char* __restrict__ ws,
                                             const char* __restrict__ wg, const char* __restrict__ wu, const char* __restrict__ wd, const u16* __restrict__ x) {
  __shared__ __attribute__((aligned(16))) char smem[163840];
  const unsigned w = __builtin_amdgcn_readfirstlane(threadIdx.x >> 6);
  const unsigned kvh = blockIdx.x & 7, rb = blockIdx.x >> 3;
  const unsigned l = (unsigned)(size_t)smem;
  u32x16 ro[8];
  asm volatile(
    "s_mov_b32 s63, %18\n"
    "s_mov_b32 s66, %19\n"
    "s_mov_b32 s67, %20\n"
    "L_K1:\n"
    "s_lshl_b32 s64, s66, 20\n"
    "s_add_u32 s40, %10, s64\n"
    "s_addc_u32 s41, %11, 0\n"
    "s_add_u32 s44, %12, s64\n"
    "s_addc_u32 s45, %13, 0\n"
    "s_lshl_b32 s64, s66, 10\n"
    "s_add_u32 s48, %8, s64\n"
    "s_addc_u32 s49, %9, 0\n"
    "s_sub_u32 s50, 3670016, s64\n"
    "s_mul_i32 s64, s66, 458752\n"
    "s_add_u32 s52, %14, s64\n"
    "s_addc_u32 s53, %15, 0\n"
    "s_mov_b32 s68, %16\n"
    "s_mov_b32 s69, %17\n"
    ".p2align 3\n"
    "s_mov_b32 s70, 20971520\n"
    "s_mov_b32 s71, 0x20000\n"
    "s_and_b32 s69, s69, 0xffff\n"
    "s_mov_b32 s51, 0x20000\n"
    "s_and_b32 s49, s49, 0xffff\n"
    "v_mbcnt_lo_u32_b32 v234, -1, 0\n"
    "v_mbcnt_hi_u32_b32 v234, -1, v234\n"
    "v_and_b32 v225, 15, v234\n"
    "v_lshrrev_b32 v226, 4, v234\n"
    "v_lshlrev_b32 v187, 5, v225\n"
    "v_mul_u32_u24 v186, 896, v226\n"
    "v_lshl_add_u32 v186, v225, 4, v186\n"
    "s_lshl_b32 s82, s67, 2\n"
    "s_add_u32 s82, s82, s63\n"
    "s_mul_i32 s82, s82, 14336\n"
    "s_add_u32 s56, s82, 65536\n"
    "s_add_u32 s57, s82, 69120\n"
    "s_add_u32 s72, s82, 72704\n"
    "s_add_u32 s73, s82, 76288\n"
    "buffer_load_dwordx4 v[64:67], v187, s[68:71], 0 offen\n"
    "buffer_load_dwordx4 v[68:71], v187, s[68:71], 0 offen offset:16\n"
    "buffer_load_dwordx4 v[72:75], v187, s[68:71], 0 offen offset:1792\n"
    "buffer_load_dwordx4 v[76:79], v187, s[68:71], 0 offen offset:1808\n"
    "buffer_load_dwordx4 v[0:3], v186, s[68:71], s56 offen offset:0\n"
    "buffer_load_dwordx4 v[4:7], v186, s[68:71], s57 offen offset:0\n"
    "buffer_load_dwordx4 v[8:11], v186, s[68:71], s72 offen offset:0\n"
    "buffer_load_dwordx4 v[12:15], v186, s[68:71], s73 offen offset:0\n"
    "s_getpc_b64 s[84:85]\n"
    "s_lshl_b32 s83, s67, 2\n"
    "s_add_u32 s83, s83, s63\n"
    "s_lshl_b32 s83, s83, 8\n"
    "s_add_u32 s84, s84, s83\n"
    "s_addc_u32 s85, s85, 0\n"
    "s_load_dword s88, s[84:85], 0x0\n"
    "s_load_dword s88, s[84:85], 0x40\n"
    "s_load_dword s88, s[84:85], 0x80\n"
    "s_load_dword s88, s[84:85], 0xc0\n"
    "s_and_b32 s83, s63, 1\n"
    "s_lshl_b32 s84, s83, 3\n"
    "v_xor_b32 v226, s84, v234\n"
    "s_lshl_b32 s76, s63, 13\n"
    "s_lshl_b32 s74, s63, 16\n"
    "s_add_u32 s75, s74, 0x8000\n"
    "v_xor_b32 v227, 0, v226\n"
    "v_lshlrev_b32 v227, 4, v227\n"
    "v_add_u32 v236, 0, v227\n"
    "v_xor_b32 v227, 1, v226\n"
    "v_lshlrev_b32 v227, 4, v227\n"
    "v_add_u32 v237, 7168, v227\n"
    "v_xor_b32 v227, 4, v226\n"
    "v_lshlrev_b32 v227, 4, v227\n"
    "v_add_u32 v238, 14336, v227\n"
    "v_xor_b32 v227, 5, v226\n"
    "v_lshlrev_b32 v227, 4, v227\n"
    "v_add_u32 v239, 21504, v227\n"
    "s_add_u32 m0, s76, 65536\n"
    "s_nop 0\n"
    "buffer_load_dwordx4 v236, s[48:51], s74 offen offset:0 lds\n"
    "buffer_load_dwordx4 v237, s[48:51], s74 offen offset:1024 lds\n"
    "buffer_load_dwordx4 v238, s[48:51], s74 offen offset:2048 lds\n"
    "buffer_load_dwordx4 v239, s[48:51], s74 offen offset:3072 lds\n"
    "s_add_u32 s74, s74, 0x40000\n"
    "s_add_u32 m0, m0, 0x1000\n"
    "s_nop 0\n"
    "buffer_load_dwordx4 v236, s[48:51], s75 offen offset:0 lds\n"
    "buffer_load_dwordx4 v237, s[48:51], s75 offen offset:1024 lds\n"
    "buffer_load_dwordx4 v238, s[48:51], s75 offen offset:2048 lds\n"
    "buffer_load_dwordx4 v239, s[48:51], s75 offen offset:3072 lds\n"
    "v_lshlrev_b32 v249, 4, v234\n"
    "s_lshl_b32 s84, s63, 10\n"
    "s_add_u32 m0, s84, 157696\n"
    "s_mov_b32 s54, 458752\n"
    "buffer_load_dwordx4 v249, s[68:71], s84 offen lds\n"
    "s_mov_b32 s55, 0x20000\n"
    "s_and_b32 s53, s53, 0xffff\n"
    "s_mul_i32 s84, s67, 112\n"
    "s_mul_i32 s85, s63, 28\n"
    "s_add_u32 s84, s84, s85\n"
    "v_min_u32 v227, 27, v234\n"
    "v_add_u32 v227, s84, v227\n"
    "v_lshrrev_b32 v228, 3, v227\n"
    "v_and_b32 v229, 7, v227\n"
    "v_lshlrev_b32 v228, 13, v228\n"
    "v_lshl_add_u32 v228, v229, 7, v228\n"
    "v_lshlrev_b32 v253, 7, v227\n"
    "buffer_load_dword v251, v228, s[48:51], 0 offen\n"
    "buffer_load_dwordx4 v[128:131], v186, s[68:71], s56 offen offset:256\n"
    "buffer_load_dwordx4 v[132:135], v186, s[68:71], s57 offen offset:256\n"
    "buffer_load_dwordx4 v[136:139], v186, s[68:71], s72 offen offset:256\n"
    "buffer_load_dwordx4 v[140:143], v186, s[68:71], s73 offen offset:256\n"
    "s_add_u32 s75, s75, 0x40000\n"
    "s_add_u32 m0, s76, 32768\n"
    "s_nop 0\n"
    "buffer_load_dwordx4 v236, s[48:51], s74 offen offset:0 lds\n"
    "buffer_load_dwordx4 v237, s[48:51], s74 offen offset:1024 lds\n"
    "buffer_load_dwordx4 v238, s[48:51], s74 offen offset:2048 lds\n"
    "buffer_load_dwordx4 v239, s[48:51], s74 offen offset:3072 lds\n"
    "s_add_u32 s74, s74, 0x40000\n"
    "s_add_u32 m0, m0, 0x1000\n"
    "s_nop 0\n"
    "buffer_load_dwordx4 v236, s[48:51], s75 offen offset:0 lds\n"
    "buffer_load_dwordx4 v237, s[48:51], s75 offen offset:1024 lds\n"
    "buffer_load_dwordx4 v238, s[48:51], s75 offen offset:2048 lds\n"
    "buffer_load_dwordx4 v239, s[48:51], s75 offen offset:3072 lds\n"
    "s_mov_b32 s42, 0x100000\n"
    "s_mov_b32 s43, 0x20000\n"
    "s_mov_b32 s46, 0x100000\n"
    "s_mov_b32 s47, 0x20000\n"
    "s_and_b32 s41, s41, 0xffff\n"
    "s_and_b32 s45, s45, 0xffff\n"
    "s_mov_b32 s60, 0x5f800000\n"
    "s_mov_b32 s61, 0x42400000\n"
    "s_mov_b32 s77, 0x4000\n"
    "s_mov_b32 s81, 0xffff0000\n"
    "v_mbcnt_lo_u32_b32 v234, -1, 0\n"
    "v_mbcnt_hi_u32_b32 v234, -1, v234\n"
    "v_and_b32 v225, 15, v234\n"
    "v_lshrrev_b32 v226, 4, v234\n"
    "v_xor_b32 v227, v225, v226\n"
    "v_xor_b32 v228, 0, v227\n"
    "v_lshlrev_b32 v228, 4, v228\n"
    "v_lshl_add_u32 v200, v225, 9, v228\n"
    "v_xor_b32 v228, 4, v227\n"
    "v_lshlrev_b32 v228, 4, v228\n"
    "v_lshl_add_u32 v201, v225, 9, v228\n"
    "v_xor_b32 v228, 8, v227\n"
    "v_lshlrev_b32 v228, 4, v228\n"
    "v_lshl_add_u32 v202, v225, 9, v228\n"
    "v_xor_b32 v228, 12, v227\n"
    "v_lshlrev_b32 v228, 4, v228\n"
    "v_lshl_add_u32 v203, v225, 9, v228\n"
    "v_lshrrev_b32 v227, 2, v234\n"
    "v_and_b32 v228, 7, v227\n"
    "v_bfe_u32 v229, v234, 1, 1\n"
    "v_lshl_add_u32 v228, v228, 1, v229\n"
    "v_lshlrev_b32 v229, 9, v227\n"
    "v_and_b32 v230, 1, v234\n"
    "v_lshl_add_u32 v229, v230, 3, v229\n"
    "v_add_u32 v229, 0x10000, v229\n"
    "v_xor_b32 v230, 0, v228\n"
    "v_lshl_add_u32 v204, v230, 4, v229\n"
    "v_xor_b32 v230, 2, v228\n"
    "v_lshl_add_u32 v205, v230, 4, v229\n"
    "v_xor_b32 v230, 4, v228\n"
    "v_lshl_add_u32 v206, v230, 4, v229\n"
    "v_xor_b32 v230, 6, v228\n"
    "v_lshl_add_u32 v207, v230, 4, v229\n"
    "v_xor_b32 v230, 8, v228\n"
    "v_lshl_add_u32 v208, v230, 4, v229\n"
    "v_xor_b32 v230, 10, v228\n"
    "v_lshl_add_u32 v209, v230, 4, v229\n"
    "v_xor_b32 v230, 12, v228\n"
    "v_lshl_add_u32 v210, v230, 4, v229\n"
    "v_xor_b32 v230, 14, v228\n"
    "v_lshl_add_u32 v211, v230, 4, v229\n"
    "v_lshrrev_b32 v231, 5, v234\n"
    "v_and_b32 v228, 31, v234\n"
    "s_lshl_b32 s59, s63, 12\n"
    "v_lshlrev_b32 v229, 9, v231\n"
    "v_add_u32 v229, s59, v229\n"
    "s_and_b32 s65, s63, 1\n"
    "s_lshl_b32 s65, s65, 3\n"
    "v_xor_b32 v227, v228, v231\n"
    "v_xor_b32 v227, s65, v227\n"
    "v_xor_b32 v230, 0, v227\n"
    "v_lshl_add_u32 v212, v230, 4, v229\n"
    "v_xor_b32 v230, 2, v227\n"
    "v_lshl_add_u32 v213, v230, 4, v229\n"
    "v_xor_b32 v230, 4, v227\n"
    "v_lshl_add_u32 v214, v230, 4, v229\n"
    "v_xor_b32 v230, 6, v227\n"
    "v_lshl_add_u32 v215, v230, 4, v229\n"
    "v_lshlrev_b32 v227, 1, v231\n"
    "v_xor_b32 v227, v228, v227\n"
    "v_xor_b32 v230, 0, v227\n"
    "v_lshl_add_u32 v216, v230, 4, v229\n"
    "v_xor_b32 v230, 4, v227\n"
    "v_lshl_add_u32 v217, v230, 4, v229\n"
    "v_xor_b32 v230, 8, v227\n"
    "v_lshl_add_u32 v218, v230, 4, v229\n"
    "v_xor_b32 v230, 12, v227\n"
    "v_lshl_add_u32 v219, v230, 4, v229\n"
    "s_lshr_b32 s84, s63, 1\n"
    "v_bfe_u32 v227, v234, 2, 2\n"
    "v_lshl_add_u32 v228, v226, 3, v227\n"
    "v_lshlrev_b32 v228, 10, v228\n"
    "v_and_b32 v229, 1, v234\n"
    "v_lshl_add_u32 v228, v229, 3, v228\n"
    "s_lshl_b32 s85, s84, 9\n"
    "v_add_u32 v228, s85, v228\n"
    "v_and_b32 v229, 1, v227\n"
    "v_lshrrev_b32 v230, 1, v227\n"
    "v_lshl_or_b32 v229, v230, 2, v229\n"
    "v_and_b32 v230, 1, v226\n"
    "v_lshl_or_b32 v229, v230, 3, v229\n"
    "v_and_b32 v230, 2, v234\n"
    "v_xor_b32 v229, v229, v230\n"
    "v_xor_b32 v230, 0, v229\n"
    "v_lshl_add_u32 v176, v230, 4, v228\n"
    "v_xor_b32 v230, 1, v229\n"
    "v_lshl_add_u32 v177, v230, 4, v228\n"
    "v_xor_b32 v230, 4, v229\n"
    "v_lshl_add_u32 v178, v230, 4, v228\n"
    "v_xor_b32 v230, 5, v229\n"
    "v_lshl_add_u32 v179, v230, 4, v228\n"
    "v_xor_b32 v230, 8, v229\n"
    "v_lshl_add_u32 v180, v230, 4, v228\n"
    "v_xor_b32 v230, 9, v229\n"
    "v_lshl_add_u32 v181, v230, 4, v228\n"
    "v_xor_b32 v230, 12, v229\n"
    "v_lshl_add_u32 v182, v230, 4, v228\n"
    "v_xor_b32 v230, 13, v229\n"
    "v_lshl_add_u32 v183, v230, 4, v228\n"
    "s_mul_i32 s85, s83, 29696\n"
    "s_add_u32 s85, s85, 98304\n"
    "v_mul_u32_u24 v184, 928, v225\n"
    "v_lshl_add_u32 v184, v226, 4, v184\n"
    "v_add_u32 v184, s85, v184\n"
    "s_mul_i32 s85, s63, 14848\n"
    "s_add_u32 s85, s85, 98304\n"
    "v_mul_u32_u24 v185, 928, v226\n"
    "v_lshl_add_u32 v185, v225, 4, v185\n"
    "v_add_u32 v185, s85, v185\n"
    "v_lshrrev_b32 v227, 3, v234\n"
    "v_and_b32 v228, 7, v234\n"
    "v_mul_u32_u24 v199, 928, v227\n"
    "v_lshl_add_u32 v199, v228, 4, v199\n"
    "s_add_u32 s85, s85, 768\n"
    "v_add_u32 v199, s85, v199\n"
    "v_mul_u32_u24 v196, 896, v227\n"
    "v_lshl_add_u32 v196, v228, 4, v196\n"
    "v_add_u32 v196, 768, v196\n"
    "v_add_u32 v197, 157696, v187\n"
    "v_lshlrev_b32 v198, 5, v228\n"
    "v_add_u32 v198, 159232, v198\n"
    "v_lshrrev_b32 v227, 2, v234\n"
    "v_lshlrev_b32 v227, 7, v227\n"
    "v_and_b32 v228, 1, v234\n"
    "v_lshl_add_u32 v227, v228, 3, v227\n"
    "v_bfe_u32 v228, v234, 3, 2\n"
    "v_lshlrev_b32 v228, 1, v228\n"
    "v_bfe_u32 v229, v234, 1, 1\n"
    "v_xor_b32 v228, v228, v229\n"
    "s_lshl_b32 s85, s84, 2\n"
    "v_xor_b32 v228, s85, v228\n"
    "v_lshl_add_u32 v244, v228, 4, v227\n"
    "v_xor_b32 v228, 2, v228\n"
    "v_lshl_add_u32 v245, v228, 4, v227\n"
    "v_lshrrev_b32 v227, 3, v234\n"
    "v_mul_u32_u24 v227, 896, v227\n"
    "v_and_b32 v228, 7, v234\n"
    "v_lshlrev_b32 v229, 1, v226\n"
    "v_xor_b32 v228, v228, v229\n"
    "v_lshl_add_u32 v240, v228, 4, v227\n"
    "v_add_u32 v241, 6144, v240\n"
    "v_add_u32 v242, 12288, v240\n"
    "v_add_u32 v243, 18432, v240\n"
    "v_add_u32 v250, 14336, v249\n"
    "s_lshl_b32 s85, s83, 14\n"
    "v_add_u32 v247, s85, v249\n"
    "v_add_u32 v248, 98304, v247\n"
    "s_mul_i32 s85, s84, 98304\n"
    "v_add_u32 v246, s85, v247\n"
    "s_mul_i32 s80, s63, 28672\n"
    "s_add_u32 s80, s80, 128\n"
    "s_lshl_b32 s85, s66, 7\n"
    "s_lshl_b32 s82, s67, 2\n"
    "s_add_u32 s85, s85, s82\n"
    "s_lshl_b32 s82, s83, 1\n"
    "s_add_u32 s85, s85, s82\n"
    "s_mul_i32 s85, s85, 14\n"
    "s_add_u32 s85, s85, s84\n"
    "s_lshl_b32 s85, s85, 10\n"
    "s_add_u32 s79, s85, 4194304\n"
    "s_mov_b32 s78, 0x7f000000\n"
    "s_lshl_b32 s89, s67, 3\n"
    "s_add_u32 s89, s89, s66\n"
    "s_lshl_b32 s89, s89, 2\n"
    "s_add_u32 s89, s89, s63\n"
    "s_mul_i32 s89, s89, 1664\n"
    "s_add_u32 s65, s89, 1536\n"
    "s_min_u32 s65, s65, 1605504\n"
    "s_min_u32 s89, s89, s65\n"
    "v_mov_b32 v220, 0\n"
    "v_mov_b32 v221, 0\n"
    "v_add_u32 v168, 0x10000, v176\n"
    "v_add_u32 v169, 0x10000, v177\n"
    "v_add_u32 v170, 0x10000, v178\n"
    "v_add_u32 v171, 0x10000, v179\n"
    "v_add_u32 v172, 0x10000, v180\n"
    "v_add_u32 v173, 0x10000, v181\n"
    "v_add_u32 v174, 0x10000, v182\n"
    "v_add_u32 v175, 0x10000, v183\n"
    "s_waitcnt vmcnt(25)\n"
    "v_lshlrev_b32 v16, 16, v0\n"
    "v_and_b32 v17, s81, v0\n"
    "v_lshlrev_b32 v18, 16, v1\n"
    "v_and_b32 v19, s81, v1\n"
    "v_pk_fma_f32 v[16:17], v[16:17], v[64:65], v[72:73]\n"
    "v_pk_fma_f32 v[18:19], v[18:19], v[66:67], v[74:75]\n"
    "v_cvt_pk_bf16_f32 v0, v16, v17\n"
    "v_cvt_pk_bf16_f32 v1, v18, v19\n"
    "v_lshlrev_b32 v20, 16, v2\n"
    "v_and_b32 v21, s81, v2\n"
    "v_lshlrev_b32 v22, 16, v3\n"
    "v_and_b32 v23, s81, v3\n"
    "v_pk_fma_f32 v[20:21], v[20:21], v[68:69], v[76:77]\n"
    "v_pk_fma_f32 v[22:23], v[22:23], v[70:71], v[78:79]\n"
    "v_cvt_pk_bf16_f32 v2, v20, v21\n"
    "v_cvt_pk_bf16_f32 v3, v22, v23\n"
    "ds_write_b128 v185, v[0:3] offset:0\n"
    "buffer_load_dwordx4 v[144:147], v186, s[68:71], s56 offen offset:512\n"
    "buffer_load_dwordx4 v[148:151], v186, s[68:71], s57 offen offset:512\n"
    "buffer_load_dwordx4 v[152:155], v186, s[68:71], s72 offen offset:512\n"
    "s_waitcnt vmcnt(27)\n"
    "v_lshlrev_b32 v16, 16, v4\n"
    "v_and_b32 v17, s81, v4\n"
    "v_lshlrev_b32 v18, 16, v5\n"
    "v_and_b32 v19, s81, v5\n"
    "v_pk_fma_f32 v[16:17], v[16:17], v[64:65], v[72:73]\n"
    "v_pk_fma_f32 v[18:19], v[18:19], v[66:67], v[74:75]\n"
    "v_cvt_pk_bf16_f32 v4, v16, v17\n"
    "v_cvt_pk_bf16_f32 v5, v18, v19\n"
    "v_lshlrev_b32 v20, 16, v6\n"
    "v_and_b32 v21, s81, v6\n"
    "v_lshlrev_b32 v22, 16, v7\n"
    "v_and_b32 v23, s81, v7\n"
    "v_pk_fma_f32 v[20:21], v[20:21], v[68:69], v[76:77]\n"
    "v_pk_fma_f32 v[22:23], v[22:23], v[70:71], v[78:79]\n"
    "v_cvt_pk_bf16_f32 v6, v20, v21\n"
    "v_cvt_pk_bf16_f32 v7, v22, v23\n"
    "ds_write_b128 v185, v[4:7] offset:3712\n"
    "buffer_load_dwordx4 v[156:159], v186, s[68:71], s73 offen offset:512\n"
    "s_add_u32 s75, s75, 0x40000\n"
    "s_add_u32 m0, s76, 0\n"
    "s_nop 0\n"
    "buffer_load_dwordx4 v236, s[48:51], s74 offen offset:0 lds\n"
    "buffer_load_dwordx4 v237, s[48:51], s74 offen offset:1024 lds\n"
    "s_waitcnt vmcnt(29)\n"
    "v_lshlrev_b32 v16, 16, v8\n"
    "v_and_b32 v17, s81, v8\n"
    "v_lshlrev_b32 v18, 16, v9\n"
    "v_and_b32 v19, s81, v9\n"
    "v_pk_fma_f32 v[16:17], v[16:17], v[64:65], v[72:73]\n"
    "v_pk_fma_f32 v[18:19], v[18:19], v[66:67], v[74:75]\n"
    "v_cvt_pk_bf16_f32 v8, v16, v17\n"
    "v_cvt_pk_bf16_f32 v9, v18, v19\n"
    "v_lshlrev_b32 v20, 16, v10\n"
    "v_and_b32 v21, s81, v10\n"
    "v_lshlrev_b32 v22, 16, v11\n"
    "v_and_b32 v23, s81, v11\n"
    "v_pk_fma_f32 v[20:21], v[20:21], v[68:69], v[76:77]\n"
    "v_pk_fma_f32 v[22:23], v[22:23], v[70:71], v[78:79]\n"
    "v_cvt_pk_bf16_f32 v10, v20, v21\n"
    "v_cvt_pk_bf16_f32 v11, v22, v23\n"
    "ds_write_b128 v185, v[8:11] offset:7424\n"
    "buffer_load_dwordx4 v238, s[48:51], s74 offen offset:2048 lds\n"
    "buffer_load_dwordx4 v239, s[48:51], s74 offen offset:3072 lds\n"
    "s_add_u32 s74, s74, 0x40000\n"
    "s_add_u32 m0, m0, 0x1000\n"
    "s_nop 0\n"
    "buffer_load_dwordx4 v236, s[48:51], s75 offen offset:0 lds\n"
    "s_waitcnt vmcnt(31)\n"
    "v_lshlrev_b32 v16, 16, v12\n"
    "v_and_b32 v17, s81, v12\n"
    "v_lshlrev_b32 v18, 16, v13\n"
    "v_and_b32 v19, s81, v13\n"
    "v_pk_fma_f32 v[16:17], v[16:17], v[64:65], v[72:73]\n"
    "v_pk_fma_f32 v[18:19], v[18:19], v[66:67], v[74:75]\n"
    "v_cvt_pk_bf16_f32 v12, v16, v17\n"
    "v_cvt_pk_bf16_f32 v13, v18, v19\n"
    "v_lshlrev_b32 v20, 16, v14\n"
    "v_and_b32 v21, s81, v14\n"
    "v_lshlrev_b32 v22, 16, v15\n"
    "v_and_b32 v23, s81, v15\n"
    "v_pk_fma_f32 v[20:21], v[20:21], v[68:69], v[76:77]\n"
    "v_pk_fma_f32 v[22:23], v[22:23], v[70:71], v[78:79]\n"
    "v_cvt_pk_bf16_f32 v14, v20, v21\n"
    "v_cvt_pk_bf16_f32 v15, v22, v23\n"
    "ds_write_b128 v185, v[12:15] offset:11136\n"
    "buffer_load_dwordx4 v237, s[48:51], s75 offen offset:1024 lds\n"
    "buffer_load_dwordx4 v238, s[48:51], s75 offen offset:2048 lds\n"
    "buffer_load_dwordx4 v239, s[48:51], s75 offen offset:3072 lds\n"
    "s_waitcnt vmcnt(25) lgkmcnt(0)\n"
    "s_barrier\n"
    "ds_read_b128 a[192:195], v184 offset:0\n"
    "ds_read_b128 a[196:199], v184 offset:14848\n"
    "ds_read_b64_tr_b16 a[0:1], v168 offset:0\n"
    "ds_read_b64_tr_b16 a[2:3], v168 offset:4096\n"
    "ds_read_b64_tr_b16 a[4:5], v169 offset:0\n"
    "ds_read_b64_tr_b16 a[6:7], v169 offset:4096\n"
    "ds_read_b64_tr_b16 a[8:9], v170 offset:0\n"
    "ds_read_b64_tr_b16 a[10:11], v170 offset:4096\n"
    "ds_read_b64_tr_b16 a[12:13], v171 offset:0\n"
    "ds_read_b64_tr_b16 a[14:15], v171 offset:4096\n"
    "ds_read_b64_tr_b16 a[16:17], v172 offset:0\n"
    "ds_read_b64_tr_b16 a[18:19], v172 offset:4096\n"
    "ds_read_b64_tr_b16 a[20:21], v173 offset:0\n"
    "ds_read_b64_tr_b16 a[22:23], v173 offset:4096\n"
    "ds_read_b64_tr_b16 a[24:25], v174 offset:0\n"
    "ds_read_b64_tr_b16 a[26:27], v174 offset:4096\n"
    "ds_read_b64_tr_b16 a[28:29], v175 offset:0\n"
    "ds_read_b64_tr_b16 a[30:31], v175 offset:4096\n"
    "ds_read_b64_tr_b16 a[32:33], v168 offset:256\n"
    "ds_read_b64_tr_b16 a[34:35], v168 offset:4352\n"
    "ds_read_b64_tr_b16 a[36:37], v169 offset:256\n"
    "ds_read_b64_tr_b16 a[38:39], v169 offset:4352\n"
    "ds_read_b64_tr_b16 a[40:41], v170 offset:256\n"
    "ds_read_b64_tr_b16 a[42:43], v170 offset:4352\n"
    "ds_read_b64_tr_b16 a[44:45], v171 offset:256\n"
    "ds_read_b64_tr_b16 a[46:47], v171 offset:4352\n"
    "ds_read_b64_tr_b16 a[48:49], v172 offset:256\n"
    "ds_read_b64_tr_b16 a[50:51], v172 offset:4352\n"
    "ds_read_b64_tr_b16 a[52:53], v173 offset:256\n"
    "ds_read_b64_tr_b16 a[54:55], v173 offset:4352\n"
    "ds_read_b64_tr_b16 a[56:57], v174 offset:256\n"
    "ds_read_b64_tr_b16 a[58:59], v174 offset:4352\n"
    "ds_read_b64_tr_b16 a[60:61], v175 offset:256\n"
    "ds_read_b64_tr_b16 a[62:63], v175 offset:4352\n"
    "ds_read_b128 v[188:191], v197 offset:512\n"
    "ds_read_b128 v[192:195], v197 offset:2304\n"
    "s_waitcnt lgkmcnt(15)\n"
    "s_nop 0\n"
    "v_mfma_f32_16x16x32_bf16 v[0:3], a[0:3], a[192:195], 0\n"
    "v_mfma_f32_16x16x32_bf16 v[4:7], a[0:3], a[196:199], 0\n"
    "s_waitcnt vmcnt(12) lgkmcnt(0)\n"
    "s_barrier\n"
    "v_mfma_f32_16x16x32_bf16 v[8:11], a[4:7], a[192:195], 0\n"
    "ds_read_b128 a[200:203], v184 offset:64\n"
    "ds_read_b128 a[204:207], v184 offset:14912\n"
    "v_lshlrev_b32 v160, 16, v128\n"
    "v_and_b32 v161, s81, v128\n"
    "v_mfma_f32_16x16x32_bf16 v[12:15], a[4:7], a[196:199], 0\n"
    "ds_read_b64_tr_b16 a[64:65], v176 offset:32768\n"
    "ds_read_b64_tr_b16 a[66:67], v176 offset:36864\n"
    "v_lshlrev_b32 v162, 16, v129\n"
    "v_and_b32 v163, s81, v129\n"
    "s_add_u32 s75, s75, 0x40000\n"
    "s_add_u32 m0, s76, 65536\n"
    "v_mfma_f32_16x16x32_bf16 v[16:19], a[8:11], a[192:195], 0\n"
    "buffer_load_dwordx4 v236, s[48:51], s74 offen offset:0 lds\n"
    "ds_read_b64_tr_b16 a[68:69], v177 offset:32768\n"
    "ds_read_b64_tr_b16 a[70:71], v177 offset:36864\n"
    "v_fma_f32 v160, v160, v188, v192\n"
    "v_fma_f32 v161, v161, v189, v193\n"
    "v_mfma_f32_16x16x32_bf16 v[20:23], a[8:11], a[196:199], 0\n"
    "ds_read_b64_tr_b16 a[72:73], v178 offset:32768\n"
    "ds_read_b64_tr_b16 a[74:75], v178 offset:36864\n"
    "v_fma_f32 v162, v162, v190, v194\n"
    "v_fma_f32 v163, v163, v191, v195\n"
    "v_mfma_f32_16x16x32_bf16 v[24:27], a[12:15], a[192:195], 0\n"
    "ds_read_b64_tr_b16 a[76:77], v179 offset:32768\n"
    "ds_read_b64_tr_b16 a[78:79], v179 offset:36864\n"
    "v_cvt_pk_bf16_f32 v128, v160, v161\n"
    "v_cvt_pk_bf16_f32 v129, v162, v163\n"
    "v_mfma_f32_16x16x32_bf16 v[28:31], a[12:15], a[196:199], 0\n"
    "buffer_load_dwordx4 v237, s[48:51], s74 offen offset:1024 lds\n"
    "ds_read_b64_tr_b16 a[80:81], v180 offset:32768\n"
    "ds_read_b64_tr_b16 a[82:83], v180 offset:36864\n"
    "v_lshlrev_b32 v164, 16, v132\n"
    "v_and_b32 v165, s81, v132\n"
    "v_mfma_f32_16x16x32_bf16 v[32:35], a[16:19], a[192:195], 0\n"
    "ds_read_b64_tr_b16 a[84:85], v181 offset:32768\n"
    "ds_read_b64_tr_b16 a[86:87], v181 offset:36864\n"
    "v_lshlrev_b32 v166, 16, v133\n"
    "v_and_b32 v167, s81, v133\n"
    "v_mfma_f32_16x16x32_bf16 v[36:39], a[16:19], a[196:199], 0\n"
    "ds_read_b64_tr_b16 a[88:89], v182 offset:32768\n"
    "ds_read_b64_tr_b16 a[90:91], v182 offset:36864\n"
    "v_fma_f32 v164, v164, v188, v192\n"
    "v_fma_f32 v165, v165, v189, v193\n"
    "v_mfma_f32_16x16x32_bf16 v[40:43], a[20:23], a[192:195], 0\n"
    "buffer_load_dwordx4 v238, s[48:51], s74 offen offset:2048 lds\n"
    "ds_read_b64_tr_b16 a[92:93], v183 offset:32768\n"
    "v_fma_f32 v166, v166, v190, v194\n"
    "v_fma_f32 v167, v167, v191, v195\n"
    "v_mfma_f32_16x16x32_bf16 v[44:47], a[20:23], a[196:199], 0\n"
    "ds_read_b64_tr_b16 a[94:95], v183 offset:36864\n"
    "ds_read_b64_tr_b16 a[96:97], v176 offset:33024\n"
    "v_cvt_pk_bf16_f32 v132, v164, v165\n"
    "v_cvt_pk_bf16_f32 v133, v166, v167\n"
    "v_mfma_f32_16x16x32_bf16 v[48:51], a[24:27], a[192:195], 0\n"
    "ds_read_b64_tr_b16 a[98:99], v176 offset:37120\n"
    "ds_read_b64_tr_b16 a[100:101], v177 offset:33024\n"
    "v_lshlrev_b32 v160, 16, v136\n"
    "v_and_b32 v161, s81, v136\n"
    "v_mfma_f32_16x16x32_bf16 v[52:55], a[24:27], a[196:199], 0\n"
    "buffer_load_dwordx4 v239, s[48:51], s74 offen offset:3072 lds\n"
    "ds_read_b64_tr_b16 a[102:103], v177 offset:37120\n"
    "v_lshlrev_b32 v162, 16, v137\n"
    "v_and_b32 v163, s81, v137\n"
    "v_mfma_f32_16x16x32_bf16 v[56:59], a[28:31], a[192:195], 0\n"
    "ds_read_b64_tr_b16 a[104:105], v178 offset:33024\n"
    "ds_read_b64_tr_b16 a[106:107], v178 offset:37120\n"
    "v_fma_f32 v160, v160, v188, v192\n"
    "v_fma_f32 v161, v161, v189, v193\n"
    "v_mfma_f32_16x16x32_bf16 v[60:63], a[28:31], a[196:199], 0\n"
    "ds_read_b64_tr_b16 a[108:109], v179 offset:33024\n"
    "ds_read_b64_tr_b16 a[110:111], v179 offset:37120\n"
    "v_fma_f32 v162, v162, v190, v194\n"
    "v_fma_f32 v163, v163, v191, v195\n"
    "v_mfma_f32_16x16x32_bf16 v[64:67], a[32:35], a[192:195], 0\n"
    "ds_read_b64_tr_b16 a[112:113], v180 offset:33024\n"
    "v_cvt_pk_bf16_f32 v136, v160, v161\n"
    "v_cvt_pk_bf16_f32 v137, v162, v163\n"
    "s_add_u32 s74, s74, 0x40000\n"
    "s_add_u32 m0, m0, 0x1000\n"
    "v_mfma_f32_16x16x32_bf16 v[68:71], a[32:35], a[196:199], 0\n"
    "buffer_load_dwordx4 v236, s[48:51], s75 offen offset:0 lds\n"
    "ds_read_b64_tr_b16 a[114:115], v180 offset:37120\n"
    "v_lshlrev_b32 v164, 16, v140\n"
    "v_and_b32 v165, s81, v140\n"
    "v_mfma_f32_16x16x32_bf16 v[72:75], a[36:39], a[192:195], 0\n"
    "ds_read_b64_tr_b16 a[116:117], v181 offset:33024\n"
    "ds_read_b64_tr_b16 a[118:119], v181 offset:37120\n"
    "v_lshlrev_b32 v166, 16, v141\n"
    "v_and_b32 v167, s81, v141\n"
    "v_mfma_f32_16x16x32_bf16 v[76:79], a[36:39], a[196:199], 0\n"
    "ds_read_b64_tr_b16 a[120:121], v182 offset:33024\n"
    "ds_read_b64_tr_b16 a[122:123], v182 offset:37120\n"
    "v_fma_f32 v164, v164, v188, v192\n"
    "v_fma_f32 v165, v165, v189, v193\n"
    "v_mfma_f32_16x16x32_bf16 v[80:83], a[40:43], a[192:195], 0\n"
    "buffer_load_dwordx4 v237, s[48:51], s75 offen offset:1024 lds\n"
    "ds_read_b64_tr_b16 a[124:125], v183 offset:33024\n"
    "v_fma_f32 v166, v166, v190, v194\n"
    "v_fma_f32 v167, v167, v191, v195\n"
    "v_mfma_f32_16x16x32_bf16 v[84:87], a[40:43], a[196:199], 0\n"
    "ds_read_b64_tr_b16 a[126:127], v183 offset:37120\n"
    "v_cvt_pk_bf16_f32 v140, v164, v165\n"
    "v_cvt_pk_bf16_f32 v141, v166, v167\n"
    "v_mfma_f32_16x16x32_bf16 v[88:91], a[44:47], a[192:195], 0\n"
    "v_mfma_f32_16x16x32_bf16 v[92:95], a[44:47], a[196:199], 0\n"
    "buffer_load_dwordx4 v238, s[48:51], s75 offen offset:2048 lds\n"
    "v_mfma_f32_16x16x32_bf16 v[96:99], a[48:51], a[192:195], 0\n"
    "v_mfma_f32_16x16x32_bf16 v[100:103], a[48:51], a[196:199], 0\n"
    "v_mfma_f32_16x16x32_bf16 v[104:107], a[52:55], a[192:195], 0\n"
    "buffer_load_dwordx4 v239, s[48:51], s75 offen offset:3072 lds\n"
    "v_mfma_f32_16x16x32_bf16 v[108:111], a[52:55], a[196:199], 0\n"
    "v_mfma_f32_16x16x32_bf16 v[112:115], a[56:59], a[192:195], 0\n"
    "ds_read_b128 v[188:191], v197 offset:528\n"
    "ds_read_b128 v[192:195], v197 offset:2320\n"
    "v_mfma_f32_16x16x32_bf16 v[116:119], a[56:59], a[196:199], 0\n"
    "v_mfma_f32_16x16x32_bf16 v[120:123], a[60:63], a[192:195], 0\n"
    "v_mfma_f32_16x16x32_bf16 v[124:127], a[60:63], a[196:199], 0\n"
    "s_waitcnt lgkmcnt(15)\n"
    "s_nop 0\n"
    "v_mfma_f32_16x16x32_bf16 v[0:3], a[64:67], a[200:203], v[0:3]\n"
    "v_mfma_f32_16x16x32_bf16 v[4:7], a[64:67], a[204:207], v[4:7]\n"
    "s_waitcnt vmcnt(8) lgkmcnt(0)\n"
    "s_barrier\n"
    "v_mfma_f32_16x16x32_bf16 v[8:11], a[68:71], a[200:203], v[8:11]\n"
    "ds_read_b128 a[192:195], v184 offset:128\n"
    "ds_read_b128 a[196:199], v184 offset:14976\n"
    "v_lshlrev_b32 v160, 16, v130\n"
    "v_and_b32 v161, s81, v130\n"
    "v_mfma_f32_16x16x32_bf16 v[12:15], a[68:71], a[204:207], v[12:15]\n"
    "ds_read_b64_tr_b16 a[0:1], v176 offset:0\n"
    "ds_read_b64_tr_b16 a[2:3], v176 offset:4096\n"
    "v_lshlrev_b32 v162, 16, v131\n"
    "v_and_b32 v163, s81, v131\n"
    "s_add_u32 s75, s75, 0x40000\n"
    "s_add_u32 m0, s76, 32768\n"
    "v_mfma_f32_16x16x32_bf16 v[16:19], a[72:75], a[200:203], v[16:19]\n"
    "buffer_load_dwordx4 v236, s[48:51], s74 offen offset:0 lds\n"
    "ds_read_b64_tr_b16 a[4:5], v177 offset:0\n"
    "ds_read_b64_tr_b16 a[6:7], v177 offset:4096\n"
    "v_fma_f32 v160, v160, v188, v192\n"
    "v_fma_f32 v161, v161, v189, v193\n"
    "v_mfma_f32_16x16x32_bf16 v[20:23], a[72:75], a[204:207], v[20:23]\n"
    "ds_read_b64_tr_b16 a[8:9], v178 offset:0\n"
    "ds_read_b64_tr_b16 a[10:11], v178 offset:4096\n"
    "v_fma_f32 v162, v162, v190, v194\n"
    "v_fma_f32 v163, v163, v191, v195\n"
    "v_mfma_f32_16x16x32_bf16 v[24:27], a[76:79], a[200:203], v[24:27]\n"
    "ds_read_b64_tr_b16 a[12:13], v179 offset:0\n"
    "ds_read_b64_tr_b16 a[14:15], v179 offset:4096\n"
    "v_cvt_pk_bf16_f32 v130, v160, v161\n"
    "v_cvt_pk_bf16_f32 v131, v162, v163\n"
    "v_mfma_f32_16x16x32_bf16 v[28:31], a[76:79], a[204:207], v[28:31]\n"
    "buffer_load_dwordx4 v237, s[48:51], s74 offen offset:1024 lds\n"
    "ds_read_b64_tr_b16 a[16:17], v180 offset:0\n"
    "ds_read_b64_tr_b16 a[18:19], v180 offset:4096\n"
    "ds_write_b128 v185, v[128:131] offset:256\n"
    "v_lshlrev_b32_e64 v164, 16, v134\n"
    "v_mfma_f32_16x16x32_bf16 v[32:35], a[80:83], a[200:203], v[32:35]\n"
    "ds_read_b64_tr_b16 a[20:21], v181 offset:0\n"
    "ds_read_b64_tr_b16 a[22:23], v181 offset:4096\n"
    "v_and_b32 v165, s81, v134\n"
    "v_lshlrev_b32 v166, 16, v135\n"
    "v_mfma_f32_16x16x32_bf16 v[36:39], a[80:83], a[204:207], v[36:39]\n"
    "ds_read_b64_tr_b16 a[24:25], v182 offset:0\n"
    "ds_read_b64_tr_b16 a[26:27], v182 offset:4096\n"
    "v_and_b32_e64 v167, s81, v135\n"
    "v_fma_f32 v164, v164, v188, v192\n"
    "v_mfma_f32_16x16x32_bf16 v[40:43], a[84:87], a[200:203], v[40:43]\n"
    "buffer_load_dwordx4 v238, s[48:51], s74 offen offset:2048 lds\n"
    "ds_read_b64_tr_b16 a[28:29], v183 offset:0\n"
    "v_fma_f32 v165, v165, v189, v193\n"
    "v_fma_f32 v166, v166, v190, v194\n"
    "v_mfma_f32_16x16x32_bf16 v[44:47], a[84:87], a[204:207], v[44:47]\n"
    "ds_read_b64_tr_b16 a[30:31], v183 offset:4096\n"
    "ds_read_b64_tr_b16 a[32:33], v176 offset:256\n"
    "v_fma_f32 v167, v167, v191, v195\n"
    "v_cvt_pk_bf16_f32 v134, v164, v165\n"
    "v_mfma_f32_16x16x32_bf16 v[48:51], a[88:91], a[200:203], v[48:51]\n"
    "ds_read_b64_tr_b16 a[34:35], v176 offset:4352\n"
    "ds_read_b64_tr_b16 a[36:37], v177 offset:256\n"
    "v_cvt_pk_bf16_f32 v135, v166, v167\n"
    "ds_write_b128 v185, v[132:135] offset:3968\n"
    "v_mfma_f32_16x16x32_bf16 v[52:55], a[88:91], a[204:207], v[52:55]\n"
    "buffer_load_dwordx4 v239, s[48:51], s74 offen offset:3072 lds\n"
    "ds_read_b64_tr_b16 a[38:39], v177 offset:4352\n"
    "v_lshlrev_b32 v160, 16, v138\n"
    "v_and_b32 v161, s81, v138\n"
    "v_mfma_f32_16x16x32_bf16 v[56:59], a[92:95], a[200:203], v[56:59]\n"
    "ds_read_b64_tr_b16 a[40:41], v178 offset:256\n"
    "ds_read_b64_tr_b16 a[42:43], v178 offset:4352\n"
    "v_lshlrev_b32 v162, 16, v139\n"
    "v_and_b32 v163, s81, v139\n"
    "v_mfma_f32_16x16x32_bf16 v[60:63], a[92:95], a[204:207], v[60:63]\n"
    "ds_read_b64_tr_b16 a[44:45], v179 offset:256\n"
    "ds_read_b64_tr_b16 a[46:47], v179 offset:4352\n"
    "v_fma_f32 v160, v160, v188, v192\n"
    "v_fma_f32 v161, v161, v189, v193\n"
    "v_mfma_f32_16x16x32_bf16 v[64:67], a[96:99], a[200:203], v[64:67]\n"
    "ds_read_b64_tr_b16 a[48:49], v180 offset:256\n"
    "v_fma_f32 v162, v162, v190, v194\n"
    "v_fma_f32 v163, v163, v191, v195\n"
    "s_add_u32 s74, s74, 0x40000\n"
    "s_add_u32 m0, m0, 0x1000\n"
    "v_mfma_f32_16x16x32_bf16 v[68:71], a[96:99], a[204:207], v[68:71]\n"
    "buffer_load_dwordx4 v236, s[48:51], s75 offen offset:0 lds\n"
    "ds_read_b64_tr_b16 a[50:51], v180 offset:4352\n"
    "v_cvt_pk_bf16_f32 v138, v160, v161\n"
    "v_cvt_pk_bf16_f32 v139, v162, v163\n"
    "v_mfma_f32_16x16x32_bf16 v[72:75], a[100:103], a[200:203], v[72:75]\n"
    "ds_read_b64_tr_b16 a[52:53], v181 offset:256\n"
    "ds_read_b64_tr_b16 a[54:55], v181 offset:4352\n"
    "ds_write_b128 v185, v[136:139] offset:7680\n"
    "v_lshlrev_b32_e64 v164, 16, v142\n"
    "v_mfma_f32_16x16x32_bf16 v[76:79], a[100:103], a[204:207], v[76:79]\n"
    "ds_read_b64_tr_b16 a[56:57], v182 offset:256\n"
    "ds_read_b64_tr_b16 a[58:59], v182 offset:4352\n"
    "v_and_b32 v165, s81, v142\n"
    "v_lshlrev_b32 v166, 16, v143\n"
    "v_mfma_f32_16x16x32_bf16 v[80:83], a[104:107], a[200:203], v[80:83]\n"
    "buffer_load_dwordx4 v237, s[48:51], s75 offen offset:1024 lds\n"
    "ds_read_b64_tr_b16 a[60:61], v183 offset:256\n"
    "v_and_b32_e64 v167, s81, v143\n"
    "v_fma_f32 v164, v164, v188, v192\n"
    "v_mfma_f32_16x16x32_bf16 v[84:87], a[104:107], a[204:207], v[84:87]\n"
    "ds_read_b64_tr_b16 a[62:63], v183 offset:4352\n"
    "v_fma_f32 v165, v165, v189, v193\n"
    "v_fma_f32 v166, v166, v190, v194\n"
    "v_mfma_f32_16x16x32_bf16 v[88:91], a[108:111], a[200:203], v[88:91]\n"
    "v_fma_f32 v167, v167, v191, v195\n"
    "v_cvt_pk_bf16_f32 v142, v164, v165\n"
    "v_mfma_f32_16x16x32_bf16 v[92:95], a[108:111], a[204:207], v[92:95]\n"
    "buffer_load_dwordx4 v238, s[48:51], s75 offen offset:2048 lds\n"
    "v_cvt_pk_bf16_f32 v143, v166, v167\n"
    "ds_write_b128 v185, v[140:143] offset:11392\n"
    "v_mfma_f32_16x16x32_bf16 v[96:99], a[112:115], a[200:203], v[96:99]\n"
    "v_mfma_f32_16x16x32_bf16 v[100:103], a[112:115], a[204:207], v[100:103]\n"
    "v_mfma_f32_16x16x32_bf16 v[104:107], a[116:119], a[200:203], v[104:107]\n"
    "buffer_load_dwordx4 v239, s[48:51], s75 offen offset:3072 lds\n"
    "v_mfma_f32_16x16x32_bf16 v[108:111], a[116:119], a[204:207], v[108:111]\n"
    "v_mfma_f32_16x16x32_bf16 v[112:115], a[120:123], a[200:203], v[112:115]\n"
    "ds_read_b128 v[188:191], v197 offset:1024\n"
    "ds_read_b128 v[192:195], v197 offset:2816\n"
    "v_mfma_f32_16x16x32_bf16 v[116:119], a[120:123], a[204:207], v[116:119]\n"
    "v_mfma_f32_16x16x32_bf16 v[120:123], a[124:127], a[200:203], v[120:123]\n"
    "v_mfma_f32_16x16x32_bf16 v[124:127], a[124:127], a[204:207], v[124:127]\n"
    "s_waitcnt lgkmcnt(15)\n"
    "s_nop 0\n"
    "v_mfma_f32_16x16x32_bf16 v[0:3], a[0:3], a[192:195], v[0:3]\n"
    "v_mfma_f32_16x16x32_bf16 v[4:7], a[0:3], a[196:199], v[4:7]\n"
    "s_waitcnt vmcnt(8) lgkmcnt(0)\n"
    "s_barrier\n"
    "v_mfma_f32_16x16x32_bf16 v[8:11], a[4:7], a[192:195], v[8:11]\n"
    "ds_read_b128 a[200:203], v184 offset:192\n"
    "ds_read_b128 a[204:207], v184 offset:15040\n"
    "v_lshlrev_b32_e64 v160, 16, v144\n"
    "v_mfma_f32_16x16x32_bf16 v[12:15], a[4:7], a[196:199], v[12:15]\n"
    "ds_read_b64_tr_b16 a[64:65], v168 offset:0\n"
    "ds_read_b64_tr_b16 a[66:67], v168 offset:4096\n"
    "v_and_b32 v161, s81, v144\n"
    "s_add_u32 s75, s75, 0x40000\n"
    "s_add_u32 m0, s76, 0\n"
    "v_mfma_f32_16x16x32_bf16 v[16:19], a[8:11], a[192:195], v[16:19]\n"
    "buffer_load_dwordx4 v236, s[48:51], s74 offen offset:0 lds\n"
    "ds_read_b64_tr_b16 a[68:69], v169 offset:0\n"
    "ds_read_b64_tr_b16 a[70:71], v169 offset:4096\n"
    "v_lshlrev_b32_e64 v162, 16, v145\n"
    "v_mfma_f32_16x16x32_bf16 v[20:23], a[8:11], a[196:199], v[20:23]\n"
    "ds_read_b64_tr_b16 a[72:73], v170 offset:0\n"
    "ds_read_b64_tr_b16 a[74:75], v170 offset:4096\n"
    "v_and_b32_e64 v163, s81, v145\n"
    "v_mfma_f32_16x16x32_bf16 v[24:27], a[12:15], a[192:195], v[24:27]\n"
    "ds_read_b64_tr_b16 a[76:77], v171 offset:0\n"
    "ds_read_b64_tr_b16 a[78:79], v171 offset:4096\n"
    "v_fma_f32 v160, v160, v188, v192\n"
    "v_mfma_f32_16x16x32_bf16 v[28:31], a[12:15], a[196:199], v[28:31]\n"
    "buffer_load_dwordx4 v237, s[48:51], s74 offen offset:1024 lds\n"
    "ds_read_b64_tr_b16 a[80:81], v172 offset:0\n"
    "ds_read_b64_tr_b16 a[82:83], v172 offset:4096\n"
    "v_fma_f32 v161, v161, v189, v193\n"
    "v_mfma_f32_16x16x32_bf16 v[32:35], a[16:19], a[192:195], v[32:35]\n"
    "ds_read_b64_tr_b16 a[84:85], v173 offset:0\n"
    "ds_read_b64_tr_b16 a[86:87], v173 offset:4096\n"
    "v_fma_f32 v162, v162, v190, v194\n"
    "v_mfma_f32_16x16x32_bf16 v[36:39], a[16:19], a[196:199], v[36:39]\n"
    "ds_read_b64_tr_b16 a[88:89], v174 offset:0\n"
    "ds_read_b64_tr_b16 a[90:91], v174 offset:4096\n"
    "v_fma_f32 v163, v163, v191, v195\n"
    "v_mfma_f32_16x16x32_bf16 v[40:43], a[20:23], a[192:195], v[40:43]\n"
    "buffer_load_dwordx4 v238, s[48:51], s74 offen offset:2048 lds\n"
    "ds_read_b64_tr_b16 a[92:93], v175 offset:0\n"
    "v_cvt_pk_bf16_f32 v144, v160, v161\n"
    "v_mfma_f32_16x16x32_bf16 v[44:47], a[20:23], a[196:199], v[44:47]\n"
    "ds_read_b64_tr_b16 a[94:95], v175 offset:4096\n"
    "ds_read_b64_tr_b16 a[96:97], v168 offset:256\n"
    "v_cvt_pk_bf16_f32 v145, v162, v163\n"
    "v_mfma_f32_16x16x32_bf16 v[48:51], a[24:27], a[192:195], v[48:51]\n"
    "ds_read_b64_tr_b16 a[98:99], v168 offset:4352\n"
    "ds_read_b64_tr_b16 a[100:101], v169 offset:256\n"
    "v_lshlrev_b32_e64 v164, 16, v148\n"
    "v_mfma_f32_16x16x32_bf16 v[52:55], a[24:27], a[196:199], v[52:55]\n"
    "buffer_load_dwordx4 v239, s[48:51], s74 offen offset:3072 lds\n"
    "ds_read_b64_tr_b16 a[102:103], v169 offset:4352\n"
    "v_and_b32_e64 v165, s81, v148\n"
    "v_mfma_f32_16x16x32_bf16 v[56:59], a[28:31], a[192:195], v[56:59]\n"
    "ds_read_b64_tr_b16 a[104:105], v170 offset:256\n"
    "ds_read_b64_tr_b16 a[106:107], v170 offset:4352\n"
    "v_lshlrev_b32_e64 v166, 16, v149\n"
    "v_mfma_f32_16x16x32_bf16 v[60:63], a[28:31], a[196:199], v[60:63]\n"
    "ds_read_b64_tr_b16 a[108:109], v171 offset:256\n"
    "ds_read_b64_tr_b16 a[110:111], v171 offset:4352\n"
    "v_and_b32_e64 v167, s81, v149\n"
    "v_mfma_f32_16x16x32_bf16 v[64:67], a[32:35], a[192:195], v[64:67]\n"
    "ds_read_b64_tr_b16 a[112:113], v172 offset:256\n"
    "v_fma_f32 v164, v164, v188, v192\n"
    "s_add_u32 s74, s74, 0x40000\n"
    "s_add_u32 m0, m0, 0x1000\n"
    "v_mfma_f32_16x16x32_bf16 v[68:71], a[32:35], a[196:199], v[68:71]\n"
    "buffer_load_dwordx4 v236, s[48:51], s75 offen offset:0 lds\n"
    "ds_read_b64_tr_b16 a[114:115], v172 offset:4352\n"
    "v_fma_f32 v165, v165, v189, v193\n"
    "v_mfma_f32_16x16x32_bf16 v[72:75], a[36:39], a[192:195], v[72:75]\n"
    "ds_read_b64_tr_b16 a[116:117], v173 offset:256\n"
    "ds_read_b64_tr_b16 a[118:119], v173 offset:4352\n"
    "v_fma_f32 v166, v166, v190, v194\n"
    "v_mfma_f32_16x16x32_bf16 v[76:79], a[36:39], a[196:199], v[76:79]\n"
    "ds_read_b64_tr_b16 a[120:121], v174 offset:256\n"
    "ds_read_b64_tr_b16 a[122:123], v174 offset:4352\n"
    "v_fma_f32 v167, v167, v191, v195\n"
    "v_mfma_f32_16x16x32_bf16 v[80:83], a[40:43], a[192:195], v[80:83]\n"
    "buffer_load_dwordx4 v237, s[48:51], s75 offen offset:1024 lds\n"
    "ds_read_b64_tr_b16 a[124:125], v175 offset:256\n"
    "v_cvt_pk_bf16_f32 v148, v164, v165\n"
    "v_mfma_f32_16x16x32_bf16 v[84:87], a[40:43], a[196:199], v[84:87]\n"
    "ds_read_b64_tr_b16 a[126:127], v175 offset:4352\n"
    "v_cvt_pk_bf16_f32 v149, v166, v167\n"
    "v_mfma_f32_16x16x32_bf16 v[88:91], a[44:47], a[192:195], v[88:91]\n"
    "v_mfma_f32_16x16x32_bf16 v[92:95], a[44:47], a[196:199], v[92:95]\n"
    "buffer_load_dwordx4 v238, s[48:51], s75 offen offset:2048 lds\n"
    "v_mfma_f32_16x16x32_bf16 v[96:99], a[48:51], a[192:195], v[96:99]\n"
    "v_mfma_f32_16x16x32_bf16 v[100:103], a[48:51], a[196:199], v[100:103]\n"
    "v_mfma_f32_16x16x32_bf16 v[104:107], a[52:55], a[192:195], v[104:107]\n"
    "buffer_load_dwordx4 v239, s[48:51], s75 offen offset:3072 lds\n"
    "v_mfma_f32_16x16x32_bf16 v[108:111], a[52:55], a[196:199], v[108:111]\n"
    "v_mfma_f32_16x16x32_bf16 v[112:115], a[56:59], a[192:195], v[112:115]\n"
    "buffer_load_dwordx4 v[128:131], v196, s[68:71], s56 offen\n"
    "v_mfma_f32_16x16x32_bf16 v[116:119], a[56:59], a[196:199], v[116:119]\n"
    "v_mfma_f32_16x16x32_bf16 v[120:123], a[60:63], a[192:195], v[120:123]\n"
    "buffer_load_dwordx4 v[132:135], v196, s[68:71], s72 offen\n"
    "v_mfma_f32_16x16x32_bf16 v[124:127], a[60:63], a[196:199], v[124:127]\n"
    "s_waitcnt lgkmcnt(15)\n"
    "s_nop 0\n"
    "v_mfma_f32_16x16x32_bf16 v[0:3], a[64:67], a[200:203], v[0:3]\n"
    "v_mfma_f32_16x16x32_bf16 v[4:7], a[64:67], a[204:207], v[4:7]\n"
    "s_waitcnt vmcnt(10) lgkmcnt(0)\n"
    "s_barrier\n"
    "v_mfma_f32_16x16x32_bf16 v[8:11], a[68:71], a[200:203], v[8:11]\n"
    "ds_read_b128 a[192:195], v184 offset:256\n"
    "ds_read_b128 a[196:199], v184 offset:15104\n"
    "v_lshlrev_b32_e64 v160, 16, v152\n"
    "v_mfma_f32_16x16x32_bf16 v[12:15], a[68:71], a[204:207], v[12:15]\n"
    "ds_read_b64_tr_b16 a[0:1], v176 offset:32768\n"
    "ds_read_b64_tr_b16 a[2:3], v176 offset:36864\n"
    "v_and_b32_e64 v161, s81, v152\n"
    "s_add_u32 s75, s75, 0x40000\n"
    "s_add_u32 m0, s76, 65536\n"
    "v_mfma_f32_16x16x32_bf16 v[16:19], a[72:75], a[200:203], v[16:19]\n"
    "buffer_load_dwordx4 v236, s[48:51], s74 offen offset:0 lds\n"
    "ds_read_b64_tr_b16 a[4:5], v177 offset:32768\n"
    "ds_read_b64_tr_b16 a[6:7], v177 offset:36864\n"
    "v_lshlrev_b32_e64 v162, 16, v153\n"
    "v_mfma_f32_16x16x32_bf16 v[20:23], a[72:75], a[204:207], v[20:23]\n"
    "ds_read_b64_tr_b16 a[8:9], v178 offset:32768\n"
    "ds_read_b64_tr_b16 a[10:11], v178 offset:36864\n"
    "v_and_b32_e64 v163, s81, v153\n"
    "v_mfma_f32_16x16x32_bf16 v[24:27], a[76:79], a[200:203], v[24:27]\n"
    "ds_read_b64_tr_b16 a[12:13], v179 offset:32768\n"
    "ds_read_b64_tr_b16 a[14:15], v179 offset:36864\n"
    "v_fma_f32 v160, v160, v188, v192\n"
    "v_mfma_f32_16x16x32_bf16 v[28:31], a[76:79], a[204:207], v[28:31]\n"
    "buffer_load_dwordx4 v237, s[48:51], s74 offen offset:1024 lds\n"
    "ds_read_b64_tr_b16 a[16:17], v180 offset:32768\n"
    "ds_read_b64_tr_b16 a[18:19], v180 offset:36864\n"
    "v_fma_f32 v161, v161, v189, v193\n"
    "v_mfma_f32_16x16x32_bf16 v[32:35], a[80:83], a[200:203], v[32:35]\n"
    "ds_read_b64_tr_b16 a[20:21], v181 offset:32768\n"
    "ds_read_b64_tr_b16 a[22:23], v181 offset:36864\n"
    "v_fma_f32 v162, v162, v190, v194\n"
    "v_mfma_f32_16x16x32_bf16 v[36:39], a[80:83], a[204:207], v[36:39]\n"
    "ds_read_b64_tr_b16 a[24:25], v182 offset:32768\n"
    "ds_read_b64_tr_b16 a[26:27], v182 offset:36864\n"
    "v_fma_f32 v163, v163, v191, v195\n"
    "v_mfma_f32_16x16x32_bf16 v[40:43], a[84:87], a[200:203], v[40:43]\n"
    "buffer_load_dwordx4 v238, s[48:51], s74 offen offset:2048 lds\n"
    "ds_read_b64_tr_b16 a[28:29], v183 offset:32768\n"
    "v_cvt_pk_bf16_f32 v152, v160, v161\n"
    "v_mfma_f32_16x16x32_bf16 v[44:47], a[84:87], a[204:207], v[44:47]\n"
    "ds_read_b64_tr_b16 a[30:31], v183 offset:36864\n"
    "ds_read_b64_tr_b16 a[32:33], v176 offset:33024\n"
    "v_cvt_pk_bf16_f32 v153, v162, v163\n"
    "v_mfma_f32_16x16x32_bf16 v[48:51], a[88:91], a[200:203], v[48:51]\n"
    "ds_read_b64_tr_b16 a[34:35], v176 offset:37120\n"
    "ds_read_b64_tr_b16 a[36:37], v177 offset:33024\n"
    "v_lshlrev_b32_e64 v164, 16, v156\n"
    "v_mfma_f32_16x16x32_bf16 v[52:55], a[88:91], a[204:207], v[52:55]\n"
    "buffer_load_dwordx4 v239, s[48:51], s74 offen offset:3072 lds\n"
    "ds_read_b64_tr_b16 a[38:39], v177 offset:37120\n"
    "v_and_b32_e64 v165, s81, v156\n"
    "v_mfma_f32_16x16x32_bf16 v[56:59], a[92:95], a[200:203], v[56:59]\n"
    "ds_read_b64_tr_b16 a[40:41], v178 offset:33024\n"
    "ds_read_b64_tr_b16 a[42:43], v178 offset:37120\n"
    "v_lshlrev_b32_e64 v166, 16, v157\n"
    "v_mfma_f32_16x16x32_bf16 v[60:63], a[92:95], a[204:207], v[60:63]\n"
    "ds_read_b64_tr_b16 a[44:45], v179 offset:33024\n"
    "ds_read_b64_tr_b16 a[46:47], v179 offset:37120\n"
    "v_and_b32_e64 v167, s81, v157\n"
    "v_mfma_f32_16x16x32_bf16 v[64:67], a[96:99], a[200:203], v[64:67]\n"
    "ds_read_b64_tr_b16 a[48:49], v180 offset:33024\n"
    "v_fma_f32 v164, v164, v188, v192\n"
    "s_add_u32 s74, s74, 0x40000\n"
    "s_add_u32 m0, m0, 0x1000\n"
    "v_mfma_f32_16x16x32_bf16 v[68:71], a[96:99], a[204:207], v[68:71]\n"
    "buffer_load_dwordx4 v236, s[48:51], s75 offen offset:0 lds\n"
    "ds_read_b64_tr_b16 a[50:51], v180 offset:37120\n"
    "v_fma_f32 v165, v165, v189, v193\n"
    "v_mfma_f32_16x16x32_bf16 v[72:75], a[100:103], a[200:203], v[72:75]\n"
    "ds_read_b64_tr_b16 a[52:53], v181 offset:33024\n"
    "ds_read_b64_tr_b16 a[54:55], v181 offset:37120\n"
    "v_fma_f32 v166, v166, v190, v194\n"
    "v_mfma_f32_16x16x32_bf16 v[76:79], a[100:103], a[204:207], v[76:79]\n"
    "ds_read_b64_tr_b16 a[56:57], v182 offset:33024\n"
    "ds_read_b64_tr_b16 a[58:59], v182 offset:37120\n"
    "v_fma_f32 v167, v167, v191, v195\n"
    "v_mfma_f32_16x16x32_bf16 v[80:83], a[104:107], a[200:203], v[80:83]\n"
    "buffer_load_dwordx4 v237, s[48:51], s75 offen offset:1024 lds\n"
    "ds_read_b64_tr_b16 a[60:61], v183 offset:33024\n"
    "v_cvt_pk_bf16_f32 v156, v164, v165\n"
    "v_mfma_f32_16x16x32_bf16 v[84:87], a[104:107], a[204:207], v[84:87]\n"
    "ds_read_b64_tr_b16 a[62:63], v183 offset:37120\n"
    "v_cvt_pk_bf16_f32 v157, v166, v167\n"
    "v_mfma_f32_16x16x32_bf16 v[88:91], a[108:111], a[200:203], v[88:91]\n"
    "v_mfma_f32_16x16x32_bf16 v[92:95], a[108:111], a[204:207], v[92:95]\n"
    "buffer_load_dwordx4 v238, s[48:51], s75 offen offset:2048 lds\n"
    "v_mfma_f32_16x16x32_bf16 v[96:99], a[112:115], a[200:203], v[96:99]\n"
    "v_mfma_f32_16x16x32_bf16 v[100:103], a[112:115], a[204:207], v[100:103]\n"
    "v_mfma_f32_16x16x32_bf16 v[104:107], a[116:119], a[200:203], v[104:107]\n"
    "buffer_load_dwordx4 v239, s[48:51], s75 offen offset:3072 lds\n"
    "v_mfma_f32_16x16x32_bf16 v[108:111], a[116:119], a[204:207], v[108:111]\n"
    "v_mfma_f32_16x16x32_bf16 v[112:115], a[120:123], a[200:203], v[112:115]\n"
    "ds_read_b128 v[188:191], v197 offset:1040\n"
    "v_mfma_f32_16x16x32_bf16 v[116:119], a[120:123], a[204:207], v[116:119]\n"
    "ds_read_b128 v[192:195], v197 offset:2832\n"
    "v_mfma_f32_16x16x32_bf16 v[120:123], a[124:127], a[200:203], v[120:123]\n"
    "v_mfma_f32_16x16x32_bf16 v[124:127], a[124:127], a[204:207], v[124:127]\n"
    "s_waitcnt lgkmcnt(15)\n"
    "s_nop 0\n"
    "v_mfma_f32_16x16x32_bf16 v[0:3], a[0:3], a[192:195], v[0:3]\n"
    "v_mfma_f32_16x16x32_bf16 v[4:7], a[0:3], a[196:199], v[4:7]\n"
    "s_waitcnt vmcnt(8) lgkmcnt(0)\n"
    "s_barrier\n"
    "v_mfma_f32_16x16x32_bf16 v[8:11], a[4:7], a[192:195], v[8:11]\n"
    "ds_read_b128 a[200:203], v184 offset:320\n"
    "ds_read_b128 a[204:207], v184 offset:15168\n"
    "v_lshlrev_b32_e64 v160, 16, v146\n"
    "v_mfma_f32_16x16x32_bf16 v[12:15], a[4:7], a[196:199], v[12:15]\n"
    "ds_read_b64_tr_b16 a[64:65], v176 offset:0\n"
    "ds_read_b64_tr_b16 a[66:67], v176 offset:4096\n"
    "v_and_b32_e64 v161, s81, v146\n"
    "s_add_u32 s75, s75, 0x40000\n"
    "s_add_u32 m0, s76, 32768\n"
    "v_mfma_f32_16x16x32_bf16 v[16:19], a[8:11], a[192:195], v[16:19]\n"
    "buffer_load_dwordx4 v236, s[48:51], s74 offen offset:0 lds\n"
    "ds_read_b64_tr_b16 a[68:69], v177 offset:0\n"
    "ds_read_b64_tr_b16 a[70:71], v177 offset:4096\n"
    "v_lshlrev_b32_e64 v162, 16, v147\n"
    "v_mfma_f32_16x16x32_bf16 v[20:23], a[8:11], a[196:199], v[20:23]\n"
    "ds_read_b64_tr_b16 a[72:73], v178 offset:0\n"
    "ds_read_b64_tr_b16 a[74:75], v178 offset:4096\n"
    "v_and_b32_e64 v163, s81, v147\n"
    "v_mfma_f32_16x16x32_bf16 v[24:27], a[12:15], a[192:195], v[24:27]\n"
    "ds_read_b64_tr_b16 a[76:77], v179 offset:0\n"
    "ds_read_b64_tr_b16 a[78:79], v179 offset:4096\n"
    "v_fma_f32 v160, v160, v188, v192\n"
    "v_mfma_f32_16x16x32_bf16 v[28:31], a[12:15], a[196:199], v[28:31]\n"
    "buffer_load_dwordx4 v237, s[48:51], s74 offen offset:1024 lds\n"
    "ds_read_b64_tr_b16 a[80:81], v180 offset:0\n"
    "ds_read_b64_tr_b16 a[82:83], v180 offset:4096\n"
    "v_fma_f32 v161, v161, v189, v193\n"
    "v_mfma_f32_16x16x32_bf16 v[32:35], a[16:19], a[192:195], v[32:35]\n"
    "ds_read_b64_tr_b16 a[84:85], v181 offset:0\n"
    "ds_read_b64_tr_b16 a[86:87], v181 offset:4096\n"
    "v_fma_f32 v162, v162, v190, v194\n"
    "v_mfma_f32_16x16x32_bf16 v[36:39], a[16:19], a[196:199], v[36:39]\n"
    "ds_read_b64_tr_b16 a[88:89], v182 offset:0\n"
    "ds_read_b64_tr_b16 a[90:91], v182 offset:4096\n"
    "v_fma_f32 v163, v163, v191, v195\n"
    "v_mfma_f32_16x16x32_bf16 v[40:43], a[20:23], a[192:195], v[40:43]\n"
    "buffer_load_dwordx4 v238, s[48:51], s74 offen offset:2048 lds\n"
    "ds_read_b64_tr_b16 a[92:93], v183 offset:0\n"
    "v_cvt_pk_bf16_f32 v146, v160, v161\n"
    "v_mfma_f32_16x16x32_bf16 v[44:47], a[20:23], a[196:199], v[44:47]\n"
    "ds_read_b64_tr_b16 a[94:95], v183 offset:4096\n"
    "ds_read_b64_tr_b16 a[96:97], v176 offset:256\n"
    "v_cvt_pk_bf16_f32 v147, v162, v163\n"
    "v_mfma_f32_16x16x32_bf16 v[48:51], a[24:27], a[192:195], v[48:51]\n"
    "ds_read_b64_tr_b16 a[98:99], v176 offset:4352\n"
    "ds_read_b64_tr_b16 a[100:101], v177 offset:256\n"
    "ds_write_b128 v185, v[144:147] offset:512\n"
    "v_mfma_f32_16x16x32_bf16 v[52:55], a[24:27], a[196:199], v[52:55]\n"
    "buffer_load_dwordx4 v239, s[48:51], s74 offen offset:3072 lds\n"
    "ds_read_b64_tr_b16 a[102:103], v177 offset:4352\n"
    "v_lshlrev_b32_e64 v164, 16, v150\n"
    "v_mfma_f32_16x16x32_bf16 v[56:59], a[28:31], a[192:195], v[56:59]\n"
    "ds_read_b64_tr_b16 a[104:105], v178 offset:256\n"
    "ds_read_b64_tr_b16 a[106:107], v178 offset:4352\n"
    "v_and_b32_e64 v165, s81, v150\n"
    "v_mfma_f32_16x16x32_bf16 v[60:63], a[28:31], a[196:199], v[60:63]\n"
    "ds_read_b64_tr_b16 a[108:109], v179 offset:256\n"
    "ds_read_b64_tr_b16 a[110:111], v179 offset:4352\n"
    "v_lshlrev_b32_e64 v166, 16, v151\n"
    "v_mfma_f32_16x16x32_bf16 v[64:67], a[32:35], a[192:195], v[64:67]\n"
    "ds_read_b64_tr_b16 a[112:113], v180 offset:256\n"
    "v_and_b32_e64 v167, s81, v151\n"
    "s_add_u32 s74, s74, 0x40000\n"
    "s_add_u32 m0, m0, 0x1000\n"
    "v_mfma_f32_16x16x32_bf16 v[68:71], a[32:35], a[196:199], v[68:71]\n"
    "buffer_load_dwordx4 v236, s[48:51], s75 offen offset:0 lds\n"
    "ds_read_b64_tr_b16 a[114:115], v180 offset:4352\n"
    "v_fma_f32 v164, v164, v188, v192\n"
    "v_mfma_f32_16x16x32_bf16 v[72:75], a[36:39], a[192:195], v[72:75]\n"
    "ds_read_b64_tr_b16 a[116:117], v181 offset:256\n"
    "ds_read_b64_tr_b16 a[118:119], v181 offset:4352\n"
    "v_fma_f32 v165, v165, v189, v193\n"
    "v_mfma_f32_16x16x32_bf16 v[76:79], a[36:39], a[196:199], v[76:79]\n"
    "ds_read_b64_tr_b16 a[120:121], v182 offset:256\n"
    "ds_read_b64_tr_b16 a[122:123], v182 offset:4352\n"
    "v_fma_f32 v166, v166, v190, v194\n"
    "v_mfma_f32_16x16x32_bf16 v[80:83], a[40:43], a[192:195], v[80:83]\n"
    "buffer_load_dwordx4 v237, s[48:51], s75 offen offset:1024 lds\n"
    "ds_read_b64_tr_b16 a[124:125], v183 offset:256\n"
    "v_fma_f32 v167, v167, v191, v195\n"
    "v_mfma_f32_16x16x32_bf16 v[84:87], a[40:43], a[196:199], v[84:87]\n"
    "ds_read_b64_tr_b16 a[126:127], v183 offset:4352\n"
    "v_cvt_pk_bf16_f32 v150, v164, v165\n"
    "v_mfma_f32_16x16x32_bf16 v[88:91], a[44:47], a[192:195], v[88:91]\n"
    "v_cvt_pk_bf16_f32 v151, v166, v167\n"
    "v_mfma_f32_16x16x32_bf16 v[92:95], a[44:47], a[196:199], v[92:95]\n"
    "buffer_load_dwordx4 v238, s[48:51], s75 offen offset:2048 lds\n"
    "ds_write_b128 v185, v[148:151] offset:4224\n"
    "v_mfma_f32_16x16x32_bf16 v[96:99], a[48:51], a[192:195], v[96:99]\n"
    "v_mfma_f32_16x16x32_bf16 v[100:103], a[48:51], a[196:199], v[100:103]\n"
    "v_mfma_f32_16x16x32_bf16 v[104:107], a[52:55], a[192:195], v[104:107]\n"
    "buffer_load_dwordx4 v239, s[48:51], s75 offen offset:3072 lds\n"
    "v_mfma_f32_16x16x32_bf16 v[108:111], a[52:55], a[196:199], v[108:111]\n"
    "v_mfma_f32_16x16x32_bf16 v[112:115], a[56:59], a[192:195], v[112:115]\n"
    "v_mfma_f32_16x16x32_bf16 v[116:119], a[56:59], a[196:199], v[116:119]\n"
    "v_mfma_f32_16x16x32_bf16 v[120:123], a[60:63], a[192:195], v[120:123]\n"
    "v_mfma_f32_16x16x32_bf16 v[124:127], a[60:63], a[196:199], v[124:127]\n"
    "s_waitcnt lgkmcnt(15)\n"
    "s_nop 0\n"
    "v_mfma_f32_16x16x32_bf16 v[0:3], a[64:67], a[200:203], v[0:3]\n"
    "v_mfma_f32_16x16x32_bf16 v[4:7], a[64:67], a[204:207], v[4:7]\n"
    "s_waitcnt vmcnt(8) lgkmcnt(0)\n"
    "s_barrier\n"
    "v_mfma_f32_16x16x32_bf16 v[8:11], a[68:71], a[200:203], v[8:11]\n"
    "ds_read_b128 a[192:195], v184 offset:384\n"
    "ds_read_b128 a[196:199], v184 offset:15232\n"
    "v_lshlrev_b32_e64 v160, 16, v154\n"
    "v_mfma_f32_16x16x32_bf16 v[12:15], a[68:71], a[204:207], v[12:15]\n"
    "ds_read_b64_tr_b16 a[0:1], v168 offset:0\n"
    "ds_read_b64_tr_b16 a[2:3], v168 offset:4096\n"
    "v_and_b32 v161, s81, v154\n"
    "s_add_u32 s75, s75, 0x40000\n"
    "s_add_u32 m0, s76, 0\n"
    "v_mfma_f32_16x16x32_bf16 v[16:19], a[72:75], a[200:203], v[16:19]\n"
    "buffer_load_dwordx4 v236, s[48:51], s74 offen offset:0 lds\n"
    "ds_read_b64_tr_b16 a[4:5], v169 offset:0\n"
    "ds_read_b64_tr_b16 a[6:7], v169 offset:4096\n"
    "v_lshlrev_b32_e64 v162, 16, v155\n"
    "v_mfma_f32_16x16x32_bf16 v[20:23], a[72:75], a[204:207], v[20:23]\n"
    "ds_read_b64_tr_b16 a[8:9], v170 offset:0\n"
    "ds_read_b64_tr_b16 a[10:11], v170 offset:4096\n"
    "v_and_b32_e64 v163, s81, v155\n"
    "v_mfma_f32_16x16x32_bf16 v[24:27], a[76:79], a[200:203], v[24:27]\n"
    "ds_read_b64_tr_b16 a[12:13], v171 offset:0\n"
    "ds_read_b64_tr_b16 a[14:15], v171 offset:4096\n"
    "v_fma_f32 v160, v160, v188, v192\n"
    "v_mfma_f32_16x16x32_bf16 v[28:31], a[76:79], a[204:207], v[28:31]\n"
    "buffer_load_dwordx4 v237, s[48:51], s74 offen offset:1024 lds\n"
    "ds_read_b64_tr_b16 a[16:17], v172 offset:0\n"
    "ds_read_b64_tr_b16 a[18:19], v172 offset:4096\n"
    "v_fma_f32 v161, v161, v189, v193\n"
    "v_mfma_f32_16x16x32_bf16 v[32:35], a[80:83], a[200:203], v[32:35]\n"
    "ds_read_b64_tr_b16 a[20:21], v173 offset:0\n"
    "ds_read_b64_tr_b16 a[22:23], v173 offset:4096\n"
    "v_fma_f32 v162, v162, v190, v194\n"
    "v_mfma_f32_16x16x32_bf16 v[36:39], a[80:83], a[204:207], v[36:39]\n"
    "ds_read_b64_tr_b16 a[24:25], v174 offset:0\n"
    "ds_read_b64_tr_b16 a[26:27], v174 offset:4096\n"
    "v_fma_f32 v163, v163, v191, v195\n"
    "v_mfma_f32_16x16x32_bf16 v[40:43], a[84:87], a[200:203], v[40:43]\n"
    "buffer_load_dwordx4 v238, s[48:51], s74 offen offset:2048 lds\n"
    "ds_read_b64_tr_b16 a[28:29], v175 offset:0\n"
    "v_cvt_pk_bf16_f32 v154, v160, v161\n"
    "v_mfma_f32_16x16x32_bf16 v[44:47], a[84:87], a[204:207], v[44:47]\n"
    "ds_read_b64_tr_b16 a[30:31], v175 offset:4096\n"
    "ds_read_b64_tr_b16 a[32:33], v168 offset:256\n"
    "v_cvt_pk_bf16_f32 v155, v162, v163\n"
    "v_mfma_f32_16x16x32_bf16 v[48:51], a[88:91], a[200:203], v[48:51]\n"
    "ds_read_b64_tr_b16 a[34:35], v168 offset:4352\n"
    "ds_read_b64_tr_b16 a[36:37], v169 offset:256\n"
    "ds_write_b128 v185, v[152:155] offset:7936\n"
    "v_mfma_f32_16x16x32_bf16 v[52:55], a[88:91], a[204:207], v[52:55]\n"
    "buffer_load_dwordx4 v239, s[48:51], s74 offen offset:3072 lds\n"
    "ds_read_b64_tr_b16 a[38:39], v169 offset:4352\n"
    "v_lshlrev_b32_e64 v164, 16, v158\n"
    "v_mfma_f32_16x16x32_bf16 v[56:59], a[92:95], a[200:203], v[56:59]\n"
    "ds_read_b64_tr_b16 a[40:41], v170 offset:256\n"
    "ds_read_b64_tr_b16 a[42:43], v170 offset:4352\n"
    "v_and_b32_e64 v165, s81, v158\n"
    "v_mfma_f32_16x16x32_bf16 v[60:63], a[92:95], a[204:207], v[60:63]\n"
    "ds_read_b64_tr_b16 a[44:45], v171 offset:256\n"
    "ds_read_b64_tr_b16 a[46:47], v171 offset:4352\n"
    "v_lshlrev_b32_e64 v166, 16, v159\n"
    "v_mfma_f32_16x16x32_bf16 v[64:67], a[96:99], a[200:203], v[64:67]\n"
    "ds_read_b64_tr_b16 a[48:49], v172 offset:256\n"
    "v_and_b32_e64 v167, s81, v159\n"
    "s_add_u32 s74, s74, 0x40000\n"
    "s_add_u32 m0, m0, 0x1000\n"
    "v_mfma_f32_16x16x32_bf16 v[68:71], a[96:99], a[204:207], v[68:71]\n"
    "buffer_load_dwordx4 v236, s[48:51], s75 offen offset:0 lds\n"
    "ds_read_b64_tr_b16 a[50:51], v172 offset:4352\n"
    "v_fma_f32 v164, v164, v188, v192\n"
    "v_mfma_f32_16x16x32_bf16 v[72:75], a[100:103], a[200:203], v[72:75]\n"
    "ds_read_b64_tr_b16 a[52:53], v173 offset:256\n"
    "ds_read_b64_tr_b16 a[54:55], v173 offset:4352\n"
    "v_fma_f32 v165, v165, v189, v193\n"
    "v_mfma_f32_16x16x32_bf16 v[76:79], a[100:103], a[204:207], v[76:79]\n"
    "ds_read_b64_tr_b16 a[56:57], v174 offset:256\n"
    "ds_read_b64_tr_b16 a[58:59], v174 offset:4352\n"
    "v_fma_f32 v166, v166, v190, v194\n"
    "v_mfma_f32_16x16x32_bf16 v[80:83], a[104:107], a[200:203], v[80:83]\n"
    "buffer_load_dwordx4 v237, s[48:51], s75 offen offset:1024 lds\n"
    "ds_read_b64_tr_b16 a[60:61], v175 offset:256\n"
    "v_fma_f32 v167, v167, v191, v195\n"
    "v_mfma_f32_16x16x32_bf16 v[84:87], a[104:107], a[204:207], v[84:87]\n"
    "ds_read_b64_tr_b16 a[62:63], v175 offset:4352\n"
    "v_cvt_pk_bf16_f32 v158, v164, v165\n"
    "v_mfma_f32_16x16x32_bf16 v[88:91], a[108:111], a[200:203], v[88:91]\n"
    "v_cvt_pk_bf16_f32 v159, v166, v167\n"
    "v_mfma_f32_16x16x32_bf16 v[92:95], a[108:111], a[204:207], v[92:95]\n"
    "buffer_load_dwordx4 v238, s[48:51], s75 offen offset:2048 lds\n"
    "ds_write_b128 v185, v[156:159] offset:11648\n"
    "v_mfma_f32_16x16x32_bf16 v[96:99], a[112:115], a[200:203], v[96:99]\n"
    "v_mfma_f32_16x16x32_bf16 v[100:103], a[112:115], a[204:207], v[100:103]\n"
    "v_mfma_f32_16x16x32_bf16 v[104:107], a[116:119], a[200:203], v[104:107]\n"
    "buffer_load_dwordx4 v239, s[48:51], s75 offen offset:3072 lds\n"
    "v_mfma_f32_16x16x32_bf16 v[108:111], a[116:119], a[204:207], v[108:111]\n"
    "v_mfma_f32_16x16x32_bf16 v[112:115], a[120:123], a[200:203], v[112:115]\n"
    "v_mfma_f32_16x16x32_bf16 v[116:119], a[120:123], a[204:207], v[116:119]\n"
    "v_mfma_f32_16x16x32_bf16 v[120:123], a[124:127], a[200:203], v[120:123]\n"
    "v_mfma_f32_16x16x32_bf16 v[124:127], a[124:127], a[204:207], v[124:127]\n"
    "s_waitcnt lgkmcnt(15)\n"
    "s_nop 0\n"
    "v_mfma_f32_16x16x32_bf16 v[0:3], a[0:3], a[192:195], v[0:3]\n"
    "v_mfma_f32_16x16x32_bf16 v[4:7], a[0:3], a[196:199], v[4:7]\n"
    "s_waitcnt vmcnt(8) lgkmcnt(0)\n"
    "s_barrier\n"
    "v_mfma_f32_16x16x32_bf16 v[8:11], a[4:7], a[192:195], v[8:11]\n"
    "ds_read_b128 a[200:203], v184 offset:448\n"
    "ds_read_b128 a[204:207], v184 offset:15296\n"
    "v_mfma_f32_16x16x32_bf16 v[12:15], a[4:7], a[196:199], v[12:15]\n"
    "ds_read_b64_tr_b16 a[64:65], v176 offset:32768\n"
    "ds_read_b64_tr_b16 a[66:67], v176 offset:36864\n"
    "s_add_u32 s75, s75, 0x40000\n"
    "s_add_u32 m0, s76, 65536\n"
    "v_mfma_f32_16x16x32_bf16 v[16:19], a[8:11], a[192:195], v[16:19]\n"
    "buffer_load_dwordx4 v236, s[48:51], s74 offen offset:0 lds\n"
    "ds_read_b64_tr_b16 a[68:69], v177 offset:32768\n"
    "ds_read_b64_tr_b16 a[70:71], v177 offset:36864\n"
    "v_mfma_f32_16x16x32_bf16 v[20:23], a[8:11], a[196:199], v[20:23]\n"
    "ds_read_b64_tr_b16 a[72:73], v178 offset:32768\n"
    "ds_read_b64_tr_b16 a[74:75], v178 offset:36864\n"
    "v_mfma_f32_16x16x32_bf16 v[24:27], a[12:15], a[192:195], v[24:27]\n"
    "ds_read_b64_tr_b16 a[76:77], v179 offset:32768\n"
    "ds_read_b64_tr_b16 a[78:79], v179 offset:36864\n"
    "v_mfma_f32_16x16x32_bf16 v[28:31], a[12:15], a[196:199], v[28:31]\n"
    "buffer_load_dwordx4 v237, s[48:51], s74 offen offset:1024 lds\n"
    "ds_read_b64_tr_b16 a[80:81], v180 offset:32768\n"
    "ds_read_b64_tr_b16 a[82:83], v180 offset:36864\n"
    "v_mfma_f32_16x16x32_bf16 v[32:35], a[16:19], a[192:195], v[32:35]\n"
    "ds_read_b64_tr_b16 a[84:85], v181 offset:32768\n"
    "ds_read_b64_tr_b16 a[86:87], v181 offset:36864\n"
    "v_mfma_f32_16x16x32_bf16 v[36:39], a[16:19], a[196:199], v[36:39]\n"
    "ds_read_b64_tr_b16 a[88:89], v182 offset:32768\n"
    "ds_read_b64_tr_b16 a[90:91], v182 offset:36864\n"
    "v_mfma_f32_16x16x32_bf16 v[40:43], a[20:23], a[192:195], v[40:43]\n"
    "buffer_load_dwordx4 v238, s[48:51], s74 offen offset:2048 lds\n"
    "ds_read_b64_tr_b16 a[92:93], v183 offset:32768\n"
    "v_mfma_f32_16x16x32_bf16 v[44:47], a[20:23], a[196:199], v[44:47]\n"
    "ds_read_b64_tr_b16 a[94:95], v183 offset:36864\n"
    "ds_read_b64_tr_b16 a[96:97], v176 offset:33024\n"
    "v_mfma_f32_16x16x32_bf16 v[48:51], a[24:27], a[192:195], v[48:51]\n"
    "ds_read_b64_tr_b16 a[98:99], v176 offset:37120\n"
    "ds_read_b64_tr_b16 a[100:101], v177 offset:33024\n"
    "v_mfma_f32_16x16x32_bf16 v[52:55], a[24:27], a[196:199], v[52:55]\n"
    "buffer_load_dwordx4 v239, s[48:51], s74 offen offset:3072 lds\n"
    "ds_read_b64_tr_b16 a[102:103], v177 offset:37120\n"
    "v_mfma_f32_16x16x32_bf16 v[56:59], a[28:31], a[192:195], v[56:59]\n"
    "ds_read_b64_tr_b16 a[104:105], v178 offset:33024\n"
    "ds_read_b64_tr_b16 a[106:107], v178 offset:37120\n"
    "v_mfma_f32_16x16x32_bf16 v[60:63], a[28:31], a[196:199], v[60:63]\n"
    "ds_read_b64_tr_b16 a[108:109], v179 offset:33024\n"
    "ds_read_b64_tr_b16 a[110:111], v179 offset:37120\n"
    "v_mfma_f32_16x16x32_bf16 v[64:67], a[32:35], a[192:195], v[64:67]\n"
    "ds_read_b64_tr_b16 a[112:113], v180 offset:33024\n"
    "s_add_u32 s74, s74, 0x40000\n"
    "s_add_u32 m0, m0, 0x1000\n"
    "v_mfma_f32_16x16x32_bf16 v[68:71], a[32:35], a[196:199], v[68:71]\n"
    "buffer_load_dwordx4 v236, s[48:51], s75 offen offset:0 lds\n"
    "ds_read_b64_tr_b16 a[114:115], v180 offset:37120\n"
    "v_mfma_f32_16x16x32_bf16 v[72:75], a[36:39], a[192:195], v[72:75]\n"
    "ds_read_b64_tr_b16 a[116:117], v181 offset:33024\n"
    "ds_read_b64_tr_b16 a[118:119], v181 offset:37120\n"
    "v_mfma_f32_16x16x32_bf16 v[76:79], a[36:39], a[196:199], v[76:79]\n"
    "ds_read_b64_tr_b16 a[120:121], v182 offset:33024\n"
    "ds_read_b64_tr_b16 a[122:123], v182 offset:37120\n"
    "v_mfma_f32_16x16x32_bf16 v[80:83], a[40:43], a[192:195], v[80:83]\n"
    "buffer_load_dwordx4 v237, s[48:51], s75 offen offset:1024 lds\n"
    "ds_read_b64_tr_b16 a[124:125], v183 offset:33024\n"
    "v_mfma_f32_16x16x32_bf16 v[84:87], a[40:43], a[196:199], v[84:87]\n"
    "ds_read_b64_tr_b16 a[126:127], v183 offset:37120\n"
    "v_mfma_f32_16x16x32_bf16 v[88:91], a[44:47], a[192:195], v[88:91]\n"
    "v_mfma_f32_16x16x32_bf16 v[92:95], a[44:47], a[196:199], v[92:95]\n"
    "buffer_load_dwordx4 v238, s[48:51], s75 offen offset:2048 lds\n"
    "v_mfma_f32_16x16x32_bf16 v[96:99], a[48:51], a[192:195], v[96:99]\n"
    "v_mfma_f32_16x16x32_bf16 v[100:103], a[48:51], a[196:199], v[100:103]\n"
    "v_mfma_f32_16x16x32_bf16 v[104:107], a[52:55], a[192:195], v[104:107]\n"
    "buffer_load_dwordx4 v239, s[48:51], s75 offen offset:3072 lds\n"
    "v_mfma_f32_16x16x32_bf16 v[108:111], a[52:55], a[196:199], v[108:111]\n"
    "v_mfma_f32_16x16x32_bf16 v[112:115], a[56:59], a[192:195], v[112:115]\n"
    "ds_read_b128 v[188:191], v198 offset:0\n"
    "v_mfma_f32_16x16x32_bf16 v[116:119], a[56:59], a[196:199], v[116:119]\n"
    "ds_read_b128 v[192:195], v198 offset:1792\n"
    "v_mfma_f32_16x16x32_bf16 v[120:123], a[60:63], a[192:195], v[120:123]\n"
    "v_mfma_f32_16x16x32_bf16 v[124:127], a[60:63], a[196:199], v[124:127]\n"
    "s_waitcnt lgkmcnt(15)\n"
    "s_nop 0\n"
    "v_mfma_f32_16x16x32_bf16 v[0:3], a[64:67], a[200:203], v[0:3]\n"
    "v_mfma_f32_16x16x32_bf16 v[4:7], a[64:67], a[204:207], v[4:7]\n"
    "s_waitcnt vmcnt(8) lgkmcnt(0)\n"
    "s_barrier\n"
    "v_mfma_f32_16x16x32_bf16 v[8:11], a[68:71], a[200:203], v[8:11]\n"
    "ds_read_b128 a[192:195], v184 offset:512\n"
    "ds_read_b128 a[196:199], v184 offset:15360\n"
    "v_lshlrev_b32_e64 v160, 16, v128\n"
    "v_mfma_f32_16x16x32_bf16 v[12:15], a[68:71], a[204:207], v[12:15]\n"
    "ds_read_b64_tr_b16 a[0:1], v176 offset:0\n"
    "ds_read_b64_tr_b16 a[2:3], v176 offset:4096\n"
    "v_and_b32_e64 v161, s81, v128\n"
    "s_add_u32 s75, s75, 0x40000\n"
    "s_add_u32 m0, s76, 32768\n"
    "v_mfma_f32_16x16x32_bf16 v[16:19], a[72:75], a[200:203], v[16:19]\n"
    "buffer_load_dwordx4 v236, s[48:51], s74 offen offset:0 lds\n"
    "ds_read_b64_tr_b16 a[4:5], v177 offset:0\n"
    "ds_read_b64_tr_b16 a[6:7], v177 offset:4096\n"
    "v_lshlrev_b32_e64 v162, 16, v129\n"
    "v_mfma_f32_16x16x32_bf16 v[20:23], a[72:75], a[204:207], v[20:23]\n"
    "ds_read_b64_tr_b16 a[8:9], v178 offset:0\n"
    "ds_read_b64_tr_b16 a[10:11], v178 offset:4096\n"
    "v_and_b32_e64 v163, s81, v129\n"
    "v_mfma_f32_16x16x32_bf16 v[24:27], a[76:79], a[200:203], v[24:27]\n"
    "ds_read_b64_tr_b16 a[12:13], v179 offset:0\n"
    "ds_read_b64_tr_b16 a[14:15], v179 offset:4096\n"
    "v_fma_f32 v160, v160, v188, v192\n"
    "v_mfma_f32_16x16x32_bf16 v[28:31], a[76:79], a[204:207], v[28:31]\n"
    "buffer_load_dwordx4 v237, s[48:51], s74 offen offset:1024 lds\n"
    "ds_read_b64_tr_b16 a[16:17], v180 offset:0\n"
    "ds_read_b64_tr_b16 a[18:19], v180 offset:4096\n"
    "v_fma_f32 v161, v161, v189, v193\n"
    "v_mfma_f32_16x16x32_bf16 v[32:35], a[80:83], a[200:203], v[32:35]\n"
    "ds_read_b64_tr_b16 a[20:21], v181 offset:0\n"
    "ds_read_b64_tr_b16 a[22:23], v181 offset:4096\n"
    "v_fma_f32 v162, v162, v190, v194\n"
    "v_mfma_f32_16x16x32_bf16 v[36:39], a[80:83], a[204:207], v[36:39]\n"
    "ds_read_b64_tr_b16 a[24:25], v182 offset:0\n"
    "ds_read_b64_tr_b16 a[26:27], v182 offset:4096\n"
    "v_fma_f32 v163, v163, v191, v195\n"
    "v_mfma_f32_16x16x32_bf16 v[40:43], a[84:87], a[200:203], v[40:43]\n"
    "buffer_load_dwordx4 v238, s[48:51], s74 offen offset:2048 lds\n"
    "ds_read_b64_tr_b16 a[28:29], v183 offset:0\n"
    "v_cvt_pk_bf16_f32 v128, v160, v161\n"
    "v_mfma_f32_16x16x32_bf16 v[44:47], a[84:87], a[204:207], v[44:47]\n"
    "ds_read_b64_tr_b16 a[30:31], v183 offset:4096\n"
    "ds_read_b64_tr_b16 a[32:33], v176 offset:256\n"
    "v_cvt_pk_bf16_f32 v129, v162, v163\n"
    "v_mfma_f32_16x16x32_bf16 v[48:51], a[88:91], a[200:203], v[48:51]\n"
    "ds_read_b64_tr_b16 a[34:35], v176 offset:4352\n"
    "ds_read_b64_tr_b16 a[36:37], v177 offset:256\n"
    "v_lshlrev_b32_e64 v164, 16, v132\n"
    "v_mfma_f32_16x16x32_bf16 v[52:55], a[88:91], a[204:207], v[52:55]\n"
    "buffer_load_dwordx4 v239, s[48:51], s74 offen offset:3072 lds\n"
    "ds_read_b64_tr_b16 a[38:39], v177 offset:4352\n"
    "v_and_b32_e64 v165, s81, v132\n"
    "v_mfma_f32_16x16x32_bf16 v[56:59], a[92:95], a[200:203], v[56:59]\n"
    "ds_read_b64_tr_b16 a[40:41], v178 offset:256\n"
    "ds_read_b64_tr_b16 a[42:43], v178 offset:4352\n"
    "v_lshlrev_b32_e64 v166, 16, v133\n"
    "v_mfma_f32_16x16x32_bf16 v[60:63], a[92:95], a[204:207], v[60:63]\n"
    "ds_read_b64_tr_b16 a[44:45], v179 offset:256\n"
    "ds_read_b64_tr_b16 a[46:47], v179 offset:4352\n"
    "v_and_b32_e64 v167, s81, v133\n"
    "v_mfma_f32_16x16x32_bf16 v[64:67], a[96:99], a[200:203], v[64:67]\n"
    "ds_read_b64_tr_b16 a[48:49], v180 offset:256\n"
    "v_fma_f32 v164, v164, v188, v192\n"
    "s_add_u32 s74, s74, 0x40000\n"
    "s_add_u32 m0, m0, 0x1000\n"
    "v_mfma_f32_16x16x32_bf16 v[68:71], a[96:99], a[204:207], v[68:71]\n"
    "buffer_load_dwordx4 v236, s[48:51], s75 offen offset:0 lds\n"
    "ds_read_b64_tr_b16 a[50:51], v180 offset:4352\n"
    "v_fma_f32 v165, v165, v189, v193\n"
    "v_mfma_f32_16x16x32_bf16 v[72:75], a[100:103], a[200:203], v[72:75]\n"
    "ds_read_b64_tr_b16 a[52:53], v181 offset:256\n"
    "ds_read_b64_tr_b16 a[54:55], v181 offset:4352\n"
    "v_fma_f32 v166, v166, v190, v194\n"
    "v_mfma_f32_16x16x32_bf16 v[76:79], a[100:103], a[204:207], v[76:79]\n"
    "ds_read_b64_tr_b16 a[56:57], v182 offset:256\n"
    "ds_read_b64_tr_b16 a[58:59], v182 offset:4352\n"
    "v_fma_f32 v167, v167, v191, v195\n"
    "v_mfma_f32_16x16x32_bf16 v[80:83], a[104:107], a[200:203], v[80:83]\n"
    "buffer_load_dwordx4 v237, s[48:51], s75 offen offset:1024 lds\n"
    "ds_read_b64_tr_b16 a[60:61], v183 offset:256\n"
    "v_cvt_pk_bf16_f32 v132, v164, v165\n"
    "v_mfma_f32_16x16x32_bf16 v[84:87], a[104:107], a[204:207], v[84:87]\n"
    "ds_read_b64_tr_b16 a[62:63], v183 offset:4352\n"
    "v_cvt_pk_bf16_f32 v133, v166, v167\n"
    "v_mfma_f32_16x16x32_bf16 v[88:91], a[108:111], a[200:203], v[88:91]\n"
    "v_mfma_f32_16x16x32_bf16 v[92:95], a[108:111], a[204:207], v[92:95]\n"
    "buffer_load_dwordx4 v238, s[48:51], s75 offen offset:2048 lds\n"
    "v_mfma_f32_16x16x32_bf16 v[96:99], a[112:115], a[200:203], v[96:99]\n"
    "v_mfma_f32_16x16x32_bf16 v[100:103], a[112:115], a[204:207], v[100:103]\n"
    "v_mfma_f32_16x16x32_bf16 v[104:107], a[116:119], a[200:203], v[104:107]\n"
    "buffer_load_dwordx4 v239, s[48:51], s75 offen offset:3072 lds\n"
    "v_mfma_f32_16x16x32_bf16 v[108:111], a[116:119], a[204:207], v[108:111]\n"
    "v_mfma_f32_16x16x32_bf16 v[112:115], a[120:123], a[200:203], v[112:115]\n"
    "ds_read_b128 v[188:191], v198 offset:16\n"
    "v_mfma_f32_16x16x32_bf16 v[116:119], a[120:123], a[204:207], v[116:119]\n"
    "ds_read_b128 v[192:195], v198 offset:1808\n"
    "v_mfma_f32_16x16x32_bf16 v[120:123], a[124:127], a[200:203], v[120:123]\n"
    "v_mfma_f32_16x16x32_bf16 v[124:127], a[124:127], a[204:207], v[124:127]\n"
    "s_waitcnt lgkmcnt(15)\n"
    "s_nop 0\n"
    "v_mfma_f32_16x16x32_bf16 v[0:3], a[0:3], a[192:195], v[0:3]\n"
    "v_mfma_f32_16x16x32_bf16 v[4:7], a[0:3], a[196:199], v[4:7]\n"
    "s_waitcnt vmcnt(8) lgkmcnt(0)\n"
    "s_barrier\n"
    "v_mfma_f32_16x16x32_bf16 v[8:11], a[4:7], a[192:195], v[8:11]\n"
    "ds_read_b128 a[200:203], v184 offset:576\n"
    "ds_read_b128 a[204:207], v184 offset:15424\n"
    "v_lshlrev_b32_e64 v160, 16, v130\n"
    "v_mfma_f32_16x16x32_bf16 v[12:15], a[4:7], a[196:199], v[12:15]\n"
    "ds_read_b64_tr_b16 a[64:65], v168 offset:0\n"
    "ds_read_b64_tr_b16 a[66:67], v168 offset:4096\n"
    "v_and_b32 v161, s81, v130\n"
    "s_add_u32 s75, s75, 0x40000\n"
    "s_add_u32 m0, s76, 0\n"
    "v_mfma_f32_16x16x32_bf16 v[16:19], a[8:11], a[192:195], v[16:19]\n"
    "buffer_load_dwordx4 v236, s[48:51], s74 offen offset:0 lds\n"
    "ds_read_b64_tr_b16 a[68:69], v169 offset:0\n"
    "ds_read_b64_tr_b16 a[70:71], v169 offset:4096\n"
    "v_lshlrev_b32_e64 v162, 16, v131\n"
    "v_mfma_f32_16x16x32_bf16 v[20:23], a[8:11], a[196:199], v[20:23]\n"
    "ds_read_b64_tr_b16 a[72:73], v170 offset:0\n"
    "ds_read_b64_tr_b16 a[74:75], v170 offset:4096\n"
    "v_and_b32_e64 v163, s81, v131\n"
    "v_mfma_f32_16x16x32_bf16 v[24:27], a[12:15], a[192:195], v[24:27]\n"
    "ds_read_b64_tr_b16 a[76:77], v171 offset:0\n"
    "ds_read_b64_tr_b16 a[78:79], v171 offset:4096\n"
    "v_fma_f32 v160, v160, v188, v192\n"
    "v_mfma_f32_16x16x32_bf16 v[28:31], a[12:15], a[196:199], v[28:31]\n"
    "buffer_load_dwordx4 v237, s[48:51], s74 offen offset:1024 lds\n"
    "ds_read_b64_tr_b16 a[80:81], v172 offset:0\n"
    "ds_read_b64_tr_b16 a[82:83], v172 offset:4096\n"
    "v_fma_f32 v161, v161, v189, v193\n"
    "v_mfma_f32_16x16x32_bf16 v[32:35], a[16:19], a[192:195], v[32:35]\n"
    "ds_read_b64_tr_b16 a[84:85], v173 offset:0\n"
    "ds_read_b64_tr_b16 a[86:87], v173 offset:4096\n"
    "v_fma_f32 v162, v162, v190, v194\n"
    "v_mfma_f32_16x16x32_bf16 v[36:39], a[16:19], a[196:199], v[36:39]\n"
    "ds_read_b64_tr_b16 a[88:89], v174 offset:0\n"
    "ds_read_b64_tr_b16 a[90:91], v174 offset:4096\n"
    "v_fma_f32 v163, v163, v191, v195\n"
    "v_mfma_f32_16x16x32_bf16 v[40:43], a[20:23], a[192:195], v[40:43]\n"
    "buffer_load_dwordx4 v238, s[48:51], s74 offen offset:2048 lds\n"
    "ds_read_b64_tr_b16 a[92:93], v175 offset:0\n"
    "v_cvt_pk_bf16_f32 v130, v160, v161\n"
    "v_mfma_f32_16x16x32_bf16 v[44:47], a[20:23], a[196:199], v[44:47]\n"
    "ds_read_b64_tr_b16 a[94:95], v175 offset:4096\n"
    "ds_read_b64_tr_b16 a[96:97], v168 offset:256\n"
    "v_cvt_pk_bf16_f32 v131, v162, v163\n"
    "v_mfma_f32_16x16x32_bf16 v[48:51], a[24:27], a[192:195], v[48:51]\n"
    "ds_read_b64_tr_b16 a[98:99], v168 offset:4352\n"
    "ds_read_b64_tr_b16 a[100:101], v169 offset:256\n"
    "ds_write_b128 v199, v[128:131] offset:0\n"
    "v_mfma_f32_16x16x32_bf16 v[52:55], a[24:27], a[196:199], v[52:55]\n"
    "buffer_load_dwordx4 v239, s[48:51], s74 offen offset:3072 lds\n"
    "ds_read_b64_tr_b16 a[102:103], v169 offset:4352\n"
    "v_lshlrev_b32_e64 v164, 16, v134\n"
    "v_mfma_f32_16x16x32_bf16 v[56:59], a[28:31], a[192:195], v[56:59]\n"
    "ds_read_b64_tr_b16 a[104:105], v170 offset:256\n"
    "ds_read_b64_tr_b16 a[106:107], v170 offset:4352\n"
    "v_and_b32_e64 v165, s81, v134\n"
    "v_mfma_f32_16x16x32_bf16 v[60:63], a[28:31], a[196:199], v[60:63]\n"
    "ds_read_b64_tr_b16 a[108:109], v171 offset:256\n"
    "ds_read_b64_tr_b16 a[110:111], v171 offset:4352\n"
    "v_lshlrev_b32_e64 v166, 16, v135\n"
    "v_mfma_f32_16x16x32_bf16 v[64:67], a[32:35], a[192:195], v[64:67]\n"
    "ds_read_b64_tr_b16 a[112:113], v172 offset:256\n"
    "v_and_b32_e64 v167, s81, v135\n"
    "s_add_u32 s74, s74, 0x40000\n"
    "s_add_u32 m0, m0, 0x1000\n"
    "v_mfma_f32_16x16x32_bf16 v[68:71], a[32:35], a[196:199], v[68:71]\n"
    "buffer_load_dwordx4 v236, s[48:51], s75 offen offset:0 lds\n"
    "ds_read_b64_tr_b16 a[114:115], v172 offset:4352\n"
    "v_fma_f32 v164, v164, v188, v192\n"
    "v_mfma_f32_16x16x32_bf16 v[72:75], a[36:39], a[192:195], v[72:75]\n"
    "ds_read_b64_tr_b16 a[116:117], v173 offset:256\n"
    "ds_read_b64_tr_b16 a[118:119], v173 offset:4352\n"
    "v_fma_f32 v165, v165, v189, v193\n"
    "v_mfma_f32_16x16x32_bf16 v[76:79], a[36:39], a[196:199], v[76:79]\n"
    "ds_read_b64_tr_b16 a[120:121], v174 offset:256\n"
    "ds_read_b64_tr_b16 a[122:123], v174 offset:4352\n"
    "v_fma_f32 v166, v166, v190, v194\n"
    "v_mfma_f32_16x16x32_bf16 v[80:83], a[40:43], a[192:195], v[80:83]\n"
    "buffer_load_dwordx4 v237, s[48:51], s75 offen offset:1024 lds\n"
    "ds_read_b64_tr_b16 a[124:125], v175 offset:256\n"
    "v_fma_f32 v167, v167, v191, v195\n"
    "v_mfma_f32_16x16x32_bf16 v[84:87], a[40:43], a[196:199], v[84:87]\n"
    "ds_read_b64_tr_b16 a[126:127], v175 offset:4352\n"
    "v_cvt_pk_bf16_f32 v134, v164, v165\n"
    "v_mfma_f32_16x16x32_bf16 v[88:91], a[44:47], a[192:195], v[88:91]\n"
    "v_cvt_pk_bf16_f32 v135, v166, v167\n"
    "v_mfma_f32_16x16x32_bf16 v[92:95], a[44:47], a[196:199], v[92:95]\n"
    "buffer_load_dwordx4 v238, s[48:51], s75 offen offset:2048 lds\n"
    "ds_write_b128 v199, v[132:135] offset:7424\n"
    "v_mfma_f32_16x16x32_bf16 v[96:99], a[48:51], a[192:195], v[96:99]\n"
    "v_mfma_f32_16x16x32_bf16 v[100:103], a[48:51], a[196:199], v[100:103]\n"
    "v_mfma_f32_16x16x32_bf16 v[104:107], a[52:55], a[192:195], v[104:107]\n"
    "buffer_load_dwordx4 v239, s[48:51], s75 offen offset:3072 lds\n"
    "v_mfma_f32_16x16x32_bf16 v[108:111], a[52:55], a[196:199], v[108:111]\n"
    "v_mfma_f32_16x16x32_bf16 v[112:115], a[56:59], a[192:195], v[112:115]\n"
    "v_mfma_f32_16x16x32_bf16 v[116:119], a[56:59], a[196:199], v[116:119]\n"
    "v_mfma_f32_16x16x32_bf16 v[120:123], a[60:63], a[192:195], v[120:123]\n"
    "v_mfma_f32_16x16x32_bf16 v[124:127], a[60:63], a[196:199], v[124:127]\n"
    "s_waitcnt lgkmcnt(15)\n"
    "s_nop 0\n"
    "v_mfma_f32_16x16x32_bf16 v[0:3], a[64:67], a[200:203], v[0:3]\n"
    "v_mfma_f32_16x16x32_bf16 v[4:7], a[64:67], a[204:207], v[4:7]\n"
    "s_waitcnt vmcnt(8) lgkmcnt(0)\n"
    "s_barrier\n"
    "v_mfma_f32_16x16x32_bf16 v[8:11], a[68:71], a[200:203], v[8:11]\n"
    "ds_read_b128 a[192:195], v184 offset:640\n"
    "ds_read_b128 a[196:199], v184 offset:15488\n"
    "v_mfma_f32_16x16x32_bf16 v[12:15], a[68:71], a[204:207], v[12:15]\n"
    "ds_read_b64_tr_b16 a[0:1], v176 offset:32768\n"
    "ds_read_b64_tr_b16 a[2:3], v176 offset:36864\n"
    "s_add_u32 s75, s75, 0x40000\n"
    "s_add_u32 m0, s76, 65536\n"
    "v_mfma_f32_16x16x32_bf16 v[16:19], a[72:75], a[200:203], v[16:19]\n"
    "buffer_load_dwordx4 v236, s[48:51], s74 offen offset:0 lds\n"
    "ds_read_b64_tr_b16 a[4:5], v177 offset:32768\n"
    "ds_read_b64_tr_b16 a[6:7], v177 offset:36864\n"
    "v_mfma_f32_16x16x32_bf16 v[20:23], a[72:75], a[204:207], v[20:23]\n"
    "ds_read_b64_tr_b16 a[8:9], v178 offset:32768\n"
    "ds_read_b64_tr_b16 a[10:11], v178 offset:36864\n"
    "v_mfma_f32_16x16x32_bf16 v[24:27], a[76:79], a[200:203], v[24:27]\n"
    "ds_read_b64_tr_b16 a[12:13], v179 offset:32768\n"
    "ds_read_b64_tr_b16 a[14:15], v179 offset:36864\n"
    "v_mfma_f32_16x16x32_bf16 v[28:31], a[76:79], a[204:207], v[28:31]\n"
    "buffer_load_dwordx4 v237, s[48:51], s74 offen offset:1024 lds\n"
    "ds_read_b64_tr_b16 a[16:17], v180 offset:32768\n"
    "ds_read_b64_tr_b16 a[18:19], v180 offset:36864\n"
    "v_mfma_f32_16x16x32_bf16 v[32:35], a[80:83], a[200:203], v[32:35]\n"
    "ds_read_b64_tr_b16 a[20:21], v181 offset:32768\n"
    "ds_read_b64_tr_b16 a[22:23], v181 offset:36864\n"
    "v_mfma_f32_16x16x32_bf16 v[36:39], a[80:83], a[204:207], v[36:39]\n"
    "ds_read_b64_tr_b16 a[24:25], v182 offset:32768\n"
    "ds_read_b64_tr_b16 a[26:27], v182 offset:36864\n"
    "v_mfma_f32_16x16x32_bf16 v[40:43], a[84:87], a[200:203], v[40:43]\n"
    "buffer_load_dwordx4 v238, s[48:51], s74 offen offset:2048 lds\n"
    "ds_read_b64_tr_b16 a[28:29], v183 offset:32768\n"
    "v_mfma_f32_16x16x32_bf16 v[44:47], a[84:87], a[204:207], v[44:47]\n"
    "ds_read_b64_tr_b16 a[30:31], v183 offset:36864\n"
    "ds_read_b64_tr_b16 a[32:33], v176 offset:33024\n"
    "v_mfma_f32_16x16x32_bf16 v[48:51], a[88:91], a[200:203], v[48:51]\n"
    "ds_read_b64_tr_b16 a[34:35], v176 offset:37120\n"
    "ds_read_b64_tr_b16 a[36:37], v177 offset:33024\n"
    "v_mfma_f32_16x16x32_bf16 v[52:55], a[88:91], a[204:207], v[52:55]\n"
    "buffer_load_dwordx4 v239, s[48:51], s74 offen offset:3072 lds\n"
    "ds_read_b64_tr_b16 a[38:39], v177 offset:37120\n"
    "v_mfma_f32_16x16x32_bf16 v[56:59], a[92:95], a[200:203], v[56:59]\n"
    "ds_read_b64_tr_b16 a[40:41], v178 offset:33024\n"
    "ds_read_b64_tr_b16 a[42:43], v178 offset:37120\n"
    "v_mfma_f32_16x16x32_bf16 v[60:63], a[92:95], a[204:207], v[60:63]\n"
    "ds_read_b64_tr_b16 a[44:45], v179 offset:33024\n"
    "ds_read_b64_tr_b16 a[46:47], v179 offset:37120\n"
    "v_mfma_f32_16x16x32_bf16 v[64:67], a[96:99], a[200:203], v[64:67]\n"
    "ds_read_b64_tr_b16 a[48:49], v180 offset:33024\n"
    "s_add_u32 s74, s74, 0x40000\n"
    "s_add_u32 m0, m0, 0x1000\n"
    "v_mfma_f32_16x16x32_bf16 v[68:71], a[96:99], a[204:207], v[68:71]\n"
    "buffer_load_dwordx4 v236, s[48:51], s75 offen offset:0 lds\n"
    "ds_read_b64_tr_b16 a[50:51], v180 offset:37120\n"
    "v_mfma_f32_16x16x32_bf16 v[72:75], a[100:103], a[200:203], v[72:75]\n"
    "ds_read_b64_tr_b16 a[52:53], v181 offset:33024\n"
    "ds_read_b64_tr_b16 a[54:55], v181 offset:37120\n"
    "v_mfma_f32_16x16x32_bf16 v[76:79], a[100:103], a[204:207], v[76:79]\n"
    "ds_read_b64_tr_b16 a[56:57], v182 offset:33024\n"
    "ds_read_b64_tr_b16 a[58:59], v182 offset:37120\n"
    "v_mfma_f32_16x16x32_bf16 v[80:83], a[104:107], a[200:203], v[80:83]\n"
    "buffer_load_dwordx4 v237, s[48:51], s75 offen offset:1024 lds\n"
    "ds_read_b64_tr_b16 a[60:61], v183 offset:33024\n"
    "v_mfma_f32_16x16x32_bf16 v[84:87], a[104:107], a[204:207], v[84:87]\n"
    "ds_read_b64_tr_b16 a[62:63], v183 offset:37120\n"
    "v_mfma_f32_16x16x32_bf16 v[88:91], a[108:111], a[200:203], v[88:91]\n"
    "v_mfma_f32_16x16x32_bf16 v[92:95], a[108:111], a[204:207], v[92:95]\n"
    "buffer_load_dwordx4 v238, s[48:51], s75 offen offset:2048 lds\n"
    "v_mfma_f32_16x16x32_bf16 v[96:99], a[112:115], a[200:203], v[96:99]\n"
    "v_mfma_f32_16x16x32_bf16 v[100:103], a[112:115], a[204:207], v[100:103]\n"
    "v_mfma_f32_16x16x32_bf16 v[104:107], a[116:119], a[200:203], v[104:107]\n"
    "buffer_load_dwordx4 v239, s[48:51], s75 offen offset:3072 lds\n"
    "v_mfma_f32_16x16x32_bf16 v[108:111], a[116:119], a[204:207], v[108:111]\n"
    "v_mfma_f32_16x16x32_bf16 v[112:115], a[120:123], a[200:203], v[112:115]\n"
    "v_mfma_f32_16x16x32_bf16 v[116:119], a[120:123], a[204:207], v[116:119]\n"
    "v_mfma_f32_16x16x32_bf16 v[120:123], a[124:127], a[200:203], v[120:123]\n"
    "v_mfma_f32_16x16x32_bf16 v[124:127], a[124:127], a[204:207], v[124:127]\n"
    "s_waitcnt lgkmcnt(15)\n"
    "s_nop 0\n"
    "v_mfma_f32_16x16x32_bf16 v[0:3], a[0:3], a[192:195], v[0:3]\n"
    "v_mfma_f32_16x16x32_bf16 v[4:7], a[0:3], a[196:199], v[4:7]\n"
    "s_waitcnt vmcnt(8) lgkmcnt(0)\n"
    "s_barrier\n"
    "v_mfma_f32_16x16x32_bf16 v[8:11], a[4:7], a[192:195], v[8:11]\n"
    "ds_read_b128 a[200:203], v184 offset:704\n"
    "ds_read_b128 a[204:207], v184 offset:15552\n"
    "v_mfma_f32_16x16x32_bf16 v[12:15], a[4:7], a[196:199], v[12:15]\n"
    "ds_read_b64_tr_b16 a[64:65], v176 offset:0\n"
    "ds_read_b64_tr_b16 a[66:67], v176 offset:4096\n"
    "s_add_u32 s75, s75, 0x40000\n"
    "s_add_u32 m0, s76, 32768\n"
    "v_mfma_f32_16x16x32_bf16 v[16:19], a[8:11], a[192:195], v[16:19]\n"
    "buffer_load_dwordx4 v236, s[48:51], s74 offen offset:0 lds\n"
    "ds_read_b64_tr_b16 a[68:69], v177 offset:0\n"
    "ds_read_b64_tr_b16 a[70:71], v177 offset:4096\n"
    "v_mfma_f32_16x16x32_bf16 v[20:23], a[8:11], a[196:199], v[20:23]\n"
    "ds_read_b64_tr_b16 a[72:73], v178 offset:0\n"
    "ds_read_b64_tr_b16 a[74:75], v178 offset:4096\n"
    "v_mfma_f32_16x16x32_bf16 v[24:27], a[12:15], a[192:195], v[24:27]\n"
    "ds_read_b64_tr_b16 a[76:77], v179 offset:0\n"
    "ds_read_b64_tr_b16 a[78:79], v179 offset:4096\n"
    "v_mfma_f32_16x16x32_bf16 v[28:31], a[12:15], a[196:199], v[28:31]\n"
    "buffer_load_dwordx4 v237, s[48:51], s74 offen offset:1024 lds\n"
    "ds_read_b64_tr_b16 a[80:81], v180 offset:0\n"
    "ds_read_b64_tr_b16 a[82:83], v180 offset:4096\n"
    "v_mfma_f32_16x16x32_bf16 v[32:35], a[16:19], a[192:195], v[32:35]\n"
    "ds_read_b64_tr_b16 a[84:85], v181 offset:0\n"
    "ds_read_b64_tr_b16 a[86:87], v181 offset:4096\n"
    "v_mfma_f32_16x16x32_bf16 v[36:39], a[16:19], a[196:199], v[36:39]\n"
    "ds_read_b64_tr_b16 a[88:89], v182 offset:0\n"
    "ds_read_b64_tr_b16 a[90:91], v182 offset:4096\n"
    "v_mfma_f32_16x16x32_bf16 v[40:43], a[20:23], a[192:195], v[40:43]\n"
    "buffer_load_dwordx4 v238, s[48:51], s74 offen offset:2048 lds\n"
    "ds_read_b64_tr_b16 a[92:93], v183 offset:0\n"
    "v_mfma_f32_16x16x32_bf16 v[44:47], a[20:23], a[196:199], v[44:47]\n"
    "ds_read_b64_tr_b16 a[94:95], v183 offset:4096\n"
    "ds_read_b64_tr_b16 a[96:97], v176 offset:256\n"
    "v_mfma_f32_16x16x32_bf16 v[48:51], a[24:27], a[192:195], v[48:51]\n"
    "ds_read_b64_tr_b16 a[98:99], v176 offset:4352\n"
    "ds_read_b64_tr_b16 a[100:101], v177 offset:256\n"
    "v_mfma_f32_16x16x32_bf16 v[52:55], a[24:27], a[196:199], v[52:55]\n"
    "buffer_load_dwordx4 v239, s[48:51], s74 offen offset:3072 lds\n"
    "ds_read_b64_tr_b16 a[102:103], v177 offset:4352\n"
    "v_mfma_f32_16x16x32_bf16 v[56:59], a[28:31], a[192:195], v[56:59]\n"
    "ds_read_b64_tr_b16 a[104:105], v178 offset:256\n"
    "ds_read_b64_tr_b16 a[106:107], v178 offset:4352\n"
    "v_mfma_f32_16x16x32_bf16 v[60:63], a[28:31], a[196:199], v[60:63]\n"
    "ds_read_b64_tr_b16 a[108:109], v179 offset:256\n"
    "ds_read_b64_tr_b16 a[110:111], v179 offset:4352\n"
    "v_mfma_f32_16x16x32_bf16 v[64:67], a[32:35], a[192:195], v[64:67]\n"
    "ds_read_b64_tr_b16 a[112:113], v180 offset:256\n"
    "s_add_u32 s74, s74, 0x40000\n"
    "s_add_u32 m0, m0, 0x1000\n"
    "v_mfma_f32_16x16x32_bf16 v[68:71], a[32:35], a[196:199], v[68:71]\n"
    "buffer_load_dwordx4 v236, s[48:51], s75 offen offset:0 lds\n"
    "ds_read_b64_tr_b16 a[114:115], v180 offset:4352\n"
    "v_mfma_f32_16x16x32_bf16 v[72:75], a[36:39], a[192:195], v[72:75]\n"
    "ds_read_b64_tr_b16 a[116:117], v181 offset:256\n"
    "ds_read_b64_tr_b16 a[118:119], v181 offset:4352\n"
    "v_mfma_f32_16x16x32_bf16 v[76:79], a[36:39], a[196:199], v[76:79]\n"
    "ds_read_b64_tr_b16 a[120:121], v182 offset:256\n"
    "ds_read_b64_tr_b16 a[122:123], v182 offset:4352\n"
    "v_mfma_f32_16x16x32_bf16 v[80:83], a[40:43], a[192:195], v[80:83]\n"
    "buffer_load_dwordx4 v237, s[48:51], s75 offen offset:1024 lds\n"
    "ds_read_b64_tr_b16 a[124:125], v183 offset:256\n"
    "v_mfma_f32_16x16x32_bf16 v[84:87], a[40:43], a[196:199], v[84:87]\n"
    "ds_read_b64_tr_b16 a[126:127], v183 offset:4352\n"
    "v_mfma_f32_16x16x32_bf16 v[88:91], a[44:47], a[192:195], v[88:91]\n"
    "v_mfma_f32_16x16x32_bf16 v[92:95], a[44:47], a[196:199], v[92:95]\n"
    "buffer_load_dwordx4 v238, s[48:51], s75 offen offset:2048 lds\n"
    "v_mfma_f32_16x16x32_bf16 v[96:99], a[48:51], a[192:195], v[96:99]\n"
    "v_mfma_f32_16x16x32_bf16 v[100:103], a[48:51], a[196:199], v[100:103]\n"
    "v_mfma_f32_16x16x32_bf16 v[104:107], a[52:55], a[192:195], v[104:107]\n"
    "buffer_load_dwordx4 v239, s[48:51], s75 offen offset:3072 lds\n"
    "v_mfma_f32_16x16x32_bf16 v[108:111], a[52:55], a[196:199], v[108:111]\n"
    "v_mfma_f32_16x16x32_bf16 v[112:115], a[56:59], a[192:195], v[112:115]\n"
    "v_mfma_f32_16x16x32_bf16 v[116:119], a[56:59], a[196:199], v[116:119]\n"
    "v_mfma_f32_16x16x32_bf16 v[120:123], a[60:63], a[192:195], v[120:123]\n"
    "v_mfma_f32_16x16x32_bf16 v[124:127], a[60:63], a[196:199], v[124:127]\n"
    "s_waitcnt lgkmcnt(15)\n"
    "s_nop 0\n"
    "v_mfma_f32_16x16x32_bf16 v[0:3], a[64:67], a[200:203], v[0:3]\n"
    "v_mfma_f32_16x16x32_bf16 v[4:7], a[64:67], a[204:207], v[4:7]\n"
    "s_waitcnt vmcnt(8) lgkmcnt(0)\n"
    "s_barrier\n"
    "v_mfma_f32_16x16x32_bf16 v[8:11], a[68:71], a[200:203], v[8:11]\n"
    "ds_read_b128 a[192:195], v184 offset:768\n"
    "ds_read_b128 a[196:199], v184 offset:15616\n"
    "v_mfma_f32_16x16x32_bf16 v[12:15], a[68:71], a[204:207], v[12:15]\n"
    "ds_read_b64_tr_b16 a[0:1], v168 offset:0\n"
    "ds_read_b64_tr_b16 a[2:3], v168 offset:4096\n"
    "s_mov_b32 s56, 0\n"
    "s_add_u32 m0, s59, 0\n"
    "v_mfma_f32_16x16x32_bf16 v[16:19], a[72:75], a[200:203], v[16:19]\n"
    "buffer_load_dwordx4 v212, s[40:43], s56 offen offset:0 lds\n"
    "ds_read_b64_tr_b16 a[4:5], v169 offset:0\n"
    "ds_read_b64_tr_b16 a[6:7], v169 offset:4096\n"
    "v_mfma_f32_16x16x32_bf16 v[20:23], a[72:75], a[204:207], v[20:23]\n"
    "ds_read_b64_tr_b16 a[8:9], v170 offset:0\n"
    "ds_read_b64_tr_b16 a[10:11], v170 offset:4096\n"
    "v_mfma_f32_16x16x32_bf16 v[24:27], a[76:79], a[200:203], v[24:27]\n"
    "ds_read_b64_tr_b16 a[12:13], v171 offset:0\n"
    "ds_read_b64_tr_b16 a[14:15], v171 offset:4096\n"
    "v_mfma_f32_16x16x32_bf16 v[28:31], a[76:79], a[204:207], v[28:31]\n"
    "buffer_load_dwordx4 v213, s[40:43], s56 offen offset:1024 lds\n"
    "ds_read_b64_tr_b16 a[16:17], v172 offset:0\n"
    "ds_read_b64_tr_b16 a[18:19], v172 offset:4096\n"
    "v_mfma_f32_16x16x32_bf16 v[32:35], a[80:83], a[200:203], v[32:35]\n"
    "ds_read_b64_tr_b16 a[20:21], v173 offset:0\n"
    "ds_read_b64_tr_b16 a[22:23], v173 offset:4096\n"
    "v_mfma_f32_16x16x32_bf16 v[36:39], a[80:83], a[204:207], v[36:39]\n"
    "ds_read_b64_tr_b16 a[24:25], v174 offset:0\n"
    "ds_read_b64_tr_b16 a[26:27], v174 offset:4096\n"
    "v_mfma_f32_16x16x32_bf16 v[40:43], a[84:87], a[200:203], v[40:43]\n"
    "buffer_load_dwordx4 v214, s[40:43], s56 offen offset:2048 lds\n"
    "ds_read_b64_tr_b16 a[28:29], v175 offset:0\n"
    "v_mfma_f32_16x16x32_bf16 v[44:47], a[84:87], a[204:207], v[44:47]\n"
    "ds_read_b64_tr_b16 a[30:31], v175 offset:4096\n"
    "ds_read_b64_tr_b16 a[32:33], v168 offset:256\n"
    "v_mfma_f32_16x16x32_bf16 v[48:51], a[88:91], a[200:203], v[48:51]\n"
    "ds_read_b64_tr_b16 a[34:35], v168 offset:4352\n"
    "ds_read_b64_tr_b16 a[36:37], v169 offset:256\n"
    "v_mfma_f32_16x16x32_bf16 v[52:55], a[88:91], a[204:207], v[52:55]\n"
    "buffer_load_dwordx4 v215, s[40:43], s56 offen offset:3072 lds\n"
    "ds_read_b64_tr_b16 a[38:39], v169 offset:4352\n"
    "v_mfma_f32_16x16x32_bf16 v[56:59], a[92:95], a[200:203], v[56:59]\n"
    "ds_read_b64_tr_b16 a[40:41], v170 offset:256\n"
    "ds_read_b64_tr_b16 a[42:43], v170 offset:4352\n"
    "v_mfma_f32_16x16x32_bf16 v[60:63], a[92:95], a[204:207], v[60:63]\n"
    "ds_read_b64_tr_b16 a[44:45], v171 offset:256\n"
    "ds_read_b64_tr_b16 a[46:47], v171 offset:4352\n"
    "v_mfma_f32_16x16x32_bf16 v[64:67], a[96:99], a[200:203], v[64:67]\n"
    "ds_read_b64_tr_b16 a[48:49], v172 offset:256\n"
    "s_mov_b32 s56, 16384\n"
    "s_add_u32 m0, s59, 16384\n"
    "v_mfma_f32_16x16x32_bf16 v[68:71], a[96:99], a[204:207], v[68:71]\n"
    "buffer_load_dwordx4 v212, s[40:43], s56 offen offset:0 lds\n"
    "ds_read_b64_tr_b16 a[50:51], v172 offset:4352\n"
    "v_mfma_f32_16x16x32_bf16 v[72:75], a[100:103], a[200:203], v[72:75]\n"
    "ds_read_b64_tr_b16 a[52:53], v173 offset:256\n"
    "ds_read_b64_tr_b16 a[54:55], v173 offset:4352\n"
    "v_mfma_f32_16x16x32_bf16 v[76:79], a[100:103], a[204:207], v[76:79]\n"
    "ds_read_b64_tr_b16 a[56:57], v174 offset:256\n"
    "ds_read_b64_tr_b16 a[58:59], v174 offset:4352\n"
    "v_mfma_f32_16x16x32_bf16 v[80:83], a[104:107], a[200:203], v[80:83]\n"
    "buffer_load_dwordx4 v213, s[40:43], s56 offen offset:1024 lds\n"
    "ds_read_b64_tr_b16 a[60:61], v175 offset:256\n"
    "v_mfma_f32_16x16x32_bf16 v[84:87], a[104:107], a[204:207], v[84:87]\n"
    "ds_read_b64_tr_b16 a[62:63], v175 offset:4352\n"
    "v_mfma_f32_16x16x32_bf16 v[88:91], a[108:111], a[200:203], v[88:91]\n"
    "v_mfma_f32_16x16x32_bf16 v[92:95], a[108:111], a[204:207], v[92:95]\n"
    "buffer_load_dwordx4 v214, s[40:43], s56 offen offset:2048 lds\n"
    "v_mfma_f32_16x16x32_bf16 v[96:99], a[112:115], a[200:203], v[96:99]\n"
    "v_mfma_f32_16x16x32_bf16 v[100:103], a[112:115], a[204:207], v[100:103]\n"
    "v_mfma_f32_16x16x32_bf16 v[104:107], a[116:119], a[200:203], v[104:107]\n"
    "buffer_load_dwordx4 v215, s[40:43], s56 offen offset:3072 lds\n"
    "v_mfma_f32_16x16x32_bf16 v[108:111], a[116:119], a[204:207], v[108:111]\n"
    "v_mfma_f32_16x16x32_bf16 v[112:115], a[120:123], a[200:203], v[112:115]\n"
    "v_mfma_f32_16x16x32_bf16 v[116:119], a[120:123], a[204:207], v[116:119]\n"
    "v_mfma_f32_16x16x32_bf16 v[120:123], a[124:127], a[200:203], v[120:123]\n"
    "v_mfma_f32_16x16x32_bf16 v[124:127], a[124:127], a[204:207], v[124:127]\n"
    "s_waitcnt lgkmcnt(15)\n"
    "s_nop 0\n"
    "v_mfma_f32_16x16x32_bf16 v[0:3], a[0:3], a[192:195], v[0:3]\n"
    "v_mfma_f32_16x16x32_bf16 v[4:7], a[0:3], a[196:199], v[4:7]\n"
    "s_waitcnt vmcnt(8) lgkmcnt(0)\n"
    "s_barrier\n"
    "v_mfma_f32_16x16x32_bf16 v[8:11], a[4:7], a[192:195], v[8:11]\n"
    "ds_read_b128 a[200:203], v184 offset:832\n"
    "ds_read_b128 a[204:207], v184 offset:15680\n"
    "v_mfma_f32_16x16x32_bf16 v[12:15], a[4:7], a[196:199], v[12:15]\n"
    "ds_read_b64_tr_b16 a[64:65], v176 offset:32768\n"
    "ds_read_b64_tr_b16 a[66:67], v176 offset:36864\n"
    "s_mov_b32 s57, 0\n"
    "s_add_u32 m0, s59, 65536\n"
    "v_mfma_f32_16x16x32_bf16 v[16:19], a[8:11], a[192:195], v[16:19]\n"
    "buffer_load_dwordx4 v216, s[44:47], s57 offen offset:0 lds\n"
    "ds_read_b64_tr_b16 a[68:69], v177 offset:32768\n"
    "ds_read_b64_tr_b16 a[70:71], v177 offset:36864\n"
    "v_mfma_f32_16x16x32_bf16 v[20:23], a[8:11], a[196:199], v[20:23]\n"
    "ds_read_b64_tr_b16 a[72:73], v178 offset:32768\n"
    "ds_read_b64_tr_b16 a[74:75], v178 offset:36864\n"
    "v_mfma_f32_16x16x32_bf16 v[24:27], a[12:15], a[192:195], v[24:27]\n"
    "ds_read_b64_tr_b16 a[76:77], v179 offset:32768\n"
    "ds_read_b64_tr_b16 a[78:79], v179 offset:36864\n"
    "v_mfma_f32_16x16x32_bf16 v[28:31], a[12:15], a[196:199], v[28:31]\n"
    "buffer_load_dwordx4 v217, s[44:47], s57 offen offset:1024 lds\n"
    "ds_read_b64_tr_b16 a[80:81], v180 offset:32768\n"
    "ds_read_b64_tr_b16 a[82:83], v180 offset:36864\n"
    "v_mfma_f32_16x16x32_bf16 v[32:35], a[16:19], a[192:195], v[32:35]\n"
    "ds_read_b64_tr_b16 a[84:85], v181 offset:32768\n"
    "ds_read_b64_tr_b16 a[86:87], v181 offset:36864\n"
    "v_mfma_f32_16x16x32_bf16 v[36:39], a[16:19], a[196:199], v[36:39]\n"
    "ds_read_b64_tr_b16 a[88:89], v182 offset:32768\n"
    "ds_read_b64_tr_b16 a[90:91], v182 offset:36864\n"
    "v_mfma_f32_16x16x32_bf16 v[40:43], a[20:23], a[192:195], v[40:43]\n"
    "buffer_load_dwordx4 v218, s[44:47], s57 offen offset:2048 lds\n"
    "ds_read_b64_tr_b16 a[92:93], v183 offset:32768\n"
    "v_mfma_f32_16x16x32_bf16 v[44:47], a[20:23], a[196:199], v[44:47]\n"
    "ds_read_b64_tr_b16 a[94:95], v183 offset:36864\n"
    "ds_read_b64_tr_b16 a[96:97], v176 offset:33024\n"
    "v_mfma_f32_16x16x32_bf16 v[48:51], a[24:27], a[192:195], v[48:51]\n"
    "ds_read_b64_tr_b16 a[98:99], v176 offset:37120\n"
    "ds_read_b64_tr_b16 a[100:101], v177 offset:33024\n"
    "v_mfma_f32_16x16x32_bf16 v[52:55], a[24:27], a[196:199], v[52:55]\n"
    "buffer_load_dwordx4 v219, s[44:47], s57 offen offset:3072 lds\n"
    "ds_read_b64_tr_b16 a[102:103], v177 offset:37120\n"
    "v_mfma_f32_16x16x32_bf16 v[56:59], a[28:31], a[192:195], v[56:59]\n"
    "ds_read_b64_tr_b16 a[104:105], v178 offset:33024\n"
    "ds_read_b64_tr_b16 a[106:107], v178 offset:37120\n"
    "v_mfma_f32_16x16x32_bf16 v[60:63], a[28:31], a[196:199], v[60:63]\n"
    "ds_read_b64_tr_b16 a[108:109], v179 offset:33024\n"
    "ds_read_b64_tr_b16 a[110:111], v179 offset:37120\n"
    "v_mfma_f32_16x16x32_bf16 v[64:67], a[32:35], a[192:195], v[64:67]\n"
    "ds_read_b64_tr_b16 a[112:113], v180 offset:33024\n"
    "s_mov_b32 s57, 16384\n"
    "s_add_u32 m0, s59, 81920\n"
    "v_mfma_f32_16x16x32_bf16 v[68:71], a[32:35], a[196:199], v[68:71]\n"
    "buffer_load_dwordx4 v216, s[44:47], s57 offen offset:0 lds\n"
    "ds_read_b64_tr_b16 a[114:115], v180 offset:37120\n"
    "v_mfma_f32_16x16x32_bf16 v[72:75], a[36:39], a[192:195], v[72:75]\n"
    "ds_read_b64_tr_b16 a[116:117], v181 offset:33024\n"
    "ds_read_b64_tr_b16 a[118:119], v181 offset:37120\n"
    "v_mfma_f32_16x16x32_bf16 v[76:79], a[36:39], a[196:199], v[76:79]\n"
    "ds_read_b64_tr_b16 a[120:121], v182 offset:33024\n"
    "ds_read_b64_tr_b16 a[122:123], v182 offset:37120\n"
    "v_mfma_f32_16x16x32_bf16 v[80:83], a[40:43], a[192:195], v[80:83]\n"
    "buffer_load_dwordx4 v217, s[44:47], s57 offen offset:1024 lds\n"
    "ds_read_b64_tr_b16 a[124:125], v183 offset:33024\n"
    "v_mfma_f32_16x16x32_bf16 v[84:87], a[40:43], a[196:199], v[84:87]\n"
    "ds_read_b64_tr_b16 a[126:127], v183 offset:37120\n"
    "v_mfma_f32_16x16x32_bf16 v[88:91], a[44:47], a[192:195], v[88:91]\n"
    "v_mfma_f32_16x16x32_bf16 v[92:95], a[44:47], a[196:199], v[92:95]\n"
    "buffer_load_dwordx4 v218, s[44:47], s57 offen offset:2048 lds\n"
    "v_mfma_f32_16x16x32_bf16 v[96:99], a[48:51], a[192:195], v[96:99]\n"
    "v_mfma_f32_16x16x32_bf16 v[100:103], a[48:51], a[196:199], v[100:103]\n"
    "v_mfma_f32_16x16x32_bf16 v[104:107], a[52:55], a[192:195], v[104:107]\n"
    "buffer_load_dwordx4 v219, s[44:47], s57 offen offset:3072 lds\n"
    "v_mfma_f32_16x16x32_bf16 v[108:111], a[52:55], a[196:199], v[108:111]\n"
    "v_mfma_f32_16x16x32_bf16 v[112:115], a[56:59], a[192:195], v[112:115]\n"
    "v_mfma_f32_16x16x32_bf16 v[116:119], a[56:59], a[196:199], v[116:119]\n"
    "v_mfma_f32_16x16x32_bf16 v[120:123], a[60:63], a[192:195], v[120:123]\n"
    "v_mfma_f32_16x16x32_bf16 v[124:127], a[60:63], a[196:199], v[124:127]\n"
    "s_waitcnt lgkmcnt(15)\n"
    "v_mfma_f32_16x16x32_bf16 v[0:3], a[64:67], a[200:203], v[0:3]\n"
    "v_mfma_f32_16x16x32_bf16 v[4:7], a[64:67], a[204:207], v[4:7]\n"
    "s_waitcnt vmcnt(8) lgkmcnt(0)\n"
    "s_barrier\n"
    "v_mfma_f32_16x16x32_bf16 v[8:11], a[68:71], a[200:203], v[8:11]\n"
    "ds_read_b128 a[0:3], v200 offset:0\n"
    "ds_read_b128 a[4:7], v200 offset:8192\n"
    "v_mfma_f32_16x16x32_bf16 v[12:15], a[68:71], a[204:207], v[12:15]\n"
    "ds_read_b128 a[8:11], v201 offset:0\n"
    "s_mov_b32 s56, 32768\n"
    "s_add_u32 m0, s59, 32768\n"
    "v_mfma_f32_16x16x32_bf16 v[16:19], a[72:75], a[200:203], v[16:19]\n"
    "buffer_load_dwordx4 v212, s[40:43], s56 offen offset:0 lds\n"
    "ds_read_b128 a[12:15], v201 offset:8192\n"
    "v_mfma_f32_16x16x32_bf16 v[20:23], a[72:75], a[204:207], v[20:23]\n"
    "ds_read_b128 a[16:19], v202 offset:0\n"
    "ds_read_b128 a[20:23], v202 offset:8192\n"
    "v_mfma_f32_16x16x32_bf16 v[24:27], a[76:79], a[200:203], v[24:27]\n"
    "ds_read_b128 a[24:27], v203 offset:0\n"
    "ds_read_b128 a[28:31], v203 offset:8192\n"
    "v_cvt_pk_bf16_f32 v0, v0, v1\n"
    "v_cvt_pk_bf16_f32 v1, v2, v3\n"
    "v_cvt_pk_bf16_f32 v8, v8, v9\n"
    "v_mfma_f32_16x16x32_bf16 v[28:31], a[76:79], a[204:207], v[28:31]\n"
    "buffer_load_dwordx4 v213, s[40:43], s56 offen offset:1024 lds\n"
    "ds_read_b128 a[32:35], v200 offset:256\n"
    "v_cvt_pk_bf16_f32 v9, v10, v11\n"
    "v_cvt_pk_bf16_f32 v4, v4, v5\n"
    "v_cvt_pk_bf16_f32 v5, v6, v7\n"
    "v_mfma_f32_16x16x32_bf16 v[32:35], a[80:83], a[200:203], v[32:35]\n"
    "ds_read_b128 a[36:39], v200 offset:8448\n"
    "ds_read_b128 a[40:43], v201 offset:256\n"
    "v_cvt_pk_bf16_f32 v12, v12, v13\n"
    "v_cvt_pk_bf16_f32 v13, v14, v15\n"
    "v_permlane16_swap_b32 v0, v8\n"
    "v_mfma_f32_16x16x32_bf16 v[36:39], a[80:83], a[204:207], v[36:39]\n"
    "ds_read_b128 a[44:47], v201 offset:8448\n"
    "ds_read_b128 a[48:51], v202 offset:256\n"
    "v_permlane16_swap_b32 v1, v9\n"
    "v_permlane16_swap_b32 v4, v12\n"
    "v_permlane16_swap_b32 v5, v13\n"
    "v_mfma_f32_16x16x32_bf16 v[40:43], a[84:87], a[200:203], v[40:43]\n"
    "buffer_load_dwordx4 v214, s[40:43], s56 offen offset:2048 lds\n"
    "ds_read_b128 a[52:55], v202 offset:8448\n"
    "v_accvgpr_write_b32 a128, v0\n"
    "v_accvgpr_write_b32 a129, v1\n"
    "v_accvgpr_write_b32 a130, v8\n"
    "v_mfma_f32_16x16x32_bf16 v[44:47], a[84:87], a[204:207], v[44:47]\n"
    "ds_read_b128 a[56:59], v203 offset:256\n"
    "ds_read_b128 a[60:63], v203 offset:8448\n"
    "v_accvgpr_write_b32 a131, v9\n"
    "v_accvgpr_write_b32 a132, v4\n"
    "v_accvgpr_write_b32 a133, v5\n"
    "v_mfma_f32_16x16x32_bf16 v[48:51], a[88:91], a[200:203], v[48:51]\n"
    "v_accvgpr_write_b32 a134, v12\n"
    "v_accvgpr_write_b32 a135, v13\n"
    "v_cvt_pk_bf16_f32 v16, v16, v17\n"
    "v_mfma_f32_16x16x32_bf16 v[52:55], a[88:91], a[204:207], v[52:55]\n"
    "buffer_load_dwordx4 v215, s[40:43], s56 offen offset:3072 lds\n"
    "v_cvt_pk_bf16_f32 v17, v18, v19\n"
    "v_cvt_pk_bf16_f32 v24, v24, v25\n"
    "v_cvt_pk_bf16_f32 v25, v26, v27\n"
    "v_mfma_f32_16x16x32_bf16 v[56:59], a[92:95], a[200:203], v[56:59]\n"
    "v_cvt_pk_bf16_f32 v20, v20, v21\n"
    "v_cvt_pk_bf16_f32 v21, v22, v23\n"
    "v_cvt_pk_bf16_f32 v28, v28, v29\n"
    "v_mfma_f32_16x16x32_bf16 v[60:63], a[92:95], a[204:207], v[60:63]\n"
    "v_cvt_pk_bf16_f32 v29, v30, v31\n"
    "v_permlane16_swap_b32 v16, v24\n"
    "v_permlane16_swap_b32 v17, v25\n"
    "v_mfma_f32_16x16x32_bf16 v[64:67], a[96:99], a[200:203], v[64:67]\n"
    "v_permlane16_swap_b32 v20, v28\n"
    "v_permlane16_swap_b32 v21, v29\n"
    "v_accvgpr_write_b32 a136, v16\n"
    "s_mov_b32 s56, 49152\n"
    "s_add_u32 m0, s59, 49152\n"
    "v_mfma_f32_16x16x32_bf16 v[68:71], a[96:99], a[204:207], v[68:71]\n"
    "buffer_load_dwordx4 v212, s[40:43], s56 offen offset:0 lds\n"
    "v_accvgpr_write_b32 a137, v17\n"
    "v_accvgpr_write_b32 a138, v24\n"
    "v_accvgpr_write_b32 a139, v25\n"
    "v_mfma_f32_16x16x32_bf16 v[72:75], a[100:103], a[200:203], v[72:75]\n"
    "v_accvgpr_write_b32 a140, v20\n"
    "v_accvgpr_write_b32 a141, v21\n"
    "v_accvgpr_write_b32 a142, v28\n"
    "v_mfma_f32_16x16x32_bf16 v[76:79], a[100:103], a[204:207], v[76:79]\n"
    "v_accvgpr_write_b32 a143, v29\n"
    "v_cvt_pk_bf16_f32 v32, v32, v33\n"
    "v_cvt_pk_bf16_f32 v33, v34, v35\n"
    "v_mfma_f32_16x16x32_bf16 v[80:83], a[104:107], a[200:203], v[80:83]\n"
    "buffer_load_dwordx4 v213, s[40:43], s56 offen offset:1024 lds\n"
    "v_cvt_pk_bf16_f32 v40, v40, v41\n"
    "v_cvt_pk_bf16_f32 v41, v42, v43\n"
    "v_cvt_pk_bf16_f32 v36, v36, v37\n"
    "v_mfma_f32_16x16x32_bf16 v[84:87], a[104:107], a[204:207], v[84:87]\n"
    "v_cvt_pk_bf16_f32 v37, v38, v39\n"
    "v_cvt_pk_bf16_f32 v44, v44, v45\n"
    "v_cvt_pk_bf16_f32 v45, v46, v47\n"
    "v_mfma_f32_16x16x32_bf16 v[88:91], a[108:111], a[200:203], v[88:91]\n"
    "v_permlane16_swap_b32 v32, v40\n"
    "v_permlane16_swap_b32 v33, v41\n"
    "v_permlane16_swap_b32 v36, v44\n"
    "v_mfma_f32_16x16x32_bf16 v[92:95], a[108:111], a[204:207], v[92:95]\n"
    "buffer_load_dwordx4 v214, s[40:43], s56 offen offset:2048 lds\n"
    "v_permlane16_swap_b32 v37, v45\n"
    "v_accvgpr_write_b32 a144, v32\n"
    "v_accvgpr_write_b32 a145, v33\n"
    "v_mfma_f32_16x16x32_bf16 v[96:99], a[112:115], a[200:203], v[96:99]\n"
    "v_accvgpr_write_b32 a146, v40\n"
    "v_accvgpr_write_b32 a147, v41\n"
    "v_accvgpr_write_b32 a148, v36\n"
    "v_mfma_f32_16x16x32_bf16 v[100:103], a[112:115], a[204:207], v[100:103]\n"
    "v_accvgpr_write_b32 a149, v37\n"
    "v_accvgpr_write_b32 a150, v44\n"
    "v_accvgpr_write_b32 a151, v45\n"
    "v_mfma_f32_16x16x32_bf16 v[104:107], a[116:119], a[200:203], v[104:107]\n"
    "buffer_load_dwordx4 v215, s[40:43], s56 offen offset:3072 lds\n"
    "v_cvt_pk_bf16_f32 v48, v48, v49\n"
    "v_cvt_pk_bf16_f32 v49, v50, v51\n"
    "v_cvt_pk_bf16_f32 v56, v56, v57\n"
    "v_mfma_f32_16x16x32_bf16 v[108:111], a[116:119], a[204:207], v[108:111]\n"
    "v_cvt_pk_bf16_f32 v57, v58, v59\n"
    "v_cvt_pk_bf16_f32 v52, v52, v53\n"
    "v_cvt_pk_bf16_f32 v53, v54, v55\n"
    "v_mfma_f32_16x16x32_bf16 v[112:115], a[120:123], a[200:203], v[112:115]\n"
    "v_cvt_pk_bf16_f32 v60, v60, v61\n"
    "v_cvt_pk_bf16_f32 v61, v62, v63\n"
    "v_permlane16_swap_b32 v48, v56\n"
    "v_mfma_f32_16x16x32_bf16 v[116:119], a[120:123], a[204:207], v[116:119]\n"
    "v_permlane16_swap_b32 v49, v57\n"
    "v_permlane16_swap_b32 v52, v60\n"
    "v_permlane16_swap_b32 v53, v61\n"
    "v_mfma_f32_16x16x32_bf16 v[120:123], a[124:127], a[200:203], v[120:123]\n"
    "v_accvgpr_write_b32 a152, v48\n"
    "v_accvgpr_write_b32 a153, v49\n"
    "v_accvgpr_write_b32 a154, v56\n"
    "v_mfma_f32_16x16x32_bf16 v[124:127], a[124:127], a[204:207], v[124:127]\n"
    "v_accvgpr_write_b32 a155, v57\n"
    "v_accvgpr_write_b32 a156, v52\n"
    "v_accvgpr_write_b32 a157, v53\n"
    "s_waitcnt lgkmcnt(0)\n"
    "s_nop 0\n"
    "s_mov_b32 s57, 32768\n"
    "s_mov_b32 s56, 65536\n"
    "s_sub_u32 s74, s80, 128\n"
    "s_add_u32 s75, s80, 114560\n"
    "v_mfma_f32_16x16x32_bf16 v[128:131], a[0:3], a[128:131], 0\n"
    "v_accvgpr_write_b32 a158, v60\n"
    "v_accvgpr_write_b32 a159, v61\n"
    "v_cvt_pk_bf16_f32 v64, v64, v65\n"
    "v_mfma_f32_16x16x32_bf16 v[136:139], a[0:3], a[132:135], 0\n"
    "s_waitcnt vmcnt(12)\n"
    "s_barrier\n"
    "v_cvt_pk_bf16_f32 v65, v66, v67\n"
    "v_cvt_pk_bf16_f32 v72, v72, v73\n"
    "v_cvt_pk_bf16_f32 v73, v74, v75\n"
    "v_mfma_f32_16x16x32_bf16 v[132:135], a[4:7], a[128:131], 0\n"
    "ds_read_b128 a[0:3], v200 offset:16384\n"
    "v_cvt_pk_bf16_f32 v68, v68, v69\n"
    "v_cvt_pk_bf16_f32 v69, v70, v71\n"
    "v_cvt_pk_bf16_f32 v76, v76, v77\n"
    "s_add_u32 m0, s59, 0x20000\n"
    "v_mfma_f32_16x16x32_bf16 v[140:143], a[4:7], a[132:135], 0\n"
    "buffer_load_dwordx4 v240, s[52:55], s74 offen offset:0 lds\n"
    "v_cvt_pk_bf16_f32 v77, v78, v79\n"
    "v_permlane16_swap_b32 v64, v72\n"
    "v_permlane16_swap_b32 v65, v73\n"
    "v_mfma_f32_16x16x32_bf16 v[128:131], a[8:11], a[136:139], v[128:131]\n"
    "ds_read_b128 a[4:7], v200 offset:24576\n"
    "buffer_load_dwordx4 v241, s[52:55], s74 offen offset:1024 lds\n"
    "v_permlane16_swap_b32 v68, v76\n"
    "v_permlane16_swap_b32 v69, v77\n"
    "v_accvgpr_write_b32 a160, v64\n"
    "v_mfma_f32_16x16x32_bf16 v[136:139], a[8:11], a[140:143], v[136:139]\n"
    "buffer_load_dwordx4 v242, s[52:55], s74 offen offset:2048 lds\n"
    "v_accvgpr_write_b32 a161, v65\n"
    "v_accvgpr_write_b32 a162, v72\n"
    "v_accvgpr_write_b32 a163, v73\n"
    "v_mfma_f32_16x16x32_bf16 v[132:135], a[12:15], a[136:139], v[132:135]\n"
    "ds_read_b128 a[8:11], v201 offset:16384\n"
    "buffer_load_dwordx4 v243, s[52:55], s74 offen offset:3072 lds\n"
    "v_accvgpr_write_b32 a164, v68\n"
    "v_accvgpr_write_b32 a165, v69\n"
    "v_accvgpr_write_b32 a166, v76\n"
    "v_mfma_f32_16x16x32_bf16 v[140:143], a[12:15], a[140:143], v[140:143]\n"
    "v_accvgpr_write_b32 a167, v77\n"
    "v_cvt_pk_bf16_f32 v80, v80, v81\n"
    "v_cvt_pk_bf16_f32 v81, v82, v83\n"
    "s_add_u32 m0, m0, 0x4000\n"
    "v_mfma_f32_16x16x32_bf16 v[128:131], a[16:19], a[144:147], v[128:131]\n"
    "ds_read_b128 a[12:15], v201 offset:24576\n"
    "buffer_load_dwordx4 v240, s[52:55], s75 offen offset:0 lds\n"
    "v_cvt_pk_bf16_f32 v88, v88, v89\n"
    "v_cvt_pk_bf16_f32 v89, v90, v91\n"
    "v_cvt_pk_bf16_f32 v84, v84, v85\n"
    "v_mfma_f32_16x16x32_bf16 v[136:139], a[16:19], a[148:151], v[136:139]\n"
    "buffer_load_dwordx4 v241, s[52:55], s75 offen offset:1024 lds\n"
    "v_cvt_pk_bf16_f32 v85, v86, v87\n"
    "v_cvt_pk_bf16_f32 v92, v92, v93\n"
    "v_cvt_pk_bf16_f32 v93, v94, v95\n"
    "v_mfma_f32_16x16x32_bf16 v[132:135], a[20:23], a[144:147], v[132:135]\n"
    "ds_read_b128 a[16:19], v202 offset:16384\n"
    "buffer_load_dwordx4 v242, s[52:55], s75 offen offset:2048 lds\n"
    "v_permlane16_swap_b32 v80, v88\n"
    "v_permlane16_swap_b32 v81, v89\n"
    "v_permlane16_swap_b32 v84, v92\n"
    "v_mfma_f32_16x16x32_bf16 v[140:143], a[20:23], a[148:151], v[140:143]\n"
    "buffer_load_dwordx4 v243, s[52:55], s75 offen offset:3072 lds\n"
    "v_permlane16_swap_b32 v85, v93\n"
    "v_accvgpr_write_b32 a168, v80\n"
    "v_accvgpr_write_b32 a169, v81\n"
    "v_mfma_f32_16x16x32_bf16 v[128:131], a[24:27], a[152:155], v[128:131]\n"
    "ds_read_b128 a[20:23], v202 offset:24576\n"
    "v_accvgpr_write_b32 a170, v88\n"
    "v_accvgpr_write_b32 a171, v89\n"
    "v_accvgpr_write_b32 a172, v84\n"
    "v_mfma_f32_16x16x32_bf16 v[136:139], a[24:27], a[156:159], v[136:139]\n"
    "v_accvgpr_write_b32 a173, v85\n"
    "v_accvgpr_write_b32 a174, v92\n"
    "v_accvgpr_write_b32 a175, v93\n"
    "s_add_u32 m0, s59, 98304\n"
    "v_mfma_f32_16x16x32_bf16 v[132:135], a[28:31], a[152:155], v[132:135]\n"
    "ds_read_b128 a[24:27], v203 offset:16384\n"
    "buffer_load_dwordx4 v216, s[44:47], s57 offen offset:0 lds\n"
    "v_cvt_pk_bf16_f32 v96, v96, v97\n"
    "v_cvt_pk_bf16_f32 v97, v98, v99\n"
    "v_cvt_pk_bf16_f32 v104, v104, v105\n"
    "v_mfma_f32_16x16x32_bf16 v[140:143], a[28:31], a[156:159], v[140:143]\n"
    "v_cvt_pk_bf16_f32 v105, v106, v107\n"
    "v_cvt_pk_bf16_f32 v100, v100, v101\n"
    "v_cvt_pk_bf16_f32 v101, v102, v103\n"
    "v_mfma_f32_16x16x32_bf16 v[128:131], a[32:35], a[160:163], v[128:131]\n"
    "ds_read_b128 a[28:31], v203 offset:24576\n"
    "buffer_load_dwordx4 v217, s[44:47], s57 offen offset:1024 lds\n"
    "v_cvt_pk_bf16_f32 v108, v108, v109\n"
    "v_cvt_pk_bf16_f32 v109, v110, v111\n"
    "v_permlane16_swap_b32 v96, v104\n"
    "v_mfma_f32_16x16x32_bf16 v[136:139], a[32:35], a[164:167], v[136:139]\n"
    "v_permlane16_swap_b32 v97, v105\n"
    "v_permlane16_swap_b32 v100, v108\n"
    "v_permlane16_swap_b32 v101, v109\n"
    "v_mfma_f32_16x16x32_bf16 v[132:135], a[36:39], a[160:163], v[132:135]\n"
    "ds_read_b128 a[32:35], v200 offset:16640\n"
    "buffer_load_dwordx4 v218, s[44:47], s57 offen offset:2048 lds\n"
    "v_accvgpr_write_b32 a176, v96\n"
    "v_accvgpr_write_b32 a177, v97\n"
    "v_accvgpr_write_b32 a178, v104\n"
    "v_mfma_f32_16x16x32_bf16 v[140:143], a[36:39], a[164:167], v[140:143]\n"
    "v_accvgpr_write_b32 a179, v105\n"
    "v_accvgpr_write_b32 a180, v100\n"
    "v_accvgpr_write_b32 a181, v101\n"
    "v_mfma_f32_16x16x32_bf16 v[128:131], a[40:43], a[168:171], v[128:131]\n"
    "ds_read_b128 a[36:39], v200 offset:24832\n"
    "buffer_load_dwordx4 v219, s[44:47], s57 offen offset:3072 lds\n"
    "v_accvgpr_write_b32 a182, v108\n"
    "v_accvgpr_write_b32 a183, v109\n"
    "v_cvt_pk_bf16_f32 v112, v112, v113\n"
    "v_mfma_f32_16x16x32_bf16 v[136:139], a[40:43], a[172:175], v[136:139]\n"
    "v_cvt_pk_bf16_f32 v113, v114, v115\n"
    "v_cvt_pk_bf16_f32 v120, v120, v121\n"
    "v_cvt_pk_bf16_f32 v121, v122, v123\n"
    "v_mfma_f32_16x16x32_bf16 v[132:135], a[44:47], a[168:171], v[132:135]\n"
    "ds_read_b128 a[40:43], v201 offset:16640\n"
    "v_cvt_pk_bf16_f32 v116, v116, v117\n"
    "v_cvt_pk_bf16_f32 v117, v118, v119\n"
    "v_cvt_pk_bf16_f32 v124, v124, v125\n"
    "s_add_u32 m0, s59, 0\n"
    "v_mfma_f32_16x16x32_bf16 v[140:143], a[44:47], a[172:175], v[140:143]\n"
    "buffer_load_dwordx4 v212, s[40:43], s56 offen offset:0 lds\n"
    "v_cvt_pk_bf16_f32 v125, v126, v127\n"
    "v_permlane16_swap_b32 v112, v120\n"
    "v_permlane16_swap_b32 v113, v121\n"
    "v_mfma_f32_16x16x32_bf16 v[128:131], a[48:51], a[176:179], v[128:131]\n"
    "ds_read_b128 a[44:47], v201 offset:24832\n"
    "v_permlane16_swap_b32 v116, v124\n"
    "v_permlane16_swap_b32 v117, v125\n"
    "v_accvgpr_write_b32 a184, v112\n"
    "v_mfma_f32_16x16x32_bf16 v[136:139], a[48:51], a[180:183], v[136:139]\n"
    "buffer_load_dwordx4 v213, s[40:43], s56 offen offset:1024 lds\n"
    "v_accvgpr_write_b32 a185, v113\n"
    "v_accvgpr_write_b32 a186, v120\n"
    "v_accvgpr_write_b32 a187, v121\n"
    "v_mfma_f32_16x16x32_bf16 v[132:135], a[52:55], a[176:179], v[132:135]\n"
    "ds_read_b128 a[48:51], v202 offset:16640\n"
    "v_accvgpr_write_b32 a188, v116\n"
    "v_accvgpr_write_b32 a189, v117\n"
    "v_accvgpr_write_b32 a190, v124\n"
    "v_mfma_f32_16x16x32_bf16 v[140:143], a[52:55], a[180:183], v[140:143]\n"
    "buffer_load_dwordx4 v214, s[40:43], s56 offen offset:2048 lds\n"
    "v_accvgpr_write_b32 a191, v125\n"
    "v_mov_b64 v[0:1], 0\n"
    "v_mov_b64_e64 v[2:3], 0\n"
    "v_mfma_f32_16x16x32_bf16 v[128:131], a[56:59], a[184:187], v[128:131]\n"
    "ds_read_b128 a[52:55], v202 offset:24832\n"
    "v_mov_b64 v[4:5], 0\n"
    "v_mov_b64 v[6:7], 0\n"
    "v_mov_b64_e64 v[8:9], 0\n"
    "v_mfma_f32_16x16x32_bf16 v[136:139], a[56:59], a[188:191], v[136:139]\n"
    "buffer_load_dwordx4 v215, s[40:43], s56 offen offset:3072 lds\n"
    "v_mov_b64 v[10:11], 0\n"
    "v_mov_b64 v[12:13], 0\n"
    "v_mov_b64_e64 v[14:15], 0\n"
    "v_mfma_f32_16x16x32_bf16 v[132:135], a[60:63], a[184:187], v[132:135]\n"
    "ds_read_b128 a[56:59], v203 offset:16640\n"
    "v_mov_b64 v[16:17], 0\n"
    "v_mov_b64 v[18:19], 0\n"
    "v_mov_b64 v[20:21], 0\n"
    "s_add_u32 s57, s57, s77\n"
    "v_mfma_f32_16x16x32_bf16 v[140:143], a[60:63], a[188:191], v[140:143]\n"
    "v_mov_b64 v[22:23], 0\n"
    "v_mov_b64 v[24:25], 0\n"
    "v_mov_b64_e64 v[26:27], 0\n"
    "s_add_u32 s56, s56, s77\n"
    "s_waitcnt lgkmcnt(15)\n"
    "buffer_load_dword v252, v253, s[52:55], 0 offen\n"
    "v_mov_b64 v[28:29], 0\n"
    "v_mov_b64 v[30:31], 0\n"
    "v_mov_b64 v[32:33], 0\n"
    "v_mov_b64 v[34:35], 0\n"
    "v_mov_b64 v[36:37], 0\n"
    "v_mov_b64 v[38:39], 0\n"
    "v_mov_b64 v[40:41], 0\n"
    "v_mov_b64 v[42:43], 0\n"
    "s_nop 7\n"
    "v_max3_f32 v225, v128, v129, v130\n"
    "v_max3_f32 v227, v131, v132, v133\n"
    "v_max3_f32 v225, v225, v134, v135\n"
    "v_max_f32 v225, v225, v227\n"
    "v_max3_f32 v226, v136, v137, v138\n"
    "v_max3_f32 v228, v139, v140, v141\n"
    "v_max3_f32 v226, v226, v142, v143\n"
    "v_max_f32 v226, v226, v228\n"
    "v_mov_b32 v227, v225\n"
    "v_mov_b32 v228, v226\n"
    "s_nop 1\n"
    "v_permlane16_swap_b32 v225, v227\n"
    "v_permlane16_swap_b32 v226, v228\n"
    "v_max_f32 v225, v225, v227\n"
    "v_max_f32 v226, v226, v228\n"
    "v_mov_b32 v227, v225\n"
    "v_mov_b32 v228, v226\n"
    "s_nop 1\n"
    "v_permlane32_swap_b32 v225, v227\n"
    "v_permlane32_swap_b32 v226, v228\n"
    "v_max_f32 v225, v225, v227\n"
    "v_max_f32 v226, v226, v228\n"
    "v_add_f32 v225, s61, v225\n"
    "v_add_f32 v226, s61, v226\n"
    "v_sub_f32 v128, v128, v225\n"
    "v_sub_f32 v129, v129, v225\n"
    "v_sub_f32 v130, v130, v225\n"
    "v_sub_f32 v131, v131, v225\n"
    "v_sub_f32 v132, v132, v225\n"
    "v_sub_f32 v133, v133, v225\n"
    "v_sub_f32 v134, v134, v225\n"
    "v_sub_f32 v135, v135, v225\n"
    "v_sub_f32 v160, 0, v225\n"
    "v_sub_f32 v161, 0, v225\n"
    "v_sub_f32 v162, 0, v225\n"
    "v_sub_f32 v163, 0, v225\n"
    "v_sub_f32 v136, v136, v226\n"
    "v_sub_f32 v137, v137, v226\n"
    "v_sub_f32 v138, v138, v226\n"
    "v_sub_f32 v139, v139, v226\n"
    "v_sub_f32 v140, v140, v226\n"
    "v_sub_f32 v141, v141, v226\n"
    "v_sub_f32 v142, v142, v226\n"
    "v_sub_f32 v143, v143, v226\n"
    "v_sub_f32 v164, 0, v226\n"
    "v_sub_f32 v165, 0, v226\n"
    "v_sub_f32 v166, 0, v226\n"
    "v_sub_f32 v167, 0, v226\n"
    "v_mov_b64 v[44:45], 0\n"
    "v_mov_b64 v[46:47], 0\n"
    "v_mov_b64 v[48:49], 0\n"
    "v_mov_b64 v[50:51], 0\n"
    "v_mov_b64 v[52:53], 0\n"
    "v_mov_b64 v[54:55], 0\n"
    "v_mov_b64 v[56:57], 0\n"
    "v_mov_b64 v[58:59], 0\n"
    "v_mov_b64 v[60:61], 0\n"
    "v_mov_b64 v[62:63], 0\n"
    "v_mov_b64 v[64:65], 0\n"
    "v_mov_b64 v[66:67], 0\n"
    "v_mov_b64 v[68:69], 0\n"
    "v_mov_b64 v[70:71], 0\n"
    "v_mov_b64 v[72:73], 0\n"
    "v_mov_b64 v[74:75], 0\n"
    "v_mov_b64 v[76:77], 0\n"
    "v_mov_b64 v[78:79], 0\n"
    "v_mov_b64 v[80:81], 0\n"
    "v_mov_b64 v[82:83], 0\n"
    "v_mov_b64 v[84:85], 0\n"
    "v_mov_b64 v[86:87], 0\n"
    "v_mov_b64 v[88:89], 0\n"
    "v_mov_b64 v[90:91], 0\n"
    "v_mov_b64 v[92:93], 0\n"
    "v_mov_b64 v[94:95], 0\n"
    "v_mov_b64 v[96:97], 0\n"
    "v_mov_b64 v[98:99], 0\n"
    "v_mov_b64 v[100:101], 0\n"
    "v_mov_b64 v[102:103], 0\n"
    "v_mov_b64 v[104:105], 0\n"
    "v_mov_b64 v[106:107], 0\n"
    "v_mov_b64 v[108:109], 0\n"
    "v_mov_b64 v[110:111], 0\n"
    "v_mov_b64 v[112:113], 0\n"
    "v_mov_b64 v[114:115], 0\n"
    "v_mov_b64 v[116:117], 0\n"
    "v_mov_b64 v[118:119], 0\n"
    "v_mov_b64 v[120:121], 0\n"
    "v_mov_b64 v[122:123], 0\n"
    "v_mov_b64 v[124:125], 0\n"
    "v_mov_b64 v[126:127], 0\n"
    "ds_read_b128 a[60:63], v203 offset:24832\n"
    "s_waitcnt lgkmcnt(14)\n"
    "ds_read_b64_tr_b16 a[64:65], v204 offset:0\n"
    "ds_read_b64_tr_b16 a[66:67], v204 offset:8192\n"
    "ds_read_b64_tr_b16 a[68:69], v205 offset:0\n"
    "ds_read_b64_tr_b16 a[70:71], v205 offset:8192\n"
    "v_exp_f32 v168, v128\n"
    "ds_read_b64_tr_b16 a[72:73], v206 offset:0\n"
    "ds_read_b64_tr_b16 a[74:75], v206 offset:8192\n"
    "v_exp_f32_e64 v169, v129\n"
    "ds_read_b64_tr_b16 a[76:77], v207 offset:0\n"
    "ds_read_b64_tr_b16 a[78:79], v207 offset:8192\n"
    "v_exp_f32_e64 v170, v130\n"
    "ds_read_b64_tr_b16 a[80:81], v208 offset:0\n"
    "ds_read_b64_tr_b16 a[82:83], v208 offset:8192\n"
    "v_exp_f32_e64 v171, v131\n"
    "ds_read_b64_tr_b16 a[84:85], v209 offset:0\n"
    "ds_read_b64_tr_b16 a[86:87], v209 offset:8192\n"
    "v_exp_f32_e64 v172, v132\n"
    "ds_read_b64_tr_b16 a[88:89], v210 offset:0\n"
    "ds_read_b64_tr_b16 a[90:91], v210 offset:8192\n"
    "v_exp_f32_e64 v173, v133\n"
    "ds_read_b64_tr_b16 a[92:93], v211 offset:0\n"
    "ds_read_b64_tr_b16 a[94:95], v211 offset:8192\n"
    "v_exp_f32_e64 v174, v134\n"
    "ds_read_b64_tr_b16 a[96:97], v204 offset:256\n"
    "ds_read_b64_tr_b16 a[98:99], v204 offset:8448\n"
    "v_exp_f32_e64 v175, v135\n"
    "ds_read_b64_tr_b16 a[100:101], v205 offset:256\n"
    "ds_read_b64_tr_b16 a[102:103], v205 offset:8448\n"
    "v_exp_f32_e64 v176, v136\n"
    "ds_read_b64_tr_b16 a[104:105], v206 offset:256\n"
    "ds_read_b64_tr_b16 a[106:107], v206 offset:8448\n"
    "v_exp_f32_e64 v177, v137\n"
    "ds_read_b64_tr_b16 a[108:109], v207 offset:256\n"
    "ds_read_b64_tr_b16 a[110:111], v207 offset:8448\n"
    "v_exp_f32_e64 v178, v138\n"
    "ds_read_b64_tr_b16 a[112:113], v208 offset:256\n"
    "ds_read_b64_tr_b16 a[114:115], v208 offset:8448\n"
    "v_exp_f32_e64 v179, v139\n"
    "ds_read_b64_tr_b16 a[116:117], v209 offset:256\n"
    "ds_read_b64_tr_b16 a[118:119], v209 offset:8448\n"
    "v_exp_f32_e64 v180, v140\n"
    "ds_read_b64_tr_b16 a[120:121], v210 offset:256\n"
    "ds_read_b64_tr_b16 a[122:123], v210 offset:8448\n"
    "v_add_f32 v222, v168, v169\n"
    "s_waitcnt lgkmcnt(15)\n"
    "s_mov_b32 s58, 15\n"
    "L_loop:\n"
    "v_mfma_f32_16x16x32_bf16 v[144:147], a[0:3], a[128:131], v[160:163]\n"
    "ds_read_b64_tr_b16 a[124:125], v211 offset:256\n"
    "ds_read_b64_tr_b16 a[126:127], v211 offset:8448\n"
    "v_mfma_f32_16x16x32_bf16 v[152:155], a[0:3], a[132:135], v[164:167]\n"
    "s_waitcnt vmcnt(12)\n"
    "s_barrier\n"
    "v_mfma_f32_16x16x32_bf16 v[148:151], a[4:7], a[128:131], v[160:163]\n"
    "ds_read_b128 a[0:3], v200 offset:32768\n"
    "v_cvt_pk_bf16_f32 v184, v168, v169\n"
    "v_mfma_f32_16x16x32_bf16 v[156:159], a[4:7], a[132:135], v[164:167]\n"
    "v_exp_f32_e64 v181, v141\n"
    "v_mfma_f32_16x16x32_bf16 v[144:147], a[8:11], a[136:139], v[144:147]\n"
    "ds_read_b128 a[4:7], v200 offset:40960\n"
    "s_add_u32 m0, s59, 114688\n"
    "v_mfma_f32_16x16x32_bf16 v[152:155], a[8:11], a[140:143], v[152:155]\n"
    "buffer_load_dwordx4 v216, s[44:47], s57 offen offset:0 lds\n"
    "v_add_f32_e64 v222, v222, v170\n"
    "v_mfma_f32_16x16x32_bf16 v[148:151], a[12:15], a[136:139], v[148:151]\n"
    "ds_read_b128 a[8:11], v201 offset:32768\n"
    "v_cvt_pk_bf16_f32 v185, v170, v171\n"
    "v_mfma_f32_16x16x32_bf16 v[156:159], a[12:15], a[140:143], v[156:159]\n"
    "v_exp_f32_e64 v182, v142\n"
    "v_mfma_f32_16x16x32_bf16 v[144:147], a[16:19], a[144:147], v[144:147]\n"
    "ds_read_b128 a[12:15], v201 offset:40960\n"
    "v_add_f32_e64 v222, v222, v171\n"
    "v_mfma_f32_16x16x32_bf16 v[152:155], a[16:19], a[148:151], v[152:155]\n"
    "buffer_load_dwordx4 v217, s[44:47], s57 offen offset:1024 lds\n"
    "v_add_f32_e64 v222, v222, v172\n"
    "v_mfma_f32_16x16x32_bf16 v[148:151], a[20:23], a[144:147], v[148:151]\n"
    "ds_read_b128 a[16:19], v202 offset:32768\n"
    "v_cvt_pk_bf16_f32 v186, v172, v173\n"
    "v_mfma_f32_16x16x32_bf16 v[156:159], a[20:23], a[148:151], v[156:159]\n"
    "v_exp_f32_e64 v183, v143\n"
    "v_mfma_f32_16x16x32_bf16 v[144:147], a[24:27], a[152:155], v[144:147]\n"
    "ds_read_b128 a[20:23], v202 offset:40960\n"
    "v_add_f32_e64 v222, v222, v173\n"
    "v_mfma_f32_16x16x32_bf16 v[152:155], a[24:27], a[156:159], v[152:155]\n"
    "buffer_load_dwordx4 v218, s[44:47], s57 offen offset:2048 lds\n"
    "v_add_f32_e64 v222, v222, v174\n"
    "v_mfma_f32_16x16x32_bf16 v[148:151], a[28:31], a[152:155], v[148:151]\n"
    "ds_read_b128 a[24:27], v203 offset:32768\n"
    "v_cvt_pk_bf16_f32 v187, v174, v175\n"
    "v_mfma_f32_16x16x32_bf16 v[156:159], a[28:31], a[156:159], v[156:159]\n"
    "v_add_f32 v222, v222, v175\n"
    "v_add_f32 v223, v176, v177\n"
    "v_mfma_f32_16x16x32_bf16 v[144:147], a[32:35], a[160:163], v[144:147]\n"
    "ds_read_b128 a[28:31], v203 offset:40960\n"
    "v_cvt_pk_bf16_f32 v188, v176, v177\n"
    "v_mfma_f32_16x16x32_bf16 v[152:155], a[32:35], a[164:167], v[152:155]\n"
    "buffer_load_dwordx4 v219, s[44:47], s57 offen offset:3072 lds\n"
    "v_add_f32_e64 v223, v223, v178\n"
    "v_mfma_f32_16x16x32_bf16 v[148:151], a[36:39], a[160:163], v[148:151]\n"
    "ds_read_b128 a[32:35], v200 offset:33024\n"
    "v_cvt_pk_bf16_f32 v189, v178, v179\n"
    "v_mfma_f32_16x16x32_bf16 v[156:159], a[36:39], a[164:167], v[156:159]\n"
    "v_add_f32 v223, v223, v179\n"
    "v_add_f32 v223, v223, v180\n"
    "v_mfma_f32_16x16x32_bf16 v[144:147], a[40:43], a[168:171], v[144:147]\n"
    "ds_read_b128 a[36:39], v200 offset:41216\n"
    "s_add_u32 m0, s59, 16384\n"
    "v_mfma_f32_16x16x32_bf16 v[152:155], a[40:43], a[172:175], v[152:155]\n"
    "buffer_load_dwordx4 v212, s[40:43], s56 offen offset:0 lds\n"
    "v_cvt_pk_bf16_f32 v190, v180, v181\n"
    "v_mfma_f32_16x16x32_bf16 v[148:151], a[44:47], a[168:171], v[148:151]\n"
    "ds_read_b128 a[40:43], v201 offset:33024\n"
    "v_add_f32_e64 v223, v223, v181\n"
    "v_mfma_f32_16x16x32_bf16 v[156:159], a[44:47], a[172:175], v[156:159]\n"
    "buffer_load_dwordx4 v213, s[40:43], s56 offen offset:1024 lds\n"
    "v_add_f32_e64 v223, v223, v182\n"
    "v_mfma_f32_16x16x32_bf16 v[144:147], a[48:51], a[176:179], v[144:147]\n"
    "ds_read_b128 a[44:47], v201 offset:41216\n"
    "v_cvt_pk_bf16_f32 v191, v182, v183\n"
    "v_mfma_f32_16x16x32_bf16 v[152:155], a[48:51], a[180:183], v[152:155]\n"
    "s_load_dword s88, %22, s89\n"
    "s_load_dword s88, %22, s89 offset:0x40\n"
    "v_mfma_f32_16x16x32_bf16 v[148:151], a[52:55], a[176:179], v[148:151]\n"
    "ds_read_b128 a[48:51], v202 offset:33024\n"
    "v_add_f32_e64 v223, v223, v183\n"
    "v_mfma_f32_16x16x32_bf16 v[156:159], a[52:55], a[180:183], v[156:159]\n"
    "buffer_load_dwordx4 v214, s[40:43], s56 offen offset:2048 lds\n"
    "v_add_f32_e64 v224, v222, v223\n"
    "v_mfma_f32_16x16x32_bf16 v[144:147], a[56:59], a[184:187], v[144:147]\n"
    "ds_read_b128 a[52:55], v202 offset:41216\n"
    "v_cmp_nle_f32_e64 vcc, v224, s60\n"
    "v_mfma_f32_16x16x32_bf16 v[152:155], a[56:59], a[188:191], v[152:155]\n"
    "buffer_load_dwordx4 v215, s[40:43], s56 offen offset:3072 lds\n"
    "s_cbranch_vccnz L_stub0\n"
    "L_ret0:\n"
    "v_mfma_f32_16x16x32_bf16 v[148:151], a[60:63], a[184:187], v[148:151]\n"
    "ds_read_b128 a[56:59], v203 offset:33024\n"
    "s_add_u32 s57, s57, s77\n"
    "v_mfma_f32_16x16x32_bf16 v[156:159], a[60:63], a[188:191], v[156:159]\n"
    "s_add_u32 s56, s56, s77\n"
    "s_waitcnt lgkmcnt(15)\n"
    "v_mfma_f32_16x16x32_bf16 v[0:3], a[64:67], v[184:187], v[0:3]\n"
    "ds_read_b128 a[60:63], v203 offset:41216\n"
    "v_mfma_f32_16x16x32_bf16 v[4:7], a[64:67], v[188:191], v[4:7]\n"
    "s_waitcnt lgkmcnt(14)\n"
    "v_mfma_f32_16x16x32_bf16 v[8:11], a[68:71], v[184:187], v[8:11]\n"
    "ds_read_b64_tr_b16 a[64:65], v204 offset:16384\n"
    "ds_read_b64_tr_b16 a[66:67], v204 offset:24576\n"
    "v_mfma_f32_16x16x32_bf16 v[12:15], a[68:71], v[188:191], v[12:15]\n"
    "v_add_f32 v220, v220, v222\n"
    "v_add_f32_e64 v221, v221, v223\n"
    "v_mfma_f32_16x16x32_bf16 v[16:19], a[72:75], v[184:187], v[16:19]\n"
    "ds_read_b64_tr_b16 a[68:69], v205 offset:16384\n"
    "ds_read_b64_tr_b16 a[70:71], v205 offset:24576\n"
    "v_mfma_f32_16x16x32_bf16 v[20:23], a[72:75], v[188:191], v[20:23]\n"
    "v_exp_f32_e64 v168, v144\n"
    "v_mfma_f32_16x16x32_bf16 v[24:27], a[76:79], v[184:187], v[24:27]\n"
    "ds_read_b64_tr_b16 a[72:73], v206 offset:16384\n"
    "ds_read_b64_tr_b16 a[74:75], v206 offset:24576\n"
    "v_mfma_f32_16x16x32_bf16 v[28:31], a[76:79], v[188:191], v[28:31]\n"
    "v_exp_f32_e64 v169, v145\n"
    "v_mfma_f32_16x16x32_bf16 v[32:35], a[80:83], v[184:187], v[32:35]\n"
    "ds_read_b64_tr_b16 a[76:77], v207 offset:16384\n"
    "ds_read_b64_tr_b16 a[78:79], v207 offset:24576\n"
    "v_mfma_f32_16x16x32_bf16 v[36:39], a[80:83], v[188:191], v[36:39]\n"
    "v_exp_f32_e64 v170, v146\n"
    "v_mfma_f32_16x16x32_bf16 v[40:43], a[84:87], v[184:187], v[40:43]\n"
    "ds_read_b64_tr_b16 a[80:81], v208 offset:16384\n"
    "ds_read_b64_tr_b16 a[82:83], v208 offset:24576\n"
    "v_mfma_f32_16x16x32_bf16 v[44:47], a[84:87], v[188:191], v[44:47]\n"
    "v_exp_f32_e64 v171, v147\n"
    "v_mfma_f32_16x16x32_bf16 v[48:51], a[88:91], v[184:187], v[48:51]\n"
    "ds_read_b64_tr_b16 a[84:85], v209 offset:16384\n"
    "ds_read_b64_tr_b16 a[86:87], v209 offset:24576\n"
    "v_mfma_f32_16x16x32_bf16 v[52:55], a[88:91], v[188:191], v[52:55]\n"
    "v_exp_f32_e64 v172, v148\n"
    "v_mfma_f32_16x16x32_bf16 v[56:59], a[92:95], v[184:187], v[56:59]\n"
    "ds_read_b64_tr_b16 a[88:89], v210 offset:16384\n"
    "ds_read_b64_tr_b16 a[90:91], v210 offset:24576\n"
    "v_mfma_f32_16x16x32_bf16 v[60:63], a[92:95], v[188:191], v[60:63]\n"
    "v_exp_f32_e64 v173, v149\n"
    "v_mfma_f32_16x16x32_bf16 v[64:67], a[96:99], v[184:187], v[64:67]\n"
    "ds_read_b64_tr_b16 a[92:93], v211 offset:16384\n"
    "ds_read_b64_tr_b16 a[94:95], v211 offset:24576\n"
    "v_mfma_f32_16x16x32_bf16 v[68:71], a[96:99], v[188:191], v[68:71]\n"
    "v_exp_f32_e64 v174, v150\n"
    "v_mfma_f32_16x16x32_bf16 v[72:75], a[100:103], v[184:187], v[72:75]\n"
    "ds_read_b64_tr_b16 a[96:97], v204 offset:16640\n"
    "ds_read_b64_tr_b16 a[98:99], v204 offset:24832\n"
    "v_mfma_f32_16x16x32_bf16 v[76:79], a[100:103], v[188:191], v[76:79]\n"
    "v_exp_f32_e64 v175, v151\n"
    "v_mfma_f32_16x16x32_bf16 v[80:83], a[104:107], v[184:187], v[80:83]\n"
    "ds_read_b64_tr_b16 a[100:101], v205 offset:16640\n"
    "ds_read_b64_tr_b16 a[102:103], v205 offset:24832\n"
    "v_mfma_f32_16x16x32_bf16 v[84:87], a[104:107], v[188:191], v[84:87]\n"
    "v_exp_f32_e64 v176, v152\n"
    "v_mfma_f32_16x16x32_bf16 v[88:91], a[108:111], v[184:187], v[88:91]\n"
    "ds_read_b64_tr_b16 a[104:105], v206 offset:16640\n"
    "ds_read_b64_tr_b16 a[106:107], v206 offset:24832\n"
    "v_mfma_f32_16x16x32_bf16 v[92:95], a[108:111], v[188:191], v[92:95]\n"
    "v_exp_f32_e64 v177, v153\n"
    "v_mfma_f32_16x16x32_bf16 v[96:99], a[112:115], v[184:187], v[96:99]\n"
    "ds_read_b64_tr_b16 a[108:109], v207 offset:16640\n"
    "ds_read_b64_tr_b16 a[110:111], v207 offset:24832\n"
    "v_mfma_f32_16x16x32_bf16 v[100:103], a[112:115], v[188:191], v[100:103]\n"
    "v_exp_f32_e64 v178, v154\n"
    "v_mfma_f32_16x16x32_bf16 v[104:107], a[116:119], v[184:187], v[104:107]\n"
    "ds_read_b64_tr_b16 a[112:113], v208 offset:16640\n"
    "ds_read_b64_tr_b16 a[114:115], v208 offset:24832\n"
    "v_mfma_f32_16x16x32_bf16 v[108:111], a[116:119], v[188:191], v[108:111]\n"
    "v_exp_f32_e64 v179, v155\n"
    "v_mfma_f32_16x16x32_bf16 v[112:115], a[120:123], v[184:187], v[112:115]\n"
    "ds_read_b64_tr_b16 a[116:117], v209 offset:16640\n"
    "ds_read_b64_tr_b16 a[118:119], v209 offset:24832\n"
    "v_mfma_f32_16x16x32_bf16 v[116:119], a[120:123], v[188:191], v[116:119]\n"
    "v_exp_f32_e64 v180, v156\n"
    "v_mfma_f32_16x16x32_bf16 v[120:123], a[124:127], v[184:187], v[120:123]\n"
    "ds_read_b64_tr_b16 a[120:121], v210 offset:16640\n"
    "ds_read_b64_tr_b16 a[122:123], v210 offset:24832\n"
    "v_mfma_f32_16x16x32_bf16 v[124:127], a[124:127], v[188:191], v[124:127]\n"
    "v_add_f32 v222, v168, v169\n"
    "s_waitcnt lgkmcnt(15)\n"
    "s_cmp_eq_u32 s58, 0\n"
    "s_cbranch_scc1 L_patch\n"
    "L_patchret:\n"
    "v_mfma_f32_16x16x32_bf16 v[128:131], a[0:3], a[128:131], v[160:163]\n"
    "ds_read_b64_tr_b16 a[124:125], v211 offset:16640\n"
    "ds_read_b64_tr_b16 a[126:127], v211 offset:24832\n"
    "v_mfma_f32_16x16x32_bf16 v[136:139], a[0:3], a[132:135], v[164:167]\n"
    "s_waitcnt vmcnt(12)\n"
    "s_barrier\n"
    "v_mfma_f32_16x16x32_bf16 v[132:135], a[4:7], a[128:131], v[160:163]\n"
    "ds_read_b128 a[0:3], v200 offset:49152\n"
    "v_cvt_pk_bf16_f32 v192, v168, v169\n"
    "v_mfma_f32_16x16x32_bf16 v[140:143], a[4:7], a[132:135], v[164:167]\n"
    "v_exp_f32_e64 v181, v157\n"
    "v_mfma_f32_16x16x32_bf16 v[128:131], a[8:11], a[136:139], v[128:131]\n"
    "ds_read_b128 a[4:7], v200 offset:57344\n"
    "s_add_u32 m0, s59, 65536\n"
    "v_mfma_f32_16x16x32_bf16 v[136:139], a[8:11], a[140:143], v[136:139]\n"
    "buffer_load_dwordx4 v216, s[44:47], s57 offen offset:0 lds\n"
    "v_add_f32_e64 v222, v222, v170\n"
    "v_mfma_f32_16x16x32_bf16 v[132:135], a[12:15], a[136:139], v[132:135]\n"
    "ds_read_b128 a[8:11], v201 offset:49152\n"
    "v_cvt_pk_bf16_f32 v193, v170, v171\n"
    "v_mfma_f32_16x16x32_bf16 v[140:143], a[12:15], a[140:143], v[140:143]\n"
    "v_exp_f32_e64 v182, v158\n"
    "v_mfma_f32_16x16x32_bf16 v[128:131], a[16:19], a[144:147], v[128:131]\n"
    "ds_read_b128 a[12:15], v201 offset:57344\n"
    "v_add_f32_e64 v222, v222, v171\n"
    "v_mfma_f32_16x16x32_bf16 v[136:139], a[16:19], a[148:151], v[136:139]\n"
    "buffer_load_dwordx4 v217, s[44:47], s57 offen offset:1024 lds\n"
    "v_add_f32_e64 v222, v222, v172\n"
    "v_mfma_f32_16x16x32_bf16 v[132:135], a[20:23], a[144:147], v[132:135]\n"
    "ds_read_b128 a[16:19], v202 offset:49152\n"
    "v_cvt_pk_bf16_f32 v194, v172, v173\n"
    "v_mfma_f32_16x16x32_bf16 v[140:143], a[20:23], a[148:151], v[140:143]\n"
    "v_exp_f32_e64 v183, v159\n"
    "v_mfma_f32_16x16x32_bf16 v[128:131], a[24:27], a[152:155], v[128:131]\n"
    "ds_read_b128 a[20:23], v202 offset:57344\n"
    "v_add_f32_e64 v222, v222, v173\n"
    "v_mfma_f32_16x16x32_bf16 v[136:139], a[24:27], a[156:159], v[136:139]\n"
    "buffer_load_dwordx4 v218, s[44:47], s57 offen offset:2048 lds\n"
    "v_add_f32_e64 v222, v222, v174\n"
    "v_mfma_f32_16x16x32_bf16 v[132:135], a[28:31], a[152:155], v[132:135]\n"
    "ds_read_b128 a[24:27], v203 offset:49152\n"
    "v_cvt_pk_bf16_f32 v195, v174, v175\n"
    "v_mfma_f32_16x16x32_bf16 v[140:143], a[28:31], a[156:159], v[140:143]\n"
    "v_add_f32 v222, v222, v175\n"
    "v_add_f32 v223, v176, v177\n"
    "v_mfma_f32_16x16x32_bf16 v[128:131], a[32:35], a[160:163], v[128:131]\n"
    "ds_read_b128 a[28:31], v203 offset:57344\n"
    "v_cvt_pk_bf16_f32 v196, v176, v177\n"
    "v_mfma_f32_16x16x32_bf16 v[136:139], a[32:35], a[164:167], v[136:139]\n"
    "buffer_load_dwordx4 v219, s[44:47], s57 offen offset:3072 lds\n"
    "v_add_f32_e64 v223, v223, v178\n"
    "v_mfma_f32_16x16x32_bf16 v[132:135], a[36:39], a[160:163], v[132:135]\n"
    "ds_read_b128 a[32:35], v200 offset:49408\n"
    "v_cvt_pk_bf16_f32 v197, v178, v179\n"
    "v_mfma_f32_16x16x32_bf16 v[140:143], a[36:39], a[164:167], v[140:143]\n"
    "v_add_f32 v223, v223, v179\n"
    "v_add_f32 v223, v223, v180\n"
    "v_mfma_f32_16x16x32_bf16 v[128:131], a[40:43], a[168:171], v[128:131]\n"
    "ds_read_b128 a[36:39], v200 offset:57600\n"
    "s_add_u32 m0, s59, 32768\n"
    "v_mfma_f32_16x16x32_bf16 v[136:139], a[40:43], a[172:175], v[136:139]\n"
    "buffer_load_dwordx4 v212, s[40:43], s56 offen offset:0 lds\n"
    "v_cvt_pk_bf16_f32 v198, v180, v181\n"
    "v_mfma_f32_16x16x32_bf16 v[132:135], a[44:47], a[168:171], v[132:135]\n"
    "ds_read_b128 a[40:43], v201 offset:49408\n"
    "v_add_f32_e64 v223, v223, v181\n"
    "v_mfma_f32_16x16x32_bf16 v[140:143], a[44:47], a[172:175], v[140:143]\n"
    "buffer_load_dwordx4 v213, s[40:43], s56 offen offset:1024 lds\n"
    "v_add_f32_e64 v223, v223, v182\n"
    "v_mfma_f32_16x16x32_bf16 v[128:131], a[48:51], a[176:179], v[128:131]\n"
    "ds_read_b128 a[44:47], v201 offset:57600\n"
    "v_cvt_pk_bf16_f32 v199, v182, v183\n"
    "v_mfma_f32_16x16x32_bf16 v[136:139], a[48:51], a[180:183], v[136:139]\n"
    "s_load_dword s88, %23, s89\n"
    "s_load_dword s88, %23, s89 offset:0x40\n"
    "v_mfma_f32_16x16x32_bf16 v[132:135], a[52:55], a[176:179], v[132:135]\n"
    "ds_read_b128 a[48:51], v202 offset:49408\n"
    "v_add_f32_e64 v223, v223, v183\n"
    "v_mfma_f32_16x16x32_bf16 v[140:143], a[52:55], a[180:183], v[140:143]\n"
    "buffer_load_dwordx4 v214, s[40:43], s56 offen offset:2048 lds\n"
    "v_add_f32_e64 v224, v222, v223\n"
    "v_mfma_f32_16x16x32_bf16 v[128:131], a[56:59], a[184:187], v[128:131]\n"
    "ds_read_b128 a[52:55], v202 offset:57600\n"
    "v_cmp_nle_f32_e64 vcc, v224, s60\n"
    "v_mfma_f32_16x16x32_bf16 v[136:139], a[56:59], a[188:191], v[136:139]\n"
    "buffer_load_dwordx4 v215, s[40:43], s56 offen offset:3072 lds\n"
    "s_cbranch_vccnz L_stub1\n"
    "L_ret1:\n"
    "v_mfma_f32_16x16x32_bf16 v[132:135], a[60:63], a[184:187], v[132:135]\n"
    "ds_read_b128 a[56:59], v203 offset:49408\n"
    "s_add_u32 s57, s57, s77\n"
    "v_mfma_f32_16x16x32_bf16 v[140:143], a[60:63], a[188:191], v[140:143]\n"
    "s_add_u32 s56, s56, s77\n"
    "s_waitcnt lgkmcnt(15)\n"
    "v_mfma_f32_16x16x32_bf16 v[0:3], a[64:67], v[192:195], v[0:3]\n"
    "ds_read_b128 a[60:63], v203 offset:57600\n"
    "v_mfma_f32_16x16x32_bf16 v[4:7], a[64:67], v[196:199], v[4:7]\n"
    "s_waitcnt lgkmcnt(14)\n"
    "v_mfma_f32_16x16x32_bf16 v[8:11], a[68:71], v[192:195], v[8:11]\n"
    "ds_read_b64_tr_b16 a[64:65], v204 offset:32768\n"
    "ds_read_b64_tr_b16 a[66:67], v204 offset:40960\n"
    "v_mfma_f32_16x16x32_bf16 v[12:15], a[68:71], v[196:199], v[12:15]\n"
    "v_add_f32 v220, v220, v222\n"
    "v_add_f32_e64 v221, v221, v223\n"
    "v_mfma_f32_16x16x32_bf16 v[16:19], a[72:75], v[192:195], v[16:19]\n"
    "ds_read_b64_tr_b16 a[68:69], v205 offset:32768\n"
    "ds_read_b64_tr_b16 a[70:71], v205 offset:40960\n"
    "v_mfma_f32_16x16x32_bf16 v[20:23], a[72:75], v[196:199], v[20:23]\n"
    "v_exp_f32_e64 v168, v128\n"
    "v_mfma_f32_16x16x32_bf16 v[24:27], a[76:79], v[192:195], v[24:27]\n"
    "ds_read_b64_tr_b16 a[72:73], v206 offset:32768\n"
    "ds_read_b64_tr_b16 a[74:75], v206 offset:40960\n"
    "v_mfma_f32_16x16x32_bf16 v[28:31], a[76:79], v[196:199], v[28:31]\n"
    "v_exp_f32_e64 v169, v129\n"
    "v_mfma_f32_16x16x32_bf16 v[32:35], a[80:83], v[192:195], v[32:35]\n"
    "ds_read_b64_tr_b16 a[76:77], v207 offset:32768\n"
    "ds_read_b64_tr_b16 a[78:79], v207 offset:40960\n"
    "v_mfma_f32_16x16x32_bf16 v[36:39], a[80:83], v[196:199], v[36:39]\n"
    "v_exp_f32_e64 v170, v130\n"
    "v_mfma_f32_16x16x32_bf16 v[40:43], a[84:87], v[192:195], v[40:43]\n"
    "ds_read_b64_tr_b16 a[80:81], v208 offset:32768\n"
    "ds_read_b64_tr_b16 a[82:83], v208 offset:40960\n"
    "v_mfma_f32_16x16x32_bf16 v[44:47], a[84:87], v[196:199], v[44:47]\n"
    "v_exp_f32_e64 v171, v131\n"
    "v_mfma_f32_16x16x32_bf16 v[48:51], a[88:91], v[192:195], v[48:51]\n"
    "ds_read_b64_tr_b16 a[84:85], v209 offset:32768\n"
    "ds_read_b64_tr_b16 a[86:87], v209 offset:40960\n"
    "v_mfma_f32_16x16x32_bf16 v[52:55], a[88:91], v[196:199], v[52:55]\n"
    "v_exp_f32_e64 v172, v132\n"
    "v_mfma_f32_16x16x32_bf16 v[56:59], a[92:95], v[192:195], v[56:59]\n"
    "ds_read_b64_tr_b16 a[88:89], v210 offset:32768\n"
    "ds_read_b64_tr_b16 a[90:91], v210 offset:40960\n"
    "v_mfma_f32_16x16x32_bf16 v[60:63], a[92:95], v[196:199], v[60:63]\n"
    "v_exp_f32_e64 v173, v133\n"
    "v_mfma_f32_16x16x32_bf16 v[64:67], a[96:99], v[192:195], v[64:67]\n"
    "ds_read_b64_tr_b16 a[92:93], v211 offset:32768\n"
    "ds_read_b64_tr_b16 a[94:95], v211 offset:40960\n"
    "v_mfma_f32_16x16x32_bf16 v[68:71], a[96:99], v[196:199], v[68:71]\n"
    "v_exp_f32_e64 v174, v134\n"
    "v_mfma_f32_16x16x32_bf16 v[72:75], a[100:103], v[192:195], v[72:75]\n"
    "ds_read_b64_tr_b16 a[96:97], v204 offset:33024\n"
    "ds_read_b64_tr_b16 a[98:99], v204 offset:41216\n"
    "v_mfma_f32_16x16x32_bf16 v[76:79], a[100:103], v[196:199], v[76:79]\n"
    "v_exp_f32_e64 v175, v135\n"
    "v_mfma_f32_16x16x32_bf16 v[80:83], a[104:107], v[192:195], v[80:83]\n"
    "ds_read_b64_tr_b16 a[100:101], v205 offset:33024\n"
    "ds_read_b64_tr_b16 a[102:103], v205 offset:41216\n"
    "v_mfma_f32_16x16x32_bf16 v[84:87], a[104:107], v[196:199], v[84:87]\n"
    "v_exp_f32_e64 v176, v136\n"
    "v_mfma_f32_16x16x32_bf16 v[88:91], a[108:111], v[192:195], v[88:91]\n"
    "ds_read_b64_tr_b16 a[104:105], v206 offset:33024\n"
    "ds_read_b64_tr_b16 a[106:107], v206 offset:41216\n"
    "v_mfma_f32_16x16x32_bf16 v[92:95], a[108:111], v[196:199], v[92:95]\n"
    "v_exp_f32_e64 v177, v137\n"
    "v_mfma_f32_16x16x32_bf16 v[96:99], a[112:115], v[192:195], v[96:99]\n"
    "ds_read_b64_tr_b16 a[108:109], v207 offset:33024\n"
    "ds_read_b64_tr_b16 a[110:111], v207 offset:41216\n"
    "v_mfma_f32_16x16x32_bf16 v[100:103], a[112:115], v[196:199], v[100:103]\n"
    "v_exp_f32_e64 v178, v138\n"
    "v_mfma_f32_16x16x32_bf16 v[104:107], a[116:119], v[192:195], v[104:107]\n"
    "ds_read_b64_tr_b16 a[112:113], v208 offset:33024\n"
    "ds_read_b64_tr_b16 a[114:115], v208 offset:41216\n"
    "v_mfma_f32_16x16x32_bf16 v[108:111], a[116:119], v[196:199], v[108:111]\n"
    "v_exp_f32_e64 v179, v139\n"
    "v_mfma_f32_16x16x32_bf16 v[112:115], a[120:123], v[192:195], v[112:115]\n"
    "ds_read_b64_tr_b16 a[116:117], v209 offset:33024\n"
    "ds_read_b64_tr_b16 a[118:119], v209 offset:41216\n"
    "v_mfma_f32_16x16x32_bf16 v[116:119], a[120:123], v[196:199], v[116:119]\n"
    "v_exp_f32_e64 v180, v140\n"
    "v_mfma_f32_16x16x32_bf16 v[120:123], a[124:127], v[192:195], v[120:123]\n"
    "ds_read_b64_tr_b16 a[120:121], v210 offset:33024\n"
    "ds_read_b64_tr_b16 a[122:123], v210 offset:41216\n"
    "v_mfma_f32_16x16x32_bf16 v[124:127], a[124:127], v[196:199], v[124:127]\n"
    "v_add_f32 v222, v168, v169\n"
    "s_waitcnt lgkmcnt(15)\n"
    "v_mfma_f32_16x16x32_bf16 v[144:147], a[0:3], a[128:131], v[160:163]\n"
    "ds_read_b64_tr_b16 a[124:125], v211 offset:33024\n"
    "ds_read_b64_tr_b16 a[126:127], v211 offset:41216\n"
    "v_mfma_f32_16x16x32_bf16 v[152:155], a[0:3], a[132:135], v[164:167]\n"
    "s_waitcnt vmcnt(12)\n"
    "s_barrier\n"
    "v_mfma_f32_16x16x32_bf16 v[148:151], a[4:7], a[128:131], v[160:163]\n"
    "ds_read_b128 a[0:3], v200 offset:0\n"
    "v_cvt_pk_bf16_f32 v184, v168, v169\n"
    "v_mfma_f32_16x16x32_bf16 v[156:159], a[4:7], a[132:135], v[164:167]\n"
    "v_exp_f32_e64 v181, v141\n"
    "v_mfma_f32_16x16x32_bf16 v[144:147], a[8:11], a[136:139], v[144:147]\n"
    "ds_read_b128 a[4:7], v200 offset:8192\n"
    "s_add_u32 m0, s59, 81920\n"
    "v_mfma_f32_16x16x32_bf16 v[152:155], a[8:11], a[140:143], v[152:155]\n"
    "buffer_load_dwordx4 v216, s[44:47], s57 offen offset:0 lds\n"
    "v_add_f32_e64 v222, v222, v170\n"
    "v_mfma_f32_16x16x32_bf16 v[148:151], a[12:15], a[136:139], v[148:151]\n"
    "ds_read_b128 a[8:11], v201 offset:0\n"
    "v_cvt_pk_bf16_f32 v185, v170, v171\n"
    "v_mfma_f32_16x16x32_bf16 v[156:159], a[12:15], a[140:143], v[156:159]\n"
    "v_exp_f32_e64 v182, v142\n"
    "v_mfma_f32_16x16x32_bf16 v[144:147], a[16:19], a[144:147], v[144:147]\n"
    "ds_read_b128 a[12:15], v201 offset:8192\n"
    "v_add_f32_e64 v222, v222, v171\n"
    "v_mfma_f32_16x16x32_bf16 v[152:155], a[16:19], a[148:151], v[152:155]\n"
    "buffer_load_dwordx4 v217, s[44:47], s57 offen offset:1024 lds\n"
    "v_add_f32_e64 v222, v222, v172\n"
    "v_mfma_f32_16x16x32_bf16 v[148:151], a[20:23], a[144:147], v[148:151]\n"
    "ds_read_b128 a[16:19], v202 offset:0\n"
    "v_cvt_pk_bf16_f32 v186, v172, v173\n"
    "v_mfma_f32_16x16x32_bf16 v[156:159], a[20:23], a[148:151], v[156:159]\n"
    "v_exp_f32_e64 v183, v143\n"
    "v_mfma_f32_16x16x32_bf16 v[144:147], a[24:27], a[152:155], v[144:147]\n"
    "ds_read_b128 a[20:23], v202 offset:8192\n"
    "v_add_f32_e64 v222, v222, v173\n"
    "v_mfma_f32_16x16x32_bf16 v[152:155], a[24:27], a[156:159], v[152:155]\n"
    "buffer_load_dwordx4 v218, s[44:47], s57 offen offset:2048 lds\n"
    "v_add_f32_e64 v222, v222, v174\n"
    "v_mfma_f32_16x16x32_bf16 v[148:151], a[28:31], a[152:155], v[148:151]\n"
    "ds_read_b128 a[24:27], v203 offset:0\n"
    "v_cvt_pk_bf16_f32 v187, v174, v175\n"
    "v_mfma_f32_16x16x32_bf16 v[156:159], a[28:31], a[156:159], v[156:159]\n"
    "v_add_f32 v222, v222, v175\n"
    "v_add_f32 v223, v176, v177\n"
    "v_mfma_f32_16x16x32_bf16 v[144:147], a[32:35], a[160:163], v[144:147]\n"
    "ds_read_b128 a[28:31], v203 offset:8192\n"
    "v_cvt_pk_bf16_f32 v188, v176, v177\n"
    "v_mfma_f32_16x16x32_bf16 v[152:155], a[32:35], a[164:167], v[152:155]\n"
    "buffer_load_dwordx4 v219, s[44:47], s57 offen offset:3072 lds\n"
    "v_add_f32_e64 v223, v223, v178\n"
    "v_mfma_f32_16x16x32_bf16 v[148:151], a[36:39], a[160:163], v[148:151]\n"
    "ds_read_b128 a[32:35], v200 offset:256\n"
    "v_cvt_pk_bf16_f32 v189, v178, v179\n"
    "v_mfma_f32_16x16x32_bf16 v[156:159], a[36:39], a[164:167], v[156:159]\n"
    "v_add_f32 v223, v223, v179\n"
    "v_add_f32 v223, v223, v180\n"
    "v_mfma_f32_16x16x32_bf16 v[144:147], a[40:43], a[168:171], v[144:147]\n"
    "ds_read_b128 a[36:39], v200 offset:8448\n"
    "s_add_u32 m0, s59, 49152\n"
    "v_mfma_f32_16x16x32_bf16 v[152:155], a[40:43], a[172:175], v[152:155]\n"
    "buffer_load_dwordx4 v212, s[40:43], s56 offen offset:0 lds\n"
    "v_cvt_pk_bf16_f32 v190, v180, v181\n"
    "v_mfma_f32_16x16x32_bf16 v[148:151], a[44:47], a[168:171], v[148:151]\n"
    "ds_read_b128 a[40:43], v201 offset:256\n"
    "v_add_f32_e64 v223, v223, v181\n"
    "v_mfma_f32_16x16x32_bf16 v[156:159], a[44:47], a[172:175], v[156:159]\n"
    "buffer_load_dwordx4 v213, s[40:43], s56 offen offset:1024 lds\n"
    "v_add_f32_e64 v223, v223, v182\n"
    "v_mfma_f32_16x16x32_bf16 v[144:147], a[48:51], a[176:179], v[144:147]\n"
    "ds_read_b128 a[44:47], v201 offset:8448\n"
    "v_cvt_pk_bf16_f32 v191, v182, v183\n"
    "v_mfma_f32_16x16x32_bf16 v[152:155], a[48:51], a[180:183], v[152:155]\n"
    "s_load_dword s88, %24, s89\n"
    "s_load_dword s88, %24, s89 offset:0x40\n"
    "v_mfma_f32_16x16x32_bf16 v[148:151], a[52:55], a[176:179], v[148:151]\n"
    "ds_read_b128 a[48:51], v202 offset:256\n"
    "v_add_f32_e64 v223, v223, v183\n"
    "v_mfma_f32_16x16x32_bf16 v[156:159], a[52:55], a[180:183], v[156:159]\n"
    "buffer_load_dwordx4 v214, s[40:43], s56 offen offset:2048 lds\n"
    "v_add_f32_e64 v224, v222, v223\n"
    "v_mfma_f32_16x16x32_bf16 v[144:147], a[56:59], a[184:187], v[144:147]\n"
    "ds_read_b128 a[52:55], v202 offset:8448\n"
    "v_cmp_nle_f32_e64 vcc, v224, s60\n"
    "v_mfma_f32_16x16x32_bf16 v[152:155], a[56:59], a[188:191], v[152:155]\n"
    "buffer_load_dwordx4 v215, s[40:43], s56 offen offset:3072 lds\n"
    "s_cbranch_vccnz L_stub2\n"
    "L_ret2:\n"
    "v_mfma_f32_16x16x32_bf16 v[148:151], a[60:63], a[184:187], v[148:151]\n"
    "ds_read_b128 a[56:59], v203 offset:256\n"
    "s_add_u32 s57, s57, s77\n"
    "v_mfma_f32_16x16x32_bf16 v[156:159], a[60:63], a[188:191], v[156:159]\n"
    "s_add_u32 s56, s56, s77\n"
    "s_waitcnt lgkmcnt(15)\n"
    "v_mfma_f32_16x16x32_bf16 v[0:3], a[64:67], v[184:187], v[0:3]\n"
    "ds_read_b128 a[60:63], v203 offset:8448\n"
    "v_mfma_f32_16x16x32_bf16 v[4:7], a[64:67], v[188:191], v[4:7]\n"
    "s_waitcnt lgkmcnt(14)\n"
    "v_mfma_f32_16x16x32_bf16 v[8:11], a[68:71], v[184:187], v[8:11]\n"
    "ds_read_b64_tr_b16 a[64:65], v204 offset:49152\n"
    "ds_read_b64_tr_b16 a[66:67], v204 offset:57344\n"
    "v_mfma_f32_16x16x32_bf16 v[12:15], a[68:71], v[188:191], v[12:15]\n"
    "v_add_f32 v220, v220, v222\n"
    "v_add_f32_e64 v221, v221, v223\n"
    "v_mfma_f32_16x16x32_bf16 v[16:19], a[72:75], v[184:187], v[16:19]\n"
    "ds_read_b64_tr_b16 a[68:69], v205 offset:49152\n"
    "ds_read_b64_tr_b16 a[70:71], v205 offset:57344\n"
    "v_mfma_f32_16x16x32_bf16 v[20:23], a[72:75], v[188:191], v[20:23]\n"
    "v_exp_f32_e64 v168, v144\n"
    "v_mfma_f32_16x16x32_bf16 v[24:27], a[76:79], v[184:187], v[24:27]\n"
    "ds_read_b64_tr_b16 a[72:73], v206 offset:49152\n"
    "ds_read_b64_tr_b16 a[74:75], v206 offset:57344\n"
    "v_mfma_f32_16x16x32_bf16 v[28:31], a[76:79], v[188:191], v[28:31]\n"
    "v_exp_f32_e64 v169, v145\n"
    "v_mfma_f32_16x16x32_bf16 v[32:35], a[80:83], v[184:187], v[32:35]\n"
    "ds_read_b64_tr_b16 a[76:77], v207 offset:49152\n"
    "ds_read_b64_tr_b16 a[78:79], v207 offset:57344\n"
    "v_mfma_f32_16x16x32_bf16 v[36:39], a[80:83], v[188:191], v[36:39]\n"
    "v_exp_f32_e64 v170, v146\n"
    "v_mfma_f32_16x16x32_bf16 v[40:43], a[84:87], v[184:187], v[40:43]\n"
    "ds_read_b64_tr_b16 a[80:81], v208 offset:49152\n"
    "ds_read_b64_tr_b16 a[82:83], v208 offset:57344\n"
    "v_mfma_f32_16x16x32_bf16 v[44:47], a[84:87], v[188:191], v[44:47]\n"
    "v_exp_f32_e64 v171, v147\n"
    "v_mfma_f32_16x16x32_bf16 v[48:51], a[88:91], v[184:187], v[48:51]\n"
    "ds_read_b64_tr_b16 a[84:85], v209 offset:49152\n"
    "ds_read_b64_tr_b16 a[86:87], v209 offset:57344\n"
    "v_mfma_f32_16x16x32_bf16 v[52:55], a[88:91], v[188:191], v[52:55]\n"
    "v_exp_f32_e64 v172, v148\n"
    "v_mfma_f32_16x16x32_bf16 v[56:59], a[92:95], v[184:187], v[56:59]\n"
    "ds_read_b64_tr_b16 a[88:89], v210 offset:49152\n"
    "ds_read_b64_tr_b16 a[90:91], v210 offset:57344\n"
    "v_mfma_f32_16x16x32_bf16 v[60:63], a[92:95], v[188:191], v[60:63]\n"
    "v_exp_f32_e64 v173, v149\n"
    "v_mfma_f32_16x16x32_bf16 v[64:67], a[96:99], v[184:187], v[64:67]\n"
    "ds_read_b64_tr_b16 a[92:93], v211 offset:49152\n"
    "ds_read_b64_tr_b16 a[94:95], v211 offset:57344\n"
    "v_mfma_f32_16x16x32_bf16 v[68:71], a[96:99], v[188:191], v[68:71]\n"
    "v_exp_f32_e64 v174, v150\n"
    "v_mfma_f32_16x16x32_bf16 v[72:75], a[100:103], v[184:187], v[72:75]\n"
    "ds_read_b64_tr_b16 a[96:97], v204 offset:49408\n"
    "ds_read_b64_tr_b16 a[98:99], v204 offset:57600\n"
    "v_mfma_f32_16x16x32_bf16 v[76:79], a[100:103], v[188:191], v[76:79]\n"
    "v_exp_f32_e64 v175, v151\n"
    "v_mfma_f32_16x16x32_bf16 v[80:83], a[104:107], v[184:187], v[80:83]\n"
    "ds_read_b64_tr_b16 a[100:101], v205 offset:49408\n"
    "ds_read_b64_tr_b16 a[102:103], v205 offset:57600\n"
    "v_mfma_f32_16x16x32_bf16 v[84:87], a[104:107], v[188:191], v[84:87]\n"
    "v_exp_f32_e64 v176, v152\n"
    "v_mfma_f32_16x16x32_bf16 v[88:91], a[108:111], v[184:187], v[88:91]\n"
    "ds_read_b64_tr_b16 a[104:105], v206 offset:49408\n"
    "ds_read_b64_tr_b16 a[106:107], v206 offset:57600\n"
    "v_mfma_f32_16x16x32_bf16 v[92:95], a[108:111], v[188:191], v[92:95]\n"
    "v_exp_f32_e64 v177, v153\n"
    "v_mfma_f32_16x16x32_bf16 v[96:99], a[112:115], v[184:187], v[96:99]\n"
    "ds_read_b64_tr_b16 a[108:109], v207 offset:49408\n"
    "ds_read_b64_tr_b16 a[110:111], v207 offset:57600\n"
    "v_mfma_f32_16x16x32_bf16 v[100:103], a[112:115], v[188:191], v[100:103]\n"
    "v_exp_f32_e64 v178, v154\n"
    "v_mfma_f32_16x16x32_bf16 v[104:107], a[116:119], v[184:187], v[104:107]\n"
    "ds_read_b64_tr_b16 a[112:113], v208 offset:49408\n"
    "ds_read_b64_tr_b16 a[114:115], v208 offset:57600\n"
    "v_mfma_f32_16x16x32_bf16 v[108:111], a[116:119], v[188:191], v[108:111]\n"
    "v_exp_f32_e64 v179, v155\n"
    "v_mfma_f32_16x16x32_bf16 v[112:115], a[120:123], v[184:187], v[112:115]\n"
    "ds_read_b64_tr_b16 a[116:117], v209 offset:49408\n"
    "ds_read_b64_tr_b16 a[118:119], v209 offset:57600\n"
    "v_mfma_f32_16x16x32_bf16 v[116:119], a[120:123], v[188:191], v[116:119]\n"
    "v_exp_f32_e64 v180, v156\n"
    "v_mfma_f32_16x16x32_bf16 v[120:123], a[124:127], v[184:187], v[120:123]\n"
    "ds_read_b64_tr_b16 a[120:121], v210 offset:49408\n"
    "ds_read_b64_tr_b16 a[122:123], v210 offset:57600\n"
    "v_mfma_f32_16x16x32_bf16 v[124:127], a[124:127], v[188:191], v[124:127]\n"
    "v_add_f32 v222, v168, v169\n"
    "s_waitcnt lgkmcnt(15)\n"
    "s_cmp_eq_u32 s58, 0\n"
    "s_cbranch_scc1 L_tail\n"
    "v_mfma_f32_16x16x32_bf16 v[128:131], a[0:3], a[128:131], v[160:163]\n"
    "ds_read_b64_tr_b16 a[124:125], v211 offset:49408\n"
    "ds_read_b64_tr_b16 a[126:127], v211 offset:57600\n"
    "v_mfma_f32_16x16x32_bf16 v[136:139], a[0:3], a[132:135], v[164:167]\n"
    "s_waitcnt vmcnt(12)\n"
    "s_barrier\n"
    "v_mfma_f32_16x16x32_bf16 v[132:135], a[4:7], a[128:131], v[160:163]\n"
    "ds_read_b128 a[0:3], v200 offset:16384\n"
    "v_cvt_pk_bf16_f32 v192, v168, v169\n"
    "v_mfma_f32_16x16x32_bf16 v[140:143], a[4:7], a[132:135], v[164:167]\n"
    "v_exp_f32_e64 v181, v157\n"
    "v_mfma_f32_16x16x32_bf16 v[128:131], a[8:11], a[136:139], v[128:131]\n"
    "ds_read_b128 a[4:7], v200 offset:24576\n"
    "s_add_u32 m0, s59, 98304\n"
    "v_mfma_f32_16x16x32_bf16 v[136:139], a[8:11], a[140:143], v[136:139]\n"
    "buffer_load_dwordx4 v216, s[44:47], s57 offen offset:0 lds\n"
    "v_add_f32_e64 v222, v222, v170\n"
    "v_mfma_f32_16x16x32_bf16 v[132:135], a[12:15], a[136:139], v[132:135]\n"
    "ds_read_b128 a[8:11], v201 offset:16384\n"
    "v_cvt_pk_bf16_f32 v193, v170, v171\n"
    "v_mfma_f32_16x16x32_bf16 v[140:143], a[12:15], a[140:143], v[140:143]\n"
    "v_exp_f32_e64 v182, v158\n"
    "v_mfma_f32_16x16x32_bf16 v[128:131], a[16:19], a[144:147], v[128:131]\n"
    "ds_read_b128 a[12:15], v201 offset:24576\n"
    "v_add_f32_e64 v222, v222, v171\n"
    "v_mfma_f32_16x16x32_bf16 v[136:139], a[16:19], a[148:151], v[136:139]\n"
    "buffer_load_dwordx4 v217, s[44:47], s57 offen offset:1024 lds\n"
    "v_add_f32_e64 v222, v222, v172\n"
    "v_mfma_f32_16x16x32_bf16 v[132:135], a[20:23], a[144:147], v[132:135]\n"
    "ds_read_b128 a[16:19], v202 offset:16384\n"
    "v_cvt_pk_bf16_f32 v194, v172, v173\n"
    "v_mfma_f32_16x16x32_bf16 v[140:143], a[20:23], a[148:151], v[140:143]\n"
    "v_exp_f32_e64 v183, v159\n"
    "v_mfma_f32_16x16x32_bf16 v[128:131], a[24:27], a[152:155], v[128:131]\n"
    "ds_read_b128 a[20:23], v202 offset:24576\n"
    "v_add_f32_e64 v222, v222, v173\n"
    "v_mfma_f32_16x16x32_bf16 v[136:139], a[24:27], a[156:159], v[136:139]\n"
    "buffer_load_dwordx4 v218, s[44:47], s57 offen offset:2048 lds\n"
    "v_add_f32_e64 v222, v222, v174\n"
    "v_mfma_f32_16x16x32_bf16 v[132:135], a[28:31], a[152:155], v[132:135]\n"
    "ds_read_b128 a[24:27], v203 offset:16384\n"
    "v_cvt_pk_bf16_f32 v195, v174, v175\n"
    "v_mfma_f32_16x16x32_bf16 v[140:143], a[28:31], a[156:159], v[140:143]\n"
    "v_add_f32 v222, v222, v175\n"
    "v_add_f32 v223, v176, v177\n"
    "v_mfma_f32_16x16x32_bf16 v[128:131], a[32:35], a[160:163], v[128:131]\n"
    "ds_read_b128 a[28:31], v203 offset:24576\n"
    "v_cvt_pk_bf16_f32 v196, v176, v177\n"
    "v_mfma_f32_16x16x32_bf16 v[136:139], a[32:35], a[164:167], v[136:139]\n"
    "buffer_load_dwordx4 v219, s[44:47], s57 offen offset:3072 lds\n"
    "v_add_f32_e64 v223, v223, v178\n"
    "v_mfma_f32_16x16x32_bf16 v[132:135], a[36:39], a[160:163], v[132:135]\n"
    "ds_read_b128 a[32:35], v200 offset:16640\n"
    "v_cvt_pk_bf16_f32 v197, v178, v179\n"
    "v_mfma_f32_16x16x32_bf16 v[140:143], a[36:39], a[164:167], v[140:143]\n"
    "v_add_f32 v223, v223, v179\n"
    "v_add_f32 v223, v223, v180\n"
    "v_mfma_f32_16x16x32_bf16 v[128:131], a[40:43], a[168:171], v[128:131]\n"
    "ds_read_b128 a[36:39], v200 offset:24832\n"
    "s_add_u32 m0, s59, 0\n"
    "v_mfma_f32_16x16x32_bf16 v[136:139], a[40:43], a[172:175], v[136:139]\n"
    "buffer_load_dwordx4 v212, s[40:43], s56 offen offset:0 lds\n"
    "v_cvt_pk_bf16_f32 v198, v180, v181\n"
    "v_mfma_f32_16x16x32_bf16 v[132:135], a[44:47], a[168:171], v[132:135]\n"
    "ds_read_b128 a[40:43], v201 offset:16640\n"
    "v_add_f32 v223, v223, v181\n"
    "v_mfma_f32_16x16x32_bf16 v[140:143], a[44:47], a[172:175], v[140:143]\n"
    "buffer_load_dwordx4 v213, s[40:43], s56 offen offset:1024 lds\n"
    "v_add_f32_e64 v223, v223, v182\n"
    "v_mfma_f32_16x16x32_bf16 v[128:131], a[48:51], a[176:179], v[128:131]\n"
    "ds_read_b128 a[44:47], v201 offset:24832\n"
    "v_cvt_pk_bf16_f32 v199, v182, v183\n"
    "v_mfma_f32_16x16x32_bf16 v[136:139], a[48:51], a[180:183], v[136:139]\n"
    "s_add_u32 s89, s89, 128\n"
    "s_min_u32 s89, s89, s65\n"
    "v_mfma_f32_16x16x32_bf16 v[132:135], a[52:55], a[176:179], v[132:135]\n"
    "ds_read_b128 a[48:51], v202 offset:16640\n"
    "v_add_f32 v223, v223, v183\n"
    "v_mfma_f32_16x16x32_bf16 v[140:143], a[52:55], a[180:183], v[140:143]\n"
    "buffer_load_dwordx4 v214, s[40:43], s56 offen offset:2048 lds\n"
    "v_add_f32_e64 v224, v222, v223\n"
    "v_mfma_f32_16x16x32_bf16 v[128:131], a[56:59], a[184:187], v[128:131]\n"
    "ds_read_b128 a[52:55], v202 offset:24832\n"
    "v_cmp_nle_f32_e64 vcc, v224, s60\n"
    "v_mfma_f32_16x16x32_bf16 v[136:139], a[56:59], a[188:191], v[136:139]\n"
    "buffer_load_dwordx4 v215, s[40:43], s56 offen offset:3072 lds\n"
    "s_cbranch_vccnz L_stub3\n"
    "L_ret3:\n"
    "v_mfma_f32_16x16x32_bf16 v[132:135], a[60:63], a[184:187], v[132:135]\n"
    "ds_read_b128 a[56:59], v203 offset:16640\n"
    "s_add_u32 s57, s57, s77\n"
    "v_mfma_f32_16x16x32_bf16 v[140:143], a[60:63], a[188:191], v[140:143]\n"
    "s_add_u32 s56, s56, s77\n"
    "s_waitcnt lgkmcnt(15)\n"
    "v_mfma_f32_16x16x32_bf16 v[0:3], a[64:67], v[192:195], v[0:3]\n"
    "ds_read_b128 a[60:63], v203 offset:24832\n"
    "v_mfma_f32_16x16x32_bf16 v[4:7], a[64:67], v[196:199], v[4:7]\n"
    "s_waitcnt lgkmcnt(14)\n"
    "v_mfma_f32_16x16x32_bf16 v[8:11], a[68:71], v[192:195], v[8:11]\n"
    "ds_read_b64_tr_b16 a[64:65], v204 offset:0\n"
    "ds_read_b64_tr_b16 a[66:67], v204 offset:8192\n"
    "v_mfma_f32_16x16x32_bf16 v[12:15], a[68:71], v[196:199], v[12:15]\n"
    "v_add_f32 v220, v220, v222\n"
    "v_add_f32_e64 v221, v221, v223\n"
    "v_mfma_f32_16x16x32_bf16 v[16:19], a[72:75], v[192:195], v[16:19]\n"
    "ds_read_b64_tr_b16 a[68:69], v205 offset:0\n"
    "ds_read_b64_tr_b16 a[70:71], v205 offset:8192\n"
    "v_mfma_f32_16x16x32_bf16 v[20:23], a[72:75], v[196:199], v[20:23]\n"
    "v_exp_f32_e64 v168, v128\n"
    "v_mfma_f32_16x16x32_bf16 v[24:27], a[76:79], v[192:195], v[24:27]\n"
    "ds_read_b64_tr_b16 a[72:73], v206 offset:0\n"
    "ds_read_b64_tr_b16 a[74:75], v206 offset:8192\n"
    "v_mfma_f32_16x16x32_bf16 v[28:31], a[76:79], v[196:199], v[28:31]\n"
    "v_exp_f32_e64 v169, v129\n"
    "v_mfma_f32_16x16x32_bf16 v[32:35], a[80:83], v[192:195], v[32:35]\n"
    "ds_read_b64_tr_b16 a[76:77], v207 offset:0\n"
    "ds_read_b64_tr_b16 a[78:79], v207 offset:8192\n"
    "v_mfma_f32_16x16x32_bf16 v[36:39], a[80:83], v[196:199], v[36:39]\n"
    "v_exp_f32_e64 v170, v130\n"
    "v_mfma_f32_16x16x32_bf16 v[40:43], a[84:87], v[192:195], v[40:43]\n"
    "ds_read_b64_tr_b16 a[80:81], v208 offset:0\n"
    "ds_read_b64_tr_b16 a[82:83], v208 offset:8192\n"
    "v_mfma_f32_16x16x32_bf16 v[44:47], a[84:87], v[196:199], v[44:47]\n"
    "v_exp_f32_e64 v171, v131\n"
    "v_mfma_f32_16x16x32_bf16 v[48:51], a[88:91], v[192:195], v[48:51]\n"
    "ds_read_b64_tr_b16 a[84:85], v209 offset:0\n"
    "ds_read_b64_tr_b16 a[86:87], v209 offset:8192\n"
    "v_mfma_f32_16x16x32_bf16 v[52:55], a[88:91], v[196:199], v[52:55]\n"
    "v_exp_f32_e64 v172, v132\n"
    "v_mfma_f32_16x16x32_bf16 v[56:59], a[92:95], v[192:195], v[56:59]\n"
    "ds_read_b64_tr_b16 a[88:89], v210 offset:0\n"
    "ds_read_b64_tr_b16 a[90:91], v210 offset:8192\n"
    "v_mfma_f32_16x16x32_bf16 v[60:63], a[92:95], v[196:199], v[60:63]\n"
    "v_exp_f32_e64 v173, v133\n"
    "v_mfma_f32_16x16x32_bf16 v[64:67], a[96:99], v[192:195], v[64:67]\n"
    "ds_read_b64_tr_b16 a[92:93], v211 offset:0\n"
    "ds_read_b64_tr_b16 a[94:95], v211 offset:8192\n"
    "v_mfma_f32_16x16x32_bf16 v[68:71], a[96:99], v[196:199], v[68:71]\n"
    "v_exp_f32_e64 v174, v134\n"
    "v_mfma_f32_16x16x32_bf16 v[72:75], a[100:103], v[192:195], v[72:75]\n"
    "ds_read_b64_tr_b16 a[96:97], v204 offset:256\n"
    "ds_read_b64_tr_b16 a[98:99], v204 offset:8448\n"
    "v_mfma_f32_16x16x32_bf16 v[76:79], a[100:103], v[196:199], v[76:79]\n"
    "v_exp_f32_e64 v175, v135\n"
    "v_mfma_f32_16x16x32_bf16 v[80:83], a[104:107], v[192:195], v[80:83]\n"
    "ds_read_b64_tr_b16 a[100:101], v205 offset:256\n"
    "ds_read_b64_tr_b16 a[102:103], v205 offset:8448\n"
    "v_mfma_f32_16x16x32_bf16 v[84:87], a[104:107], v[196:199], v[84:87]\n"
    "v_exp_f32_e64 v176, v136\n"
    "v_mfma_f32_16x16x32_bf16 v[88:91], a[108:111], v[192:195], v[88:91]\n"
    "ds_read_b64_tr_b16 a[104:105], v206 offset:256\n"
    "ds_read_b64_tr_b16 a[106:107], v206 offset:8448\n"
    "v_mfma_f32_16x16x32_bf16 v[92:95], a[108:111], v[196:199], v[92:95]\n"
    "v_exp_f32_e64 v177, v137\n"
    "v_mfma_f32_16x16x32_bf16 v[96:99], a[112:115], v[192:195], v[96:99]\n"
    "ds_read_b64_tr_b16 a[108:109], v207 offset:256\n"
    "ds_read_b64_tr_b16 a[110:111], v207 offset:8448\n"
    "v_mfma_f32_16x16x32_bf16 v[100:103], a[112:115], v[196:199], v[100:103]\n"
    "v_exp_f32_e64 v178, v138\n"
    "v_mfma_f32_16x16x32_bf16 v[104:107], a[116:119], v[192:195], v[104:107]\n"
    "ds_read_b64_tr_b16 a[112:113], v208 offset:256\n"
    "ds_read_b64_tr_b16 a[114:115], v208 offset:8448\n"
    "v_mfma_f32_16x16x32_bf16 v[108:111], a[116:119], v[196:199], v[108:111]\n"
    "v_exp_f32_e64 v179, v139\n"
    "v_mfma_f32_16x16x32_bf16 v[112:115], a[120:123], v[192:195], v[112:115]\n"
    "ds_read_b64_tr_b16 a[116:117], v209 offset:256\n"
    "ds_read_b64_tr_b16 a[118:119], v209 offset:8448\n"
    "v_mfma_f32_16x16x32_bf16 v[116:119], a[120:123], v[196:199], v[116:119]\n"
    "v_exp_f32_e64 v180, v140\n"
    "v_mfma_f32_16x16x32_bf16 v[120:123], a[124:127], v[192:195], v[120:123]\n"
    "ds_read_b64_tr_b16 a[120:121], v210 offset:256\n"
    "ds_read_b64_tr_b16 a[122:123], v210 offset:8448\n"
    "v_mfma_f32_16x16x32_bf16 v[124:127], a[124:127], v[196:199], v[124:127]\n"
    "v_add_f32 v222, v168, v169\n"
    "s_waitcnt lgkmcnt(15)\n"
    "s_sub_u32 s58, s58, 1\n"
    "s_branch L_loop\n"
    "L_tail:\n"
    "ds_read_b64_tr_b16 a[124:125], v211 offset:49408\n"
    "ds_read_b64_tr_b16 a[126:127], v211 offset:57600\n"
    "v_exp_f32 v181, v157\n"
    "v_exp_f32 v182, v158\n"
    "v_exp_f32 v183, v159\n"
    "s_nop 0\n"
    "v_cvt_pk_bf16_f32 v192, v168, v169\n"
    "v_add_f32 v222, v222, v170\n"
    "v_cvt_pk_bf16_f32 v193, v170, v171\n"
    "v_add_f32 v222, v222, v171\n"
    "v_add_f32 v222, v222, v172\n"
    "v_cvt_pk_bf16_f32 v194, v172, v173\n"
    "v_add_f32 v222, v222, v173\n"
    "v_add_f32 v222, v222, v174\n"
    "v_cvt_pk_bf16_f32 v195, v174, v175\n"
    "v_add_f32 v222, v222, v175\n"
    "v_add_f32 v223, v176, v177\n"
    "v_cvt_pk_bf16_f32 v196, v176, v177\n"
    "v_add_f32 v223, v223, v178\n"
    "v_cvt_pk_bf16_f32 v197, v178, v179\n"
    "v_add_f32 v223, v223, v179\n"
    "v_add_f32 v223, v223, v180\n"
    "v_cvt_pk_bf16_f32 v198, v180, v181\n"
    "v_add_f32 v223, v223, v181\n"
    "v_add_f32 v223, v223, v182\n"
    "v_cvt_pk_bf16_f32 v199, v182, v183\n"
    "v_add_f32 v223, v223, v183\n"
    "v_add_f32 v224, v222, v223\n"
    "v_cmp_nle_f32_e64 vcc, v224, s60\n"
    "s_cbranch_vccnz L_stub4\n"
    "L_ret4:\n"
    "v_add_f32 v220, v220, v222\n"
    "v_add_f32 v221, v221, v223\n"
    "s_waitcnt vmcnt(16) lgkmcnt(0)\n"
    "s_barrier\n"
    "s_nop 0\n"
    "v_mfma_f32_16x16x32_bf16 v[0:3], a[64:67], v[192:195], v[0:3]\n"
    "v_mov_b32 v225, v220\n"
    "v_mov_b32 v226, v221\n"
    "v_add_u32 v227, 0x20000, v244\n"
    "v_add_u32 v228, 0x20000, v245\n"
    "v_mfma_f32_16x16x32_bf16 v[4:7], a[64:67], v[196:199], v[4:7]\n"
    "v_permlane16_swap_b32 v220, v225\n"
    "v_permlane16_swap_b32 v221, v226\n"
    "v_add_f32 v220, v220, v225\n"
    "v_add_f32 v221, v221, v226\n"
    "v_mfma_f32_16x16x32_bf16 v[8:11], a[68:71], v[192:195], v[8:11]\n"
    "v_mov_b32 v225, v220\n"
    "v_mov_b32 v226, v221\n"
    "ds_read_b64_tr_b16 a[0:1], v227 offset:0\n"
    "ds_read_b64_tr_b16 a[2:3], v227 offset:2048\n"
    "v_mfma_f32_16x16x32_bf16 v[12:15], a[68:71], v[196:199], v[12:15]\n"
    "v_permlane32_swap_b32 v220, v225\n"
    "v_permlane32_swap_b32 v221, v226\n"
    "v_add_f32 v220, v220, v225\n"
    "v_add_f32 v221, v221, v226\n"
    "v_mfma_f32_16x16x32_bf16 v[16:19], a[72:75], v[192:195], v[16:19]\n"
    "v_rcp_f32 v232, v220\n"
    "v_rcp_f32 v233, v221\n"
    "ds_read_b64_tr_b16 a[4:5], v228 offset:0\n"
    "ds_read_b64_tr_b16 a[6:7], v228 offset:2048\n"
    "v_mfma_f32_16x16x32_bf16 v[20:23], a[72:75], v[196:199], v[20:23]\n"
    "v_mfma_f32_16x16x32_bf16 v[24:27], a[76:79], v[192:195], v[24:27]\n"
    "v_mul_f32 v0, v0, v232\n"
    "v_mul_f32 v1, v1, v232\n"
    "v_mul_f32 v2, v2, v232\n"
    "v_mul_f32 v3, v3, v232\n"
    "v_mfma_f32_16x16x32_bf16 v[28:31], a[76:79], v[196:199], v[28:31]\n"
    "v_cvt_pk_bf16_f32 v128, v0, v1\n"
    "v_cvt_pk_bf16_f32 v129, v2, v3\n"
    "v_mul_f32 v8, v8, v232\n"
    "v_mul_f32 v9, v9, v232\n"
    "v_mfma_f32_16x16x32_bf16 v[32:35], a[80:83], v[192:195], v[32:35]\n"
    "v_mul_f32 v10, v10, v232\n"
    "v_mul_f32 v11, v11, v232\n"
    "v_cvt_pk_bf16_f32 v130, v8, v9\n"
    "v_cvt_pk_bf16_f32 v131, v10, v11\n"
    "v_mfma_f32_16x16x32_bf16 v[36:39], a[80:83], v[196:199], v[36:39]\n"
    "ds_write_b128 v246, v[128:131] offset:0\n"
    "v_mul_f32 v4, v4, v233\n"
    "v_mul_f32 v5, v5, v233\n"
    "v_mul_f32_e64 v6, v6, v233\n"
    "v_mfma_f32_16x16x32_bf16 v[40:43], a[84:87], v[192:195], v[40:43]\n"
    "v_mul_f32 v7, v7, v233\n"
    "v_cvt_pk_bf16_f32 v132, v4, v5\n"
    "v_cvt_pk_bf16_f32 v133, v6, v7\n"
    "v_mul_f32 v12, v12, v233\n"
    "v_mfma_f32_16x16x32_bf16 v[44:47], a[84:87], v[196:199], v[44:47]\n"
    "v_mul_f32 v13, v13, v233\n"
    "v_mul_f32 v14, v14, v233\n"
    "v_mul_f32_e64 v15, v15, v233\n"
    "v_cvt_pk_bf16_f32 v134, v12, v13\n"
    "v_mfma_f32_16x16x32_bf16 v[48:51], a[88:91], v[192:195], v[48:51]\n"
    "v_cvt_pk_bf16_f32 v135, v14, v15\n"
    "ds_write_b128 v246, v[132:135] offset:1024\n"
    "ds_read_b64_tr_b16 a[8:9], v227 offset:4096\n"
    "ds_read_b64_tr_b16 a[10:11], v227 offset:6144\n"
    "v_mfma_f32_16x16x32_bf16 v[52:55], a[88:91], v[196:199], v[52:55]\n"
    "ds_read_b64_tr_b16 a[12:13], v228 offset:4096\n"
    "ds_read_b64_tr_b16 a[14:15], v228 offset:6144\n"
    "v_mul_f32 v16, v16, v232\n"
    "v_mul_f32 v17, v17, v232\n"
    "v_mfma_f32_16x16x32_bf16 v[56:59], a[92:95], v[192:195], v[56:59]\n"
    "v_mul_f32 v18, v18, v232\n"
    "v_mul_f32 v19, v19, v232\n"
    "v_cvt_pk_bf16_f32 v136, v16, v17\n"
    "v_cvt_pk_bf16_f32 v137, v18, v19\n"
    "v_mfma_f32_16x16x32_bf16 v[60:63], a[92:95], v[196:199], v[60:63]\n"
    "v_mul_f32 v24, v24, v232\n"
    "v_mul_f32 v25, v25, v232\n"
    "v_mul_f32 v26, v26, v232\n"
    "v_mul_f32 v27, v27, v232\n"
    "v_mfma_f32_16x16x32_bf16 v[64:67], a[96:99], v[192:195], v[64:67]\n"
    "v_cvt_pk_bf16_f32 v138, v24, v25\n"
    "v_cvt_pk_bf16_f32 v139, v26, v27\n"
    "ds_write_b128 v246, v[136:139] offset:2048\n"
    "v_mul_f32_e64 v20, v20, v233\n"
    "v_mfma_f32_16x16x32_bf16 v[68:71], a[96:99], v[196:199], v[68:71]\n"
    "v_mul_f32 v21, v21, v233\n"
    "v_mul_f32 v22, v22, v233\n"
    "v_mul_f32_e64 v23, v23, v233\n"
    "v_cvt_pk_bf16_f32 v140, v20, v21\n"
    "v_mfma_f32_16x16x32_bf16 v[72:75], a[100:103], v[192:195], v[72:75]\n"
    "v_cvt_pk_bf16_f32 v141, v22, v23\n"
    "v_mul_f32 v28, v28, v233\n"
    "v_mul_f32 v29, v29, v233\n"
    "v_mul_f32_e64 v30, v30, v233\n"
    "v_mfma_f32_16x16x32_bf16 v[76:79], a[100:103], v[196:199], v[76:79]\n"
    "v_mul_f32_e64 v31, v31, v233\n"
    "v_cvt_pk_bf16_f32 v142, v28, v29\n"
    "v_cvt_pk_bf16_f32 v143, v30, v31\n"
    "ds_write_b128 v246, v[140:143] offset:3072\n"
    "v_mfma_f32_16x16x32_bf16 v[80:83], a[104:107], v[192:195], v[80:83]\n"
    "ds_read_b64_tr_b16 a[16:17], v227 offset:8192\n"
    "ds_read_b64_tr_b16 a[18:19], v227 offset:10240\n"
    "ds_read_b64_tr_b16 a[20:21], v228 offset:8192\n"
    "ds_read_b64_tr_b16 a[22:23], v228 offset:10240\n"
    "v_mfma_f32_16x16x32_bf16 v[84:87], a[104:107], v[196:199], v[84:87]\n"
    "v_mul_f32 v32, v32, v232\n"
    "v_mul_f32 v33, v33, v232\n"
    "v_mul_f32 v34, v34, v232\n"
    "v_mul_f32 v35, v35, v232\n"
    "v_mfma_f32_16x16x32_bf16 v[88:91], a[108:111], v[192:195], v[88:91]\n"
    "v_cvt_pk_bf16_f32 v144, v32, v33\n"
    "v_cvt_pk_bf16_f32 v145, v34, v35\n"
    "v_mul_f32 v40, v40, v232\n"
    "v_mul_f32 v41, v41, v232\n"
    "v_mfma_f32_16x16x32_bf16 v[92:95], a[108:111], v[196:199], v[92:95]\n"
    "v_mul_f32 v42, v42, v232\n"
    "v_mul_f32 v43, v43, v232\n"
    "v_cvt_pk_bf16_f32 v146, v40, v41\n"
    "v_cvt_pk_bf16_f32 v147, v42, v43\n"
    "v_mfma_f32_16x16x32_bf16 v[96:99], a[112:115], v[192:195], v[96:99]\n"
    "ds_write_b128 v246, v[144:147] offset:4096\n"
    "v_mul_f32 v36, v36, v233\n"
    "v_mul_f32 v37, v37, v233\n"
    "v_mul_f32_e64 v38, v38, v233\n"
    "v_mfma_f32_16x16x32_bf16 v[100:103], a[112:115], v[196:199], v[100:103]\n"
    "v_mul_f32 v39, v39, v233\n"
    "v_cvt_pk_bf16_f32 v148, v36, v37\n"
    "v_cvt_pk_bf16_f32 v149, v38, v39\n"
    "v_mul_f32 v44, v44, v233\n"
    "v_mfma_f32_16x16x32_bf16 v[104:107], a[116:119], v[192:195], v[104:107]\n"
    "v_mul_f32 v45, v45, v233\n"
    "v_mul_f32 v46, v46, v233\n"
    "v_mul_f32_e64 v47, v47, v233\n"
    "v_cvt_pk_bf16_f32 v150, v44, v45\n"
    "v_mfma_f32_16x16x32_bf16 v[108:111], a[116:119], v[196:199], v[108:111]\n"
    "v_cvt_pk_bf16_f32 v151, v46, v47\n"
    "ds_write_b128 v246, v[148:151] offset:5120\n"
    "ds_read_b64_tr_b16 a[24:25], v227 offset:12288\n"
    "ds_read_b64_tr_b16 a[26:27], v227 offset:14336\n"
    "v_mfma_f32_16x16x32_bf16 v[112:115], a[120:123], v[192:195], v[112:115]\n"
    "ds_read_b64_tr_b16 a[28:29], v228 offset:12288\n"
    "ds_read_b64_tr_b16 a[30:31], v228 offset:14336\n"
    "v_mul_f32 v48, v48, v232\n"
    "v_mul_f32 v49, v49, v232\n"
    "v_mfma_f32_16x16x32_bf16 v[116:119], a[120:123], v[196:199], v[116:119]\n"
    "v_mul_f32 v50, v50, v232\n"
    "v_mul_f32 v51, v51, v232\n"
    "v_cvt_pk_bf16_f32 v152, v48, v49\n"
    "v_cvt_pk_bf16_f32 v153, v50, v51\n"
    "v_mfma_f32_16x16x32_bf16 v[120:123], a[124:127], v[192:195], v[120:123]\n"
    "v_mul_f32 v56, v56, v232\n"
    "v_mul_f32 v57, v57, v232\n"
    "v_mul_f32 v58, v58, v232\n"
    "v_mul_f32 v59, v59, v232\n"
    "v_mfma_f32_16x16x32_bf16 v[124:127], a[124:127], v[196:199], v[124:127]\n"
    "v_cvt_pk_bf16_f32 v154, v56, v57\n"
    "v_cvt_pk_bf16_f32 v155, v58, v59\n"
    "ds_write_b128 v246, v[152:155] offset:6144\n"
    "v_mul_f32_e64 v52, v52, v233\n"
    "s_nop 7\n"
    "v_mul_f32 v53, v53, v233\n"
    "v_mul_f32 v54, v54, v233\n"
    "v_mul_f32 v55, v55, v233\n"
    "v_cvt_pk_bf16_f32 v156, v52, v53\n"
    "v_cvt_pk_bf16_f32 v157, v54, v55\n"
    "v_mul_f32 v60, v60, v233\n"
    "v_mul_f32 v61, v61, v233\n"
    "v_mul_f32 v62, v62, v233\n"
    "v_mul_f32 v63, v63, v233\n"
    "v_cvt_pk_bf16_f32 v158, v60, v61\n"
    "v_cvt_pk_bf16_f32 v159, v62, v63\n"
    "ds_write_b128 v246, v[156:159] offset:7168\n"
    "ds_read_b64_tr_b16 a[32:33], v227 offset:16384\n"
    "ds_read_b64_tr_b16 a[34:35], v227 offset:18432\n"
    "ds_read_b64_tr_b16 a[36:37], v228 offset:16384\n"
    "ds_read_b64_tr_b16 a[38:39], v228 offset:18432\n"
    "v_mul_f32 v64, v64, v232\n"
    "v_mul_f32 v65, v65, v232\n"
    "v_mul_f32 v66, v66, v232\n"
    "v_mul_f32 v67, v67, v232\n"
    "v_cvt_pk_bf16_f32 v160, v64, v65\n"
    "v_cvt_pk_bf16_f32 v161, v66, v67\n"
    "v_mul_f32 v72, v72, v232\n"
    "v_mul_f32 v73, v73, v232\n"
    "v_mul_f32 v74, v74, v232\n"
    "v_mul_f32 v75, v75, v232\n"
    "v_cvt_pk_bf16_f32 v162, v72, v73\n"
    "v_cvt_pk_bf16_f32 v163, v74, v75\n"
    "ds_write_b128 v246, v[160:163] offset:8192\n"
    "v_mul_f32 v68, v68, v233\n"
    "v_mul_f32 v69, v69, v233\n"
    "v_mul_f32 v70, v70, v233\n"
    "v_mul_f32 v71, v71, v233\n"
    "v_cvt_pk_bf16_f32 v164, v68, v69\n"
    "v_cvt_pk_bf16_f32 v165, v70, v71\n"
    "v_mul_f32 v76, v76, v233\n"
    "v_mul_f32 v77, v77, v233\n"
    "v_mul_f32 v78, v78, v233\n"
    "v_mul_f32 v79, v79, v233\n"
    "v_cvt_pk_bf16_f32 v166, v76, v77\n"
    "v_cvt_pk_bf16_f32 v167, v78, v79\n"
    "ds_write_b128 v246, v[164:167] offset:9216\n"
    "ds_read_b64_tr_b16 a[40:41], v227 offset:20480\n"
    "ds_read_b64_tr_b16 a[42:43], v227 offset:22528\n"
    "ds_read_b64_tr_b16 a[44:45], v228 offset:20480\n"
    "ds_read_b64_tr_b16 a[46:47], v228 offset:22528\n"
    "v_mul_f32 v80, v80, v232\n"
    "v_mul_f32 v81, v81, v232\n"
    "v_mul_f32 v82, v82, v232\n"
    "v_mul_f32 v83, v83, v232\n"
    "v_cvt_pk_bf16_f32 v168, v80, v81\n"
    "v_cvt_pk_bf16_f32 v169, v82, v83\n"
    "v_mul_f32 v88, v88, v232\n"
    "v_mul_f32 v89, v89, v232\n"
    "v_mul_f32 v90, v90, v232\n"
    "v_mul_f32 v91, v91, v232\n"
    "v_cvt_pk_bf16_f32 v170, v88, v89\n"
    "v_cvt_pk_bf16_f32 v171, v90, v91\n"
    "ds_write_b128 v246, v[168:171] offset:10240\n"
    "v_mul_f32 v84, v84, v233\n"
    "v_mul_f32 v85, v85, v233\n"
    "v_mul_f32 v86, v86, v233\n"
    "v_mul_f32 v87, v87, v233\n"
    "v_cvt_pk_bf16_f32 v172, v84, v85\n"
    "v_cvt_pk_bf16_f32 v173, v86, v87\n"
    "v_mul_f32 v92, v92, v233\n"
    "v_mul_f32 v93, v93, v233\n"
    "v_mul_f32 v94, v94, v233\n"
    "v_mul_f32 v95, v95, v233\n"
    "v_cvt_pk_bf16_f32 v174, v92, v93\n"
    "v_cvt_pk_bf16_f32 v175, v94, v95\n"
    "ds_write_b128 v246, v[172:175] offset:11264\n"
    "ds_read_b64_tr_b16 a[48:49], v227 offset:24576\n"
    "ds_read_b64_tr_b16 a[50:51], v227 offset:26624\n"
    "ds_read_b64_tr_b16 a[52:53], v228 offset:24576\n"
    "ds_read_b64_tr_b16 a[54:55], v228 offset:26624\n"
    "v_mul_f32 v96, v96, v232\n"
    "v_mul_f32 v97, v97, v232\n"
    "v_mul_f32 v98, v98, v232\n"
    "v_mul_f32 v99, v99, v232\n"
    "v_cvt_pk_bf16_f32 v176, v96, v97\n"
    "v_cvt_pk_bf16_f32 v177, v98, v99\n"
    "v_mul_f32 v104, v104, v232\n"
    "v_mul_f32 v105, v105, v232\n"
    "v_mul_f32 v106, v106, v232\n"
    "v_mul_f32 v107, v107, v232\n"
    "v_cvt_pk_bf16_f32 v178, v104, v105\n"
    "v_cvt_pk_bf16_f32 v179, v106, v107\n"
    "ds_write_b128 v246, v[176:179] offset:12288\n"
    "v_mul_f32 v100, v100, v233\n"
    "v_mul_f32 v101, v101, v233\n"
    "v_mul_f32 v102, v102, v233\n"
    "v_mul_f32 v103, v103, v233\n"
    "v_cvt_pk_bf16_f32 v180, v100, v101\n"
    "v_cvt_pk_bf16_f32 v181, v102, v103\n"
    "v_mul_f32 v108, v108, v233\n"
    "v_mul_f32 v109, v109, v233\n"
    "v_mul_f32 v110, v110, v233\n"
    "v_mul_f32 v111, v111, v233\n"
    "v_cvt_pk_bf16_f32 v182, v108, v109\n"
    "v_cvt_pk_bf16_f32 v183, v110, v111\n"
    "ds_write_b128 v246, v[180:183] offset:13312\n"
    "ds_read_b64_tr_b16 a[56:57], v227 offset:28672\n"
    "ds_read_b64_tr_b16 a[58:59], v227 offset:30720\n"
    "ds_read_b64_tr_b16 a[60:61], v228 offset:28672\n"
    "ds_read_b64_tr_b16 a[62:63], v228 offset:30720\n"
    "v_mul_f32 v112, v112, v232\n"
    "v_mul_f32 v113, v113, v232\n"
    "v_mul_f32 v114, v114, v232\n"
    "v_mul_f32 v115, v115, v232\n"
    "v_cvt_pk_bf16_f32 v184, v112, v113\n"
    "v_cvt_pk_bf16_f32 v185, v114, v115\n"
    "v_mul_f32 v120, v120, v232\n"
    "v_mul_f32 v121, v121, v232\n"
    "v_mul_f32 v122, v122, v232\n"
    "v_mul_f32 v123, v123, v232\n"
    "v_cvt_pk_bf16_f32 v186, v120, v121\n"
    "v_cvt_pk_bf16_f32 v187, v122, v123\n"
    "ds_write_b128 v246, v[184:187] offset:14336\n"
    "v_mul_f32 v116, v116, v233\n"
    "v_mul_f32 v117, v117, v233\n"
    "v_mul_f32 v118, v118, v233\n"
    "v_mul_f32 v119, v119, v233\n"
    "v_cvt_pk_bf16_f32 v188, v116, v117\n"
    "v_cvt_pk_bf16_f32 v189, v118, v119\n"
    "v_mul_f32 v124, v124, v233\n"
    "v_mul_f32 v125, v125, v233\n"
    "v_mul_f32 v126, v126, v233\n"
    "v_mul_f32 v127, v127, v233\n"
    "v_cvt_pk_bf16_f32 v190, v124, v125\n"
    "v_cvt_pk_bf16_f32 v191, v126, v127\n"
    "ds_write_b128 v246, v[188:191] offset:15360\n"
    "s_waitcnt vmcnt(4) lgkmcnt(0)\n"
    "s_barrier\n"
    "ds_read_b128 v[0:3], v247 offset:0\n"
    "ds_read_b128 v[4:7], v247 offset:1024\n"
    "ds_read_b128 v[8:11], v247 offset:2048\n"
    "ds_read_b128 v[12:15], v247 offset:3072\n"
    "ds_read_b128 v[16:19], v247 offset:4096\n"
    "ds_read_b128 v[20:23], v247 offset:5120\n"
    "ds_read_b128 v[24:27], v247 offset:6144\n"
    "ds_read_b128 v[28:31], v247 offset:7168\n"
    "ds_read_b128 v[32:35], v247 offset:8192\n"
    "ds_read_b128 v[36:39], v247 offset:9216\n"
    "ds_read_b128 v[40:43], v247 offset:10240\n"
    "ds_read_b128 v[44:47], v247 offset:11264\n"
    "ds_read_b128 v[48:51], v247 offset:12288\n"
    "ds_read_b128 v[52:55], v247 offset:13312\n"
    "ds_read_b128 v[56:59], v247 offset:14336\n"
    "ds_read_b128 v[60:63], v247 offset:15360\n"
    "ds_read_b128 v[128:131], v248 offset:0\n"
    "ds_read_b128 v[132:135], v248 offset:1024\n"
    "ds_read_b128 v[136:139], v248 offset:2048\n"
    "ds_read_b128 v[140:143], v248 offset:3072\n"
    "ds_read_b128 v[144:147], v248 offset:4096\n"
    "ds_read_b128 v[148:151], v248 offset:5120\n"
    "ds_read_b128 v[152:155], v248 offset:6144\n"
    "ds_read_b128 v[156:159], v248 offset:7168\n"
    "ds_read_b128 v[160:163], v248 offset:8192\n"
    "ds_read_b128 v[164:167], v248 offset:9216\n"
    "ds_read_b128 v[168:171], v248 offset:10240\n"
    "ds_read_b128 v[172:175], v248 offset:11264\n"
    "ds_read_b128 v[176:179], v248 offset:12288\n"
    "ds_read_b128 v[180:183], v248 offset:13312\n"
    "ds_read_b128 v[184:187], v248 offset:14336\n"
    "ds_read_b128 v[188:191], v248 offset:15360\n"
    "s_mov_b32 s73, 0x20000\n"
    "s_mov_b32 s72, 0x10000\n"
    "s_mov_b32 s58, 6\n"
    "s_waitcnt lgkmcnt(15)\n"
    "L_oloop:\n"
    "v_mfma_f32_16x16x32_bf16 v[64:67], a[0:3], v[0:3], 0\n"
    "v_add_u32 v104, s72, v244\n"
    "v_add_u32 v105, s72, v245\n"
    "s_add_u32 s74, s80, 229376\n"
    "s_add_u32 s75, s80, 344064\n"
    "v_mfma_f32_16x16x32_bf16 v[68:71], a[0:3], v[4:7], 0\n"
    "s_waitcnt vmcnt(8) lgkmcnt(0)\n"
    "s_barrier\n"
    "v_mfma_f32_16x16x32_bf16 v[72:75], a[4:7], v[0:3], 0\n"
    "ds_read_b64_tr_b16 a[64:65], v104 offset:0\n"
    "ds_read_b64_tr_b16 a[66:67], v104 offset:2048\n"
    "v_cvt_pk_bf16_f32 v96, v80, v81\n"
    "v_cvt_pk_bf16_f32 v97, v82, v83\n"
    "v_cvt_pk_bf16_f32 v98, v88, v89\n"
    "v_mfma_f32_16x16x32_bf16 v[76:79], a[4:7], v[4:7], 0\n"
    "ds_read_b64_tr_b16 a[68:69], v105 offset:0\n"
    "ds_read_b64_tr_b16 a[70:71], v105 offset:2048\n"
    "v_cvt_pk_bf16_f32 v99, v90, v91\n"
    "v_cvt_pk_bf16_f32 v100, v84, v85\n"
    "v_cvt_pk_bf16_f32 v101, v86, v87\n"
    "v_mfma_f32_16x16x32_bf16 v[64:67], a[8:11], v[8:11], v[64:67]\n"
    "ds_read_b64_tr_b16 a[72:73], v104 offset:4096\n"
    "ds_read_b64_tr_b16 a[74:75], v104 offset:6144\n"
    "v_cvt_pk_bf16_f32 v102, v92, v93\n"
    "v_cvt_pk_bf16_f32 v103, v94, v95\n"
    "buffer_store_dwordx4 v[96:99], v249, s[68:71], s78 offen sc1\n"
    "v_mfma_f32_16x16x32_bf16 v[68:71], a[8:11], v[12:15], v[68:71]\n"
    "ds_read_b64_tr_b16 a[76:77], v105 offset:4096\n"
    "ds_read_b64_tr_b16 a[78:79], v105 offset:6144\n"
    "buffer_store_dwordx4 v[100:103], v250, s[68:71], s78 offen sc1\n"
    "s_mov_b32 s78, s79\n"
    "s_add_u32 s79, s79, 2048\n"
    "v_mfma_f32_16x16x32_bf16 v[72:75], a[12:15], v[8:11], v[72:75]\n"
    "ds_read_b64_tr_b16 a[80:81], v104 offset:8192\n"
    "ds_read_b64_tr_b16 a[82:83], v104 offset:10240\n"
    "s_add_u32 m0, s73, s59\n"
    "v_mfma_f32_16x16x32_bf16 v[76:79], a[12:15], v[12:15], v[76:79]\n"
    "buffer_load_dwordx4 v240, s[52:55], s74 offen offset:0 lds\n"
    "ds_read_b64_tr_b16 a[84:85], v105 offset:8192\n"
    "v_mfma_f32_16x16x32_bf16 v[64:67], a[16:19], v[16:19], v[64:67]\n"
    "ds_read_b64_tr_b16 a[86:87], v105 offset:10240\n"
    "ds_read_b64_tr_b16 a[88:89], v104 offset:12288\n"
    "v_mfma_f32_16x16x32_bf16 v[68:71], a[16:19], v[20:23], v[68:71]\n"
    "buffer_load_dwordx4 v241, s[52:55], s74 offen offset:1024 lds\n"
    "ds_read_b64_tr_b16 a[90:91], v104 offset:14336\n"
    "v_mfma_f32_16x16x32_bf16 v[72:75], a[20:23], v[16:19], v[72:75]\n"
    "ds_read_b64_tr_b16 a[92:93], v105 offset:12288\n"
    "ds_read_b64_tr_b16 a[94:95], v105 offset:14336\n"
    "v_mfma_f32_16x16x32_bf16 v[76:79], a[20:23], v[20:23], v[76:79]\n"
    "ds_read_b64_tr_b16 a[96:97], v104 offset:16384\n"
    "ds_read_b64_tr_b16 a[98:99], v104 offset:18432\n"
    "v_mfma_f32_16x16x32_bf16 v[64:67], a[24:27], v[24:27], v[64:67]\n"
    "buffer_load_dwordx4 v242, s[52:55], s74 offen offset:2048 lds\n"
    "ds_read_b64_tr_b16 a[100:101], v105 offset:16384\n"
    "v_mfma_f32_16x16x32_bf16 v[68:71], a[24:27], v[28:31], v[68:71]\n"
    "ds_read_b64_tr_b16 a[102:103], v105 offset:18432\n"
    "ds_read_b64_tr_b16 a[104:105], v104 offset:20480\n"
    "v_mfma_f32_16x16x32_bf16 v[72:75], a[28:31], v[24:27], v[72:75]\n"
    "ds_read_b64_tr_b16 a[106:107], v104 offset:22528\n"
    "ds_read_b64_tr_b16 a[108:109], v105 offset:20480\n"
    "v_mfma_f32_16x16x32_bf16 v[76:79], a[28:31], v[28:31], v[76:79]\n"
    "buffer_load_dwordx4 v243, s[52:55], s74 offen offset:3072 lds\n"
    "ds_read_b64_tr_b16 a[110:111], v105 offset:22528\n"
    "v_mfma_f32_16x16x32_bf16 v[64:67], a[32:35], v[32:35], v[64:67]\n"
    "ds_read_b64_tr_b16 a[112:113], v104 offset:24576\n"
    "ds_read_b64_tr_b16 a[114:115], v104 offset:26624\n"
    "v_mfma_f32_16x16x32_bf16 v[68:71], a[32:35], v[36:39], v[68:71]\n"
    "ds_read_b64_tr_b16 a[116:117], v105 offset:24576\n"
    "s_add_u32 m0, m0, 0x4000\n"
    "v_mfma_f32_16x16x32_bf16 v[72:75], a[36:39], v[32:35], v[72:75]\n"
    "buffer_load_dwordx4 v240, s[52:55], s75 offen offset:0 lds\n"
    "ds_read_b64_tr_b16 a[118:119], v105 offset:26624\n"
    "v_mfma_f32_16x16x32_bf16 v[76:79], a[36:39], v[36:39], v[76:79]\n"
    "ds_read_b64_tr_b16 a[120:121], v104 offset:28672\n"
    "ds_read_b64_tr_b16 a[122:123], v104 offset:30720\n"
    "v_mfma_f32_16x16x32_bf16 v[64:67], a[40:43], v[40:43], v[64:67]\n"
    "ds_read_b64_tr_b16 a[124:125], v105 offset:28672\n"
    "ds_read_b64_tr_b16 a[126:127], v105 offset:30720\n"
    "v_mfma_f32_16x16x32_bf16 v[68:71], a[40:43], v[44:47], v[68:71]\n"
    "buffer_load_dwordx4 v241, s[52:55], s75 offen offset:1024 lds\n"
    "v_mfma_f32_16x16x32_bf16 v[72:75], a[44:47], v[40:43], v[72:75]\n"
    "v_mfma_f32_16x16x32_bf16 v[76:79], a[44:47], v[44:47], v[76:79]\n"
    "v_mfma_f32_16x16x32_bf16 v[64:67], a[48:51], v[48:51], v[64:67]\n"
    "buffer_load_dwordx4 v242, s[52:55], s75 offen offset:2048 lds\n"
    "v_mfma_f32_16x16x32_bf16 v[68:71], a[48:51], v[52:55], v[68:71]\n"
    "v_mfma_f32_16x16x32_bf16 v[72:75], a[52:55], v[48:51], v[72:75]\n"
    "v_mfma_f32_16x16x32_bf16 v[76:79], a[52:55], v[52:55], v[76:79]\n"
    "buffer_load_dwordx4 v243, s[52:55], s75 offen offset:3072 lds\n"
    "v_mfma_f32_16x16x32_bf16 v[64:67], a[56:59], v[56:59], v[64:67]\n"
    "v_mfma_f32_16x16x32_bf16 v[68:71], a[56:59], v[60:63], v[68:71]\n"
    "v_mfma_f32_16x16x32_bf16 v[72:75], a[60:63], v[56:59], v[72:75]\n"
    "v_mfma_f32_16x16x32_bf16 v[76:79], a[60:63], v[60:63], v[76:79]\n"
    "s_mov_b32 s73, s72\n"
    "s_lshr_b32 s72, s72, 1\n"
    "s_cmp_eq_u32 s72, 0x4000\n"
    "s_cselect_b32 s72, 0x20000, s72\n"
    "s_waitcnt lgkmcnt(15)\n"
    "s_nop 0\n"
    "v_mfma_f32_16x16x32_bf16 v[80:83], a[64:67], v[128:131], v[64:67]\n"
    "v_add_u32 v104, s72, v244\n"
    "v_add_u32_e64 v105, s72, v245\n"
    "s_add_u32 s80, s80, 128\n"
    "s_cmp_le_u32 s58, 1\n"
    "s_cselect_b32 s54, 0, s54\n"
    "s_mov_b32 s74, s80\n"
    "s_add_u32 s75, s80, 114688\n"
    "v_mfma_f32_16x16x32_bf16 v[84:87], a[64:67], v[132:135], v[68:71]\n"
    "s_waitcnt vmcnt(10) lgkmcnt(0)\n"
    "s_barrier\n"
    "v_mfma_f32_16x16x32_bf16 v[88:91], a[68:71], v[128:131], v[72:75]\n"
    "ds_read_b64_tr_b16 a[0:1], v104 offset:0\n"
    "ds_read_b64_tr_b16 a[2:3], v104 offset:2048\n"
    "v_mfma_f32_16x16x32_bf16 v[92:95], a[68:71], v[132:135], v[76:79]\n"
    "ds_read_b64_tr_b16 a[4:5], v105 offset:0\n"
    "ds_read_b64_tr_b16 a[6:7], v105 offset:2048\n"
    "v_mfma_f32_16x16x32_bf16 v[80:83], a[72:75], v[136:139], v[80:83]\n"
    "ds_read_b64_tr_b16 a[8:9], v104 offset:4096\n"
    "ds_read_b64_tr_b16 a[10:11], v104 offset:6144\n"
    "s_add_u32 m0, s73, s59\n"
    "v_mfma_f32_16x16x32_bf16 v[84:87], a[72:75], v[140:143], v[84:87]\n"
    "buffer_load_dwordx4 v240, s[52:55], s74 offen offset:0 lds\n"
    "ds_read_b64_tr_b16 a[12:13], v105 offset:4096\n"
    "ds_read_b64_tr_b16 a[14:15], v105 offset:6144\n"
    "v_mfma_f32_16x16x32_bf16 v[88:91], a[76:79], v[136:139], v[88:91]\n"
    "ds_read_b64_tr_b16 a[16:17], v104 offset:8192\n"
    "ds_read_b64_tr_b16 a[18:19], v104 offset:10240\n"
    "v_mfma_f32_16x16x32_bf16 v[92:95], a[76:79], v[140:143], v[92:95]\n"
    "ds_read_b64_tr_b16 a[20:21], v105 offset:8192\n"
    "ds_read_b64_tr_b16 a[22:23], v105 offset:10240\n"
    "v_mfma_f32_16x16x32_bf16 v[80:83], a[80:83], v[144:147], v[80:83]\n"
    "buffer_load_dwordx4 v241, s[52:55], s74 offen offset:1024 lds\n"
    "ds_read_b64_tr_b16 a[24:25], v104 offset:12288\n"
    "v_mfma_f32_16x16x32_bf16 v[84:87], a[80:83], v[148:151], v[84:87]\n"
    "ds_read_b64_tr_b16 a[26:27], v104 offset:14336\n"
    "ds_read_b64_tr_b16 a[28:29], v105 offset:12288\n"
    "v_mfma_f32_16x16x32_bf16 v[88:91], a[84:87], v[144:147], v[88:91]\n"
    "ds_read_b64_tr_b16 a[30:31], v105 offset:14336\n"
    "ds_read_b64_tr_b16 a[32:33], v104 offset:16384\n"
    "v_mfma_f32_16x16x32_bf16 v[92:95], a[84:87], v[148:151], v[92:95]\n"
    "buffer_load_dwordx4 v242, s[52:55], s74 offen offset:2048 lds\n"
    "ds_read_b64_tr_b16 a[34:35], v104 offset:18432\n"
    "v_mfma_f32_16x16x32_bf16 v[80:83], a[88:91], v[152:155], v[80:83]\n"
    "ds_read_b64_tr_b16 a[36:37], v105 offset:16384\n"
    "ds_read_b64_tr_b16 a[38:39], v105 offset:18432\n"
    "v_mfma_f32_16x16x32_bf16 v[84:87], a[88:91], v[156:159], v[84:87]\n"
    "ds_read_b64_tr_b16 a[40:41], v104 offset:20480\n"
    "ds_read_b64_tr_b16 a[42:43], v104 offset:22528\n"
    "v_mfma_f32_16x16x32_bf16 v[88:91], a[92:95], v[152:155], v[88:91]\n"
    "buffer_load_dwordx4 v243, s[52:55], s74 offen offset:3072 lds\n"
    "ds_read_b64_tr_b16 a[44:45], v105 offset:20480\n"
    "v_mfma_f32_16x16x32_bf16 v[92:95], a[92:95], v[156:159], v[92:95]\n"
    "ds_read_b64_tr_b16 a[46:47], v105 offset:22528\n"
    "ds_read_b64_tr_b16 a[48:49], v104 offset:24576\n"
    "v_mfma_f32_16x16x32_bf16 v[80:83], a[96:99], v[160:163], v[80:83]\n"
    "ds_read_b64_tr_b16 a[50:51], v104 offset:26624\n"
    "ds_read_b64_tr_b16 a[52:53], v105 offset:24576\n"
    "v_mfma_f32_16x16x32_bf16 v[84:87], a[96:99], v[164:167], v[84:87]\n"
    "ds_read_b64_tr_b16 a[54:55], v105 offset:26624\n"
    "s_add_u32 m0, m0, 0x4000\n"
    "v_mfma_f32_16x16x32_bf16 v[88:91], a[100:103], v[160:163], v[88:91]\n"
    "buffer_load_dwordx4 v240, s[52:55], s75 offen offset:0 lds\n"
    "ds_read_b64_tr_b16 a[56:57], v104 offset:28672\n"
    "v_mfma_f32_16x16x32_bf16 v[92:95], a[100:103], v[164:167], v[92:95]\n"
    "ds_read_b64_tr_b16 a[58:59], v104 offset:30720\n"
    "ds_read_b64_tr_b16 a[60:61], v105 offset:28672\n"
    "v_mfma_f32_16x16x32_bf16 v[80:83], a[104:107], v[168:171], v[80:83]\n"
    "ds_read_b64_tr_b16 a[62:63], v105 offset:30720\n"
    "v_mfma_f32_16x16x32_bf16 v[84:87], a[104:107], v[172:175], v[84:87]\n"
    "buffer_load_dwordx4 v241, s[52:55], s75 offen offset:1024 lds\n"
    "v_mfma_f32_16x16x32_bf16 v[88:91], a[108:111], v[168:171], v[88:91]\n"
    "v_mfma_f32_16x16x32_bf16 v[92:95], a[108:111], v[172:175], v[92:95]\n"
    "v_mfma_f32_16x16x32_bf16 v[80:83], a[112:115], v[176:179], v[80:83]\n"
    "buffer_load_dwordx4 v242, s[52:55], s75 offen offset:2048 lds\n"
    "v_mfma_f32_16x16x32_bf16 v[84:87], a[112:115], v[180:183], v[84:87]\n"
    "v_mfma_f32_16x16x32_bf16 v[88:91], a[116:119], v[176:179], v[88:91]\n"
    "v_mfma_f32_16x16x32_bf16 v[92:95], a[116:119], v[180:183], v[92:95]\n"
    "buffer_load_dwordx4 v243, s[52:55], s75 offen offset:3072 lds\n"
    "v_mfma_f32_16x16x32_bf16 v[80:83], a[120:123], v[184:187], v[80:83]\n"
    "v_mfma_f32_16x16x32_bf16 v[84:87], a[120:123], v[188:191], v[84:87]\n"
    "v_mfma_f32_16x16x32_bf16 v[88:91], a[124:127], v[184:187], v[88:91]\n"
    "v_mfma_f32_16x16x32_bf16 v[92:95], a[124:127], v[188:191], v[92:95]\n"
    "s_mov_b32 s73, s72\n"
    "s_lshr_b32 s72, s72, 1\n"
    "s_cmp_eq_u32 s72, 0x4000\n"
    "s_cselect_b32 s72, 0x20000, s72\n"
    "s_waitcnt lgkmcnt(15)\n"
    "s_sub_u32 s58, s58, 1\n"
    "s_cmp_lt_i32 s58, 0\n"
    "s_cbranch_scc0 L_oloop\n"
    "v_mbcnt_lo_u32_b32 v213, -1, 0\n"
    "v_mbcnt_hi_u32_b32 v213, -1, v213\n"
    "s_lshl_b32 s82, s67, 10\n"
    "s_add_u32 s82, s82, 32768\n"
    "v_min_u32 v213, 7, v213\n"
    "v_lshlrev_b32 v213, 4, v213\n"
    "v_mov_b32 v212, 0\n"
    "s_cmp_eq_u32 s63, 3\n"
    "s_cselect_b64 exec, -1, 0\n"
    "buffer_load_dword v212, v213, s[68:71], s82 offen sc1\n"
    "s_mov_b64 exec, -1\n"
    "v_cvt_pk_bf16_f32 v96, v80, v81\n"
    "v_cvt_pk_bf16_f32 v97, v82, v83\n"
    "v_cvt_pk_bf16_f32 v98, v88, v89\n"
    "v_cvt_pk_bf16_f32 v99, v90, v91\n"
    "v_cvt_pk_bf16_f32 v100, v84, v85\n"
    "v_cvt_pk_bf16_f32 v101, v86, v87\n"
    "v_cvt_pk_bf16_f32 v102, v92, v93\n"
    "v_cvt_pk_bf16_f32 v103, v94, v95\n"
    "buffer_store_dwordx4 v[96:99], v249, s[68:71], s78 offen sc1\n"
    "buffer_store_dwordx4 v[100:103], v250, s[68:71], s78 offen sc1\n"
    "v_mbcnt_lo_u32_b32 v200, -1, 0\n"
    "v_mbcnt_hi_u32_b32 v200, -1, v200\n"
    "s_lshl_b32 s98, s67, 3\n"
    "s_add_u32 s98, s98, %19\n"
    "s_add_u32 s99, s63, 4\n"
    "s_min_u32 s99, s99, 6\n"
    "v_and_b32 v206, 7, v200\n"
    "v_lshrrev_b32 v207, 3, v200\n"
    "v_lshrrev_b32 v208, 5, v200\n"
    "v_and_b32 v209, 3, v207\n"
    "v_lshlrev_b32 v209, 4, v209\n"
    "v_lshl_add_u32 v209, v208, 7, v209\n"
    "s_lshl_b32 s91, s63, 8\n"
    "s_add_u32 s91, s91, 11776\n"
    "s_lshl_b32 s92, s99, 8\n"
    "s_add_u32 s92, s92, 11776\n"
    "buffer_load_dwordx4 v[128:131], v209, s[68:71], s91 offen offset:0\n"
    "buffer_load_dwordx4 v[132:135], v209, s[68:71], s91 offen offset:64\n"
    "buffer_load_dwordx4 v[136:139], v209, s[68:71], s91 offen offset:1792\n"
    "buffer_load_dwordx4 v[140:143], v209, s[68:71], s91 offen offset:1856\n"
    "buffer_load_dwordx4 v[144:147], v209, s[68:71], s91 offen offset:3584\n"
    "buffer_load_dwordx4 v[148:151], v209, s[68:71], s91 offen offset:3648\n"
    "buffer_load_dwordx4 v[160:163], v209, s[68:71], s92 offen offset:0\n"
    "buffer_load_dwordx4 v[164:167], v209, s[68:71], s92 offen offset:64\n"
    "buffer_load_dwordx4 v[168:171], v209, s[68:71], s92 offen offset:1792\n"
    "buffer_load_dwordx4 v[172:175], v209, s[68:71], s92 offen offset:1856\n"
    "buffer_load_dwordx4 v[176:179], v209, s[68:71], s92 offen offset:3584\n"
    "buffer_load_dwordx4 v[180:183], v209, s[68:71], s92 offen offset:3648\n"
    "v_mul_u32_u24 v210, 896, v206\n"
    "v_lshrrev_b32 v211, 1, v209\n"
    "v_add_u32 v210, v210, v211\n"
    "s_mul_i32 s96, s98, 7168\n"
    "s_lshl_b32 s95, s63, 7\n"
    "s_add_u32 s95, s95, s96\n"
    "s_lshl_b32 s94, s99, 7\n"
    "s_add_u32 s94, s94, s96\n"
    "v_add_u32 v211, s94, v210\n"
    "v_add_u32 v210, s95, v210\n"
    "global_load_dwordx2 v[152:153], v210, %25\n"
    "global_load_dwordx2 v[154:155], v210, %25 offset:32\n"
    "global_load_dwordx2 v[184:185], v211, %25\n"
    "global_load_dwordx2 v[186:187], v211, %25 offset:32\n"
    "v_lshlrev_b32 v204, 8, v207\n"
    "v_lshl_add_u32 v204, v206, 4, v204\n"
    "s_lshl_b32 s96, s99, 11\n"
    "s_cmp_eq_u32 s63, 3\n"
    "s_cselect_b32 s96, 0x7ff00000, s96\n"
    "v_add_u32 v205, s96, v204\n"
    "s_lshl_b32 s96, s63, 11\n"
    "v_add_u32 v204, s96, v204\n"
    "s_lshr_b32 s90, s98, 1\n"
    "s_mul_i32 s90, s90, 14336\n"
    "s_and_b32 s96, s98, 1\n"
    "s_lshl_b32 s96, s96, 7\n"
    "s_add_u32 s90, s90, s96\n"
    "s_add_u32 s90, s90, 4194304\n"
    "s_lshl_b32 s82, s67, 10\n"
    "s_add_u32 s82, s82, 32768\n"
    "s_lshl_b32 s89, s66, 4\n"
    "s_add_u32 s89, s89, s82\n"
    "v_mov_b32 v201, 1\n"
    "v_mov_b32 v202, 0\n"
    "v_min_u32 v200, 7, v200\n"
    "v_lshlrev_b32 v200, 4, v200\n"
    "s_lshl_b32 s97, 1, s66\n"
    "s_mov_b32 s58, 0\n"
    "s_mov_b32 s93, 0\n"
    "s_waitcnt vmcnt(16) lgkmcnt(0)\n"
    "v_cmp_eq_u32 vcc, 1, v212\n"
    "s_and_b32 s94, vcc_lo, 0xff\n"
    "s_or_b32 s94, s94, s97\n"
    "s_cmp_lg_u32 s63, 3\n"
    "s_barrier\n"
    "s_cbranch_scc0 L_w0\n"
    "s_getpc_b64 s[84:85]\n"
    "L_pc2:\n"
    "s_mul_i32 s83, s67, 3\n"
    "s_add_u32 s83, s83, s63\n"
    "s_and_b32 s83, s83, 63\n"
    "s_lshl_b32 s83, s83, 6\n"
    "s_add_u32 s83, s83, L_end-L_pc2\n"
    "s_add_u32 s84, s84, s83\n"
    "s_addc_u32 s85, s85, 0\n"
    "s_load_dword s88, s[84:85], 0x0\n"
    "s_barrier\n"
    "s_branch L_iss\n"
    "L_w0:\n"
    "s_mov_b64 exec, 1\n"
    "buffer_store_dword v201, v202, s[68:71], s89 offen sc1\n"
    "s_mov_b64 exec, -1\n"
    "L_np:\n"
    "buffer_load_dword v212, v200, s[68:71], s82 offen sc1\n"
    "s_cmp_eq_u32 s94, 0xff\n"
    "s_cselect_b32 s96, 0x100, 0\n"
    "s_or_b32 s94, s94, s96\n"
    "v_mov_b32 v203, s94\n"
    "ds_write_b32 v202, v203\n"
    "s_cmp_lg_u32 s96, 0\n"
    "s_waitcnt lgkmcnt(0)\n"
    "s_barrier\n"
    "s_branch L_iss\n"
    "L_poll:\n"
    "s_add_u32 s58, s58, 1\n"
    "s_waitcnt vmcnt(0)\n"
    "v_cmp_eq_u32 vcc, 1, v212\n"
    "s_and_b32 s94, vcc_lo, 0xff\n"
    "s_or_b32 s94, s94, s97\n"
    "s_cmp_ge_u32 s58, 400\n"
    "s_cselect_b32 s96, 0x100, 0\n"
    "s_cmp_eq_u32 s94, 0xff\n"
    "s_cselect_b32 s96, 0x100, s96\n"
    "s_or_b32 s94, s94, s96\n"
    "v_mov_b32 v203, s94\n"
    "ds_write_b32 v202, v203\n"
    "s_cmp_lg_u32 s96, 0\n"
    "s_cbranch_scc1 L_iss\n"
    "s_sleep 1\n"
    "buffer_load_dword v212, v200, s[68:71], s82 offen sc1\n"
    "s_branch L_iss\n"
    "L_rd:\n"
    "s_cmp_eq_u32 s63, 3\n"
    "s_cbranch_scc1 L_poll\n"
    "s_sleep 1\n"
    "ds_read_b32 v203, v202\n"
    "s_waitcnt lgkmcnt(0)\n"
    "v_readfirstlane_b32 s94, v203\n"
    "L_iss:\n"
    "s_andn2_b32 s95, s94, s93\n"
    "s_or_b32 s93, s93, s94\n"
    "s_cmp_eq_u32 s95, 0\n"
    "s_cbranch_scc1 L_rd\n"
    "s_bitcmp1_b32 s95, 0\n"
    "s_cbranch_scc0 L_n0\n"
    "s_add_u32 s96, s90, 0\n"
    "buffer_load_dwordx4 v[0:3], v204, s[68:71], s96 offen sc1\n"
    "buffer_load_dwordx4 v[32:35], v205, s[68:71], s96 offen sc1\n"
    "L_n0:\n"
    "s_bitcmp1_b32 s95, 1\n"
    "s_cbranch_scc0 L_n1\n"
    "s_add_u32 s96, s90, 1835008\n"
    "buffer_load_dwordx4 v[4:7], v204, s[68:71], s96 offen sc1\n"
    "buffer_load_dwordx4 v[36:39], v205, s[68:71], s96 offen sc1\n"
    "L_n1:\n"
    "s_bitcmp1_b32 s95, 2\n"
    "s_cbranch_scc0 L_n2\n"
    "s_add_u32 s96, s90, 3670016\n"
    "buffer_load_dwordx4 v[8:11], v204, s[68:71], s96 offen sc1\n"
    "buffer_load_dwordx4 v[40:43], v205, s[68:71], s96 offen sc1\n"
    "L_n2:\n"
    "s_bitcmp1_b32 s95, 3\n"
    "s_cbranch_scc0 L_n3\n"
    "s_add_u32 s96, s90, 5505024\n"
    "buffer_load_dwordx4 v[12:15], v204, s[68:71], s96 offen sc1\n"
    "buffer_load_dwordx4 v[44:47], v205, s[68:71], s96 offen sc1\n"
    "L_n3:\n"
    "s_bitcmp1_b32 s95, 4\n"
    "s_cbranch_scc0 L_n4\n"
    "s_add_u32 s96, s90, 7340032\n"
    "buffer_load_dwordx4 v[16:19], v204, s[68:71], s96 offen sc1\n"
    "buffer_load_dwordx4 v[48:51], v205, s[68:71], s96 offen sc1\n"
    "L_n4:\n"
    "s_bitcmp1_b32 s95, 5\n"
    "s_cbranch_scc0 L_n5\n"
    "s_add_u32 s96, s90, 9175040\n"
    "buffer_load_dwordx4 v[20:23], v204, s[68:71], s96 offen sc1\n"
    "buffer_load_dwordx4 v[52:55], v205, s[68:71], s96 offen sc1\n"
    "L_n5:\n"
    "s_bitcmp1_b32 s95, 6\n"
    "s_cbranch_scc0 L_n6\n"
    "s_add_u32 s96, s90, 11010048\n"
    "buffer_load_dwordx4 v[24:27], v204, s[68:71], s96 offen sc1\n"
    "buffer_load_dwordx4 v[56:59], v205, s[68:71], s96 offen sc1\n"
    "L_n6:\n"
    "s_bitcmp1_b32 s95, 7\n"
    "s_cbranch_scc0 L_n7\n"
    "s_add_u32 s96, s90, 12845056\n"
    "buffer_load_dwordx4 v[28:31], v204, s[68:71], s96 offen sc1\n"
    "buffer_load_dwordx4 v[60:63], v205, s[68:71], s96 offen sc1\n"
    "L_n7:\n"
    "s_bitcmp1_b32 s94, 8\n"
    "s_cbranch_scc0 L_rd\n"
    "s_cmp_eq_u32 s94, 0x1ff\n"
    "s_waitcnt vmcnt(0) lgkmcnt(0)\n"
    "s_cbranch_scc1 L_end\n"
    "s_barrier\n"
    "s_andn2_b32 s94, 0xff, s94\n"
    "s_ff1_i32_b32 s66, s94\n"
    "s_min_u32 s66, s66, 7\n"
    "s_branch L_K1\n"
    "L_patch:\n"
    "s_mov_b32 s40, s52\n"
    "s_mov_b32 s44, s52\n"
    "v_mov_b32 v212, v240\n"
    "v_mov_b32 v216, v240\n"
    "s_mov_b32 s41, s53\n"
    "s_mov_b32 s45, s53\n"
    "v_mov_b32 v213, v241\n"
    "v_mov_b32 v217, v241\n"
    "s_mov_b32 s42, s54\n"
    "s_mov_b32 s46, s54\n"
    "v_mov_b32 v214, v242\n"
    "v_mov_b32 v218, v242\n"
    "s_mov_b32 s43, s55\n"
    "s_mov_b32 s47, s55\n"
    "v_mov_b32 v215, v243\n"
    "v_mov_b32 v219, v243\n"
    "s_add_u32 s57, s80, 229248\n"
    "s_mov_b32 s56, s80\n"
    "s_mov_b32 s77, 114688\n"
    "s_branch L_patchret\n"
    "L_stub0:\n"
    "s_mov_b32 s62, 0\n"
    "s_branch L_rare0\n"
    "L_stub1:\n"
    "s_mov_b32 s62, 1\n"
    "s_branch L_rare1\n"
    "L_stub2:\n"
    "s_mov_b32 s62, 2\n"
    "s_branch L_rare0\n"
    "L_stub3:\n"
    "s_mov_b32 s62, 3\n"
    "s_branch L_rare1\n"
    "L_stub4:\n"
    "s_mov_b32 s62, 4\n"
    "s_branch L_rare1\n"
    "L_rare0:\n"
    "s_nop 7\n"
    "s_nop 3\n"
    "v_max3_f32 v225, v128, v129, v130\n"
    "v_max3_f32 v227, v131, v132, v133\n"
    "v_max3_f32 v225, v225, v134, v135\n"
    "v_max_f32 v225, v225, v227\n"
    "v_max3_f32 v226, v136, v137, v138\n"
    "v_max3_f32 v228, v139, v140, v141\n"
    "v_max3_f32 v226, v226, v142, v143\n"
    "v_max_f32 v226, v226, v228\n"
    "v_mov_b32 v227, v225\n"
    "v_mov_b32 v228, v226\n"
    "s_nop 1\n"
    "v_permlane16_swap_b32 v225, v227\n"
    "v_permlane16_swap_b32 v226, v228\n"
    "v_max_f32 v225, v225, v227\n"
    "v_max_f32 v226, v226, v228\n"
    "v_mov_b32 v227, v225\n"
    "v_mov_b32 v228, v226\n"
    "s_nop 1\n"
    "v_permlane32_swap_b32 v225, v227\n"
    "v_permlane32_swap_b32 v226, v228\n"
    "v_max_f32 v225, v225, v227\n"
    "v_max_f32 v226, v226, v228\n"
    "v_add_f32 v225, s61, v225\n"
    "v_max_f32 v225, 0, v225\n"
    "v_exp_f32_e64 v232, -v225\n"
    "v_add_f32 v226, s61, v226\n"
    "v_max_f32 v226, 0, v226\n"
    "v_exp_f32_e64 v233, -v226\n"
    "v_sub_f32 v128, v128, v225\n"
    "v_sub_f32 v129, v129, v225\n"
    "v_sub_f32 v130, v130, v225\n"
    "v_sub_f32 v131, v131, v225\n"
    "v_sub_f32 v132, v132, v225\n"
    "v_sub_f32 v133, v133, v225\n"
    "v_sub_f32 v134, v134, v225\n"
    "v_sub_f32 v135, v135, v225\n"
    "v_sub_f32 v144, v144, v225\n"
    "v_sub_f32 v145, v145, v225\n"
    "v_sub_f32 v146, v146, v225\n"
    "v_sub_f32 v147, v147, v225\n"
    "v_sub_f32 v148, v148, v225\n"
    "v_sub_f32 v149, v149, v225\n"
    "v_sub_f32 v150, v150, v225\n"
    "v_sub_f32 v151, v151, v225\n"
    "v_sub_f32 v160, v160, v225\n"
    "v_sub_f32 v161, v161, v225\n"
    "v_sub_f32 v162, v162, v225\n"
    "v_sub_f32 v163, v163, v225\n"
    "v_mul_f32 v220, v220, v232\n"
    "v_sub_f32 v136, v136, v226\n"
    "v_sub_f32 v137, v137, v226\n"
    "v_sub_f32 v138, v138, v226\n"
    "v_sub_f32 v139, v139, v226\n"
    "v_sub_f32 v140, v140, v226\n"
    "v_sub_f32 v141, v141, v226\n"
    "v_sub_f32 v142, v142, v226\n"
    "v_sub_f32 v143, v143, v226\n"
    "v_sub_f32 v152, v152, v226\n"
    "v_sub_f32 v153, v153, v226\n"
    "v_sub_f32 v154, v154, v226\n"
    "v_sub_f32 v155, v155, v226\n"
    "v_sub_f32 v156, v156, v226\n"
    "v_sub_f32 v157, v157, v226\n"
    "v_sub_f32 v158, v158, v226\n"
    "v_sub_f32 v159, v159, v226\n"
    "v_sub_f32 v164, v164, v226\n"
    "v_sub_f32 v165, v165, v226\n"
    "v_sub_f32 v166, v166, v226\n"
    "v_sub_f32 v167, v167, v226\n"
    "v_mul_f32 v221, v221, v233\n"
    "v_pk_mul_f32 v[0:1], v[0:1], v[232:233] op_sel:[0,0] op_sel_hi:[1,0]\n"
    "v_pk_mul_f32 v[2:3], v[2:3], v[232:233] op_sel:[0,0] op_sel_hi:[1,0]\n"
    "v_pk_mul_f32 v[4:5], v[4:5], v[232:233] op_sel:[0,1] op_sel_hi:[1,1]\n"
    "v_pk_mul_f32 v[6:7], v[6:7], v[232:233] op_sel:[0,1] op_sel_hi:[1,1]\n"
    "v_pk_mul_f32 v[8:9], v[8:9], v[232:233] op_sel:[0,0] op_sel_hi:[1,0]\n"
    "v_pk_mul_f32 v[10:11], v[10:11], v[232:233] op_sel:[0,0] op_sel_hi:[1,0]\n"
    "v_pk_mul_f32 v[12:13], v[12:13], v[232:233] op_sel:[0,1] op_sel_hi:[1,1]\n"
    "v_pk_mul_f32 v[14:15], v[14:15], v[232:233] op_sel:[0,1] op_sel_hi:[1,1]\n"
    "v_pk_mul_f32 v[16:17], v[16:17], v[232:233] op_sel:[0,0] op_sel_hi:[1,0]\n"
    "v_pk_mul_f32 v[18:19], v[18:19], v[232:233] op_sel:[0,0] op_sel_hi:[1,0]\n"
    "v_pk_mul_f32 v[20:21], v[20:21], v[232:233] op_sel:[0,1] op_sel_hi:[1,1]\n"
    "v_pk_mul_f32 v[22:23], v[22:23], v[232:233] op_sel:[0,1] op_sel_hi:[1,1]\n"
    "v_pk_mul_f32 v[24:25], v[24:25], v[232:233] op_sel:[0,0] op_sel_hi:[1,0]\n"
    "v_pk_mul_f32 v[26:27], v[26:27], v[232:233] op_sel:[0,0] op_sel_hi:[1,0]\n"
    "v_pk_mul_f32 v[28:29], v[28:29], v[232:233] op_sel:[0,1] op_sel_hi:[1,1]\n"
    "v_pk_mul_f32 v[30:31], v[30:31], v[232:233] op_sel:[0,1] op_sel_hi:[1,1]\n"
    "v_pk_mul_f32 v[32:33], v[32:33], v[232:233] op_sel:[0,0] op_sel_hi:[1,0]\n"
    "v_pk_mul_f32 v[34:35], v[34:35], v[232:233] op_sel:[0,0] op_sel_hi:[1,0]\n"
    "v_pk_mul_f32 v[36:37], v[36:37], v[232:233] op_sel:[0,1] op_sel_hi:[1,1]\n"
    "v_pk_mul_f32 v[38:39], v[38:39], v[232:233] op_sel:[0,1] op_sel_hi:[1,1]\n"
    "v_pk_mul_f32 v[40:41], v[40:41], v[232:233] op_sel:[0,0] op_sel_hi:[1,0]\n"
    "v_pk_mul_f32 v[42:43], v[42:43], v[232:233] op_sel:[0,0] op_sel_hi:[1,0]\n"
    "v_pk_mul_f32 v[44:45], v[44:45], v[232:233] op_sel:[0,1] op_sel_hi:[1,1]\n"
    "v_pk_mul_f32 v[46:47], v[46:47], v[232:233] op_sel:[0,1] op_sel_hi:[1,1]\n"
    "v_pk_mul_f32 v[48:49], v[48:49], v[232:233] op_sel:[0,0] op_sel_hi:[1,0]\n"
    "v_pk_mul_f32 v[50:51], v[50:51], v[232:233] op_sel:[0,0] op_sel_hi:[1,0]\n"
    "v_pk_mul_f32 v[52:53], v[52:53], v[232:233] op_sel:[0,1] op_sel_hi:[1,1]\n"
    "v_pk_mul_f32 v[54:55], v[54:55], v[232:233] op_sel:[0,1] op_sel_hi:[1,1]\n"
    "v_pk_mul_f32 v[56:57], v[56:57], v[232:233] op_sel:[0,0] op_sel_hi:[1,0]\n"
    "v_pk_mul_f32 v[58:59], v[58:59], v[232:233] op_sel:[0,0] op_sel_hi:[1,0]\n"
    "v_pk_mul_f32 v[60:61], v[60:61], v[232:233] op_sel:[0,1] op_sel_hi:[1,1]\n"
    "v_pk_mul_f32 v[62:63], v[62:63], v[232:233] op_sel:[0,1] op_sel_hi:[1,1]\n"
    "v_pk_mul_f32 v[64:65], v[64:65], v[232:233] op_sel:[0,0] op_sel_hi:[1,0]\n"
    "v_pk_mul_f32 v[66:67], v[66:67], v[232:233] op_sel:[0,0] op_sel_hi:[1,0]\n"
    "v_pk_mul_f32 v[68:69], v[68:69], v[232:233] op_sel:[0,1] op_sel_hi:[1,1]\n"
    "v_pk_mul_f32 v[70:71], v[70:71], v[232:233] op_sel:[0,1] op_sel_hi:[1,1]\n"
    "v_pk_mul_f32 v[72:73], v[72:73], v[232:233] op_sel:[0,0] op_sel_hi:[1,0]\n"
    "v_pk_mul_f32 v[74:75], v[74:75], v[232:233] op_sel:[0,0] op_sel_hi:[1,0]\n"
    "v_pk_mul_f32 v[76:77], v[76:77], v[232:233] op_sel:[0,1] op_sel_hi:[1,1]\n"
    "v_pk_mul_f32 v[78:79], v[78:79], v[232:233] op_sel:[0,1] op_sel_hi:[1,1]\n"
    "v_pk_mul_f32 v[80:81], v[80:81], v[232:233] op_sel:[0,0] op_sel_hi:[1,0]\n"
    "v_pk_mul_f32 v[82:83], v[82:83], v[232:233] op_sel:[0,0] op_sel_hi:[1,0]\n"
    "v_pk_mul_f32 v[84:85], v[84:85], v[232:233] op_sel:[0,1] op_sel_hi:[1,1]\n"
    "v_pk_mul_f32 v[86:87], v[86:87], v[232:233] op_sel:[0,1] op_sel_hi:[1,1]\n"
    "v_pk_mul_f32 v[88:89], v[88:89], v[232:233] op_sel:[0,0] op_sel_hi:[1,0]\n"
    "v_pk_mul_f32 v[90:91], v[90:91], v[232:233] op_sel:[0,0] op_sel_hi:[1,0]\n"
    "v_pk_mul_f32 v[92:93], v[92:93], v[232:233] op_sel:[0,1] op_sel_hi:[1,1]\n"
    "v_pk_mul_f32 v[94:95], v[94:95], v[232:233] op_sel:[0,1] op_sel_hi:[1,1]\n"
    "v_pk_mul_f32 v[96:97], v[96:97], v[232:233] op_sel:[0,0] op_sel_hi:[1,0]\n"
    "v_pk_mul_f32 v[98:99], v[98:99], v[232:233] op_sel:[0,0] op_sel_hi:[1,0]\n"
    "v_pk_mul_f32 v[100:101], v[100:101], v[232:233] op_sel:[0,1] op_sel_hi:[1,1]\n"
    "v_pk_mul_f32 v[102:103], v[102:103], v[232:233] op_sel:[0,1] op_sel_hi:[1,1]\n"
    "v_pk_mul_f32 v[104:105], v[104:105], v[232:233] op_sel:[0,0] op_sel_hi:[1,0]\n"
    "v_pk_mul_f32 v[106:107], v[106:107], v[232:233] op_sel:[0,0] op_sel_hi:[1,0]\n"
    "v_pk_mul_f32 v[108:109], v[108:109], v[232:233] op_sel:[0,1] op_sel_hi:[1,1]\n"
    "v_pk_mul_f32 v[110:111], v[110:111], v[232:233] op_sel:[0,1] op_sel_hi:[1,1]\n"
    "v_pk_mul_f32 v[112:113], v[112:113], v[232:233] op_sel:[0,0] op_sel_hi:[1,0]\n"
    "v_pk_mul_f32 v[114:115], v[114:115], v[232:233] op_sel:[0,0] op_sel_hi:[1,0]\n"
    "v_pk_mul_f32 v[116:117], v[116:117], v[232:233] op_sel:[0,1] op_sel_hi:[1,1]\n"
    "v_pk_mul_f32 v[118:119], v[118:119], v[232:233] op_sel:[0,1] op_sel_hi:[1,1]\n"
    "v_pk_mul_f32 v[120:121], v[120:121], v[232:233] op_sel:[0,0] op_sel_hi:[1,0]\n"
    "v_pk_mul_f32 v[122:123], v[122:123], v[232:233] op_sel:[0,0] op_sel_hi:[1,0]\n"
    "v_pk_mul_f32 v[124:125], v[124:125], v[232:233] op_sel:[0,1] op_sel_hi:[1,1]\n"
    "v_pk_mul_f32 v[126:127], v[126:127], v[232:233] op_sel:[0,1] op_sel_hi:[1,1]\n"
    "v_exp_f32 v168, v128\n"
    "v_exp_f32 v169, v129\n"
    "v_exp_f32 v170, v130\n"
    "v_exp_f32 v171, v131\n"
    "v_exp_f32 v172, v132\n"
    "v_exp_f32 v173, v133\n"
    "v_exp_f32 v174, v134\n"
    "v_exp_f32 v175, v135\n"
    "v_exp_f32 v176, v136\n"
    "v_exp_f32 v177, v137\n"
    "v_exp_f32 v178, v138\n"
    "v_exp_f32 v179, v139\n"
    "v_exp_f32 v180, v140\n"
    "v_exp_f32 v181, v141\n"
    "v_exp_f32 v182, v142\n"
    "v_exp_f32 v183, v143\n"
    "s_nop 0\n"
    "v_add_f32 v222, v168, v169\n"
    "v_cvt_pk_bf16_f32 v184, v168, v169\n"
    "v_add_f32 v222, v222, v170\n"
    "v_cvt_pk_bf16_f32 v185, v170, v171\n"
    "v_add_f32 v222, v222, v171\n"
    "v_add_f32 v222, v222, v172\n"
    "v_cvt_pk_bf16_f32 v186, v172, v173\n"
    "v_add_f32 v222, v222, v173\n"
    "v_add_f32 v222, v222, v174\n"
    "v_cvt_pk_bf16_f32 v187, v174, v175\n"
    "v_add_f32 v222, v222, v175\n"
    "v_add_f32 v223, v176, v177\n"
    "v_cvt_pk_bf16_f32 v188, v176, v177\n"
    "v_add_f32 v223, v223, v178\n"
    "v_cvt_pk_bf16_f32 v189, v178, v179\n"
    "v_add_f32 v223, v223, v179\n"
    "v_add_f32 v223, v223, v180\n"
    "v_cvt_pk_bf16_f32 v190, v180, v181\n"
    "v_add_f32 v223, v223, v181\n"
    "v_add_f32 v223, v223, v182\n"
    "v_cvt_pk_bf16_f32 v191, v182, v183\n"
    "v_add_f32 v223, v223, v183\n"
    "v_add_f32 v224, v222, v223\n"
    "s_nop 1\n"
    "s_cmp_eq_u32 s62, 0\n"
    "s_cbranch_scc1 L_ret0\n"
    "s_branch L_ret2\n"
    "L_rare1:\n"
    "s_nop 7\n"
    "s_nop 3\n"
    "v_max3_f32 v225, v144, v145, v146\n"
    "v_max3_f32 v227, v147, v148, v149\n"
    "v_max3_f32 v225, v225, v150, v151\n"
    "v_max_f32 v225, v225, v227\n"
    "v_max3_f32 v226, v152, v153, v154\n"
    "v_max3_f32 v228, v155, v156, v157\n"
    "v_max3_f32 v226, v226, v158, v159\n"
    "v_max_f32 v226, v226, v228\n"
    "v_mov_b32 v227, v225\n"
    "v_mov_b32 v228, v226\n"
    "s_nop 1\n"
    "v_permlane16_swap_b32 v225, v227\n"
    "v_permlane16_swap_b32 v226, v228\n"
    "v_max_f32 v225, v225, v227\n"
    "v_max_f32 v226, v226, v228\n"
    "v_mov_b32 v227, v225\n"
    "v_mov_b32 v228, v226\n"
    "s_nop 1\n"
    "v_permlane32_swap_b32 v225, v227\n"
    "v_permlane32_swap_b32 v226, v228\n"
    "v_max_f32 v225, v225, v227\n"
    "v_max_f32 v226, v226, v228\n"
    "v_add_f32 v225, s61, v225\n"
    "v_max_f32 v225, 0, v225\n"
    "v_exp_f32_e64 v232, -v225\n"
    "v_add_f32 v226, s61, v226\n"
    "v_max_f32 v226, 0, v226\n"
    "v_exp_f32_e64 v233, -v226\n"
    "v_sub_f32 v144, v144, v225\n"
    "v_sub_f32 v145, v145, v225\n"
    "v_sub_f32 v146, v146, v225\n"
    "v_sub_f32 v147, v147, v225\n"
    "v_sub_f32 v148, v148, v225\n"
    "v_sub_f32 v149, v149, v225\n"
    "v_sub_f32 v150, v150, v225\n"
    "v_sub_f32 v151, v151, v225\n"
    "v_sub_f32 v128, v128, v225\n"
    "v_sub_f32 v129, v129, v225\n"
    "v_sub_f32 v130, v130, v225\n"
    "v_sub_f32 v131, v131, v225\n"
    "v_sub_f32 v132, v132, v225\n"
    "v_sub_f32 v133, v133, v225\n"
    "v_sub_f32 v134, v134, v225\n"
    "v_sub_f32 v135, v135, v225\n"
    "v_sub_f32 v160, v160, v225\n"
    "v_sub_f32 v161, v161, v225\n"
    "v_sub_f32 v162, v162, v225\n"
    "v_sub_f32 v163, v163, v225\n"
    "v_mul_f32 v220, v220, v232\n"
    "v_sub_f32 v152, v152, v226\n"
    "v_sub_f32 v153, v153, v226\n"
    "v_sub_f32 v154, v154, v226\n"
    "v_sub_f32 v155, v155, v226\n"
    "v_sub_f32 v156, v156, v226\n"
    "v_sub_f32 v157, v157, v226\n"
    "v_sub_f32 v158, v158, v226\n"
    "v_sub_f32 v159, v159, v226\n"
    "v_sub_f32 v136, v136, v226\n"
    "v_sub_f32 v137, v137, v226\n"
    "v_sub_f32 v138, v138, v226\n"
    "v_sub_f32 v139, v139, v226\n"
    "v_sub_f32 v140, v140, v226\n"
    "v_sub_f32 v141, v141, v226\n"
    "v_sub_f32 v142, v142, v226\n"
    "v_sub_f32 v143, v143, v226\n"
    "v_sub_f32 v164, v164, v226\n"
    "v_sub_f32 v165, v165, v226\n"
    "v_sub_f32 v166, v166, v226\n"
    "v_sub_f32 v167, v167, v226\n"
    "v_mul_f32 v221, v221, v233\n"
    "v_pk_mul_f32 v[0:1], v[0:1], v[232:233] op_sel:[0,0] op_sel_hi:[1,0]\n"
    "v_pk_mul_f32 v[2:3], v[2:3], v[232:233] op_sel:[0,0] op_sel_hi:[1,0]\n"
    "v_pk_mul_f32 v[4:5], v[4:5], v[232:233] op_sel:[0,1] op_sel_hi:[1,1]\n"
    "v_pk_mul_f32 v[6:7], v[6:7], v[232:233] op_sel:[0,1] op_sel_hi:[1,1]\n"
    "v_pk_mul_f32 v[8:9], v[8:9], v[232:233] op_sel:[0,0] op_sel_hi:[1,0]\n"
    "v_pk_mul_f32 v[10:11], v[10:11], v[232:233] op_sel:[0,0] op_sel_hi:[1,0]\n"
    "v_pk_mul_f32 v[12:13], v[12:13], v[232:233] op_sel:[0,1] op_sel_hi:[1,1]\n"
    "v_pk_mul_f32 v[14:15], v[14:15], v[232:233] op_sel:[0,1] op_sel_hi:[1,1]\n"
    "v_pk_mul_f32 v[16:17], v[16:17], v[232:233] op_sel:[0,0] op_sel_hi:[1,0]\n"
    "v_pk_mul_f32 v[18:19], v[18:19], v[232:233] op_sel:[0,0] op_sel_hi:[1,0]\n"
    "v_pk_mul_f32 v[20:21], v[20:21], v[232:233] op_sel:[0,1] op_sel_hi:[1,1]\n"
    "v_pk_mul_f32 v[22:23], v[22:23], v[232:233] op_sel:[0,1] op_sel_hi:[1,1]\n"
    "v_pk_mul_f32 v[24:25], v[24:25], v[232:233] op_sel:[0,0] op_sel_hi:[1,0]\n"
    "v_pk_mul_f32 v[26:27], v[26:27], v[232:233] op_sel:[0,0] op_sel_hi:[1,0]\n"
    "v_pk_mul_f32 v[28:29], v[28:29], v[232:233] op_sel:[0,1] op_sel_hi:[1,1]\n"
    "v_pk_mul_f32 v[30:31], v[30:31], v[232:233] op_sel:[0,1] op_sel_hi:[1,1]\n"
    "v_pk_mul_f32 v[32:33], v[32:33], v[232:233] op_sel:[0,0] op_sel_hi:[1,0]\n"
    "v_pk_mul_f32 v[34:35], v[34:35], v[232:233] op_sel:[0,0] op_sel_hi:[1,0]\n"
    "v_pk_mul_f32 v[36:37], v[36:37], v[232:233] op_sel:[0,1] op_sel_hi:[1,1]\n"
    "v_pk_mul_f32 v[38:39], v[38:39], v[232:233] op_sel:[0,1] op_sel_hi:[1,1]\n"
    "v_pk_mul_f32 v[40:41], v[40:41], v[232:233] op_sel:[0,0] op_sel_hi:[1,0]\n"
    "v_pk_mul_f32 v[42:43], v[42:43], v[232:233] op_sel:[0,0] op_sel_hi:[1,0]\n"
    "v_pk_mul_f32 v[44:45], v[44:45], v[232:233] op_sel:[0,1] op_sel_hi:[1,1]\n"
    "v_pk_mul_f32 v[46:47], v[46:47], v[232:233] op_sel:[0,1] op_sel_hi:[1,1]\n"
    "v_pk_mul_f32 v[48:49], v[48:49], v[232:233] op_sel:[0,0] op_sel_hi:[1,0]\n"
    "v_pk_mul_f32 v[50:51], v[50:51], v[232:233] op_sel:[0,0] op_sel_hi:[1,0]\n"
    "v_pk_mul_f32 v[52:53], v[52:53], v[232:233] op_sel:[0,1] op_sel_hi:[1,1]\n"
    "v_pk_mul_f32 v[54:55], v[54:55], v[232:233] op_sel:[0,1] op_sel_hi:[1,1]\n"
    "v_pk_mul_f32 v[56:57], v[56:57], v[232:233] op_sel:[0,0] op_sel_hi:[1,0]\n"
    "v_pk_mul_f32 v[58:59], v[58:59], v[232:233] op_sel:[0,0] op_sel_hi:[1,0]\n"
    "v_pk_mul_f32 v[60:61], v[60:61], v[232:233] op_sel:[0,1] op_sel_hi:[1,1]\n"
    "v_pk_mul_f32 v[62:63], v[62:63], v[232:233] op_sel:[0,1] op_sel_hi:[1,1]\n"
    "v_pk_mul_f32 v[64:65], v[64:65], v[232:233] op_sel:[0,0] op_sel_hi:[1,0]\n"
    "v_pk_mul_f32 v[66:67], v[66:67], v[232:233] op_sel:[0,0] op_sel_hi:[1,0]\n"
    "v_pk_mul_f32 v[68:69], v[68:69], v[232:233] op_sel:[0,1] op_sel_hi:[1,1]\n"
    "v_pk_mul_f32 v[70:71], v[70:71], v[232:233] op_sel:[0,1] op_sel_hi:[1,1]\n"
    "v_pk_mul_f32 v[72:73], v[72:73], v[232:233] op_sel:[0,0] op_sel_hi:[1,0]\n"
    "v_pk_mul_f32 v[74:75], v[74:75], v[232:233] op_sel:[0,0] op_sel_hi:[1,0]\n"
    "v_pk_mul_f32 v[76:77], v[76:77], v[232:233] op_sel:[0,1] op_sel_hi:[1,1]\n"
    "v_pk_mul_f32 v[78:79], v[78:79], v[232:233] op_sel:[0,1] op_sel_hi:[1,1]\n"
    "v_pk_mul_f32 v[80:81], v[80:81], v[232:233] op_sel:[0,0] op_sel_hi:[1,0]\n"
    "v_pk_mul_f32 v[82:83], v[82:83], v[232:233] op_sel:[0,0] op_sel_hi:[1,0]\n"
    "v_pk_mul_f32 v[84:85], v[84:85], v[232:233] op_sel:[0,1] op_sel_hi:[1,1]\n"
    "v_pk_mul_f32 v[86:87], v[86:87], v[232:233] op_sel:[0,1] op_sel_hi:[1,1]\n"
    "v_pk_mul_f32 v[88:89], v[88:89], v[232:233] op_sel:[0,0] op_sel_hi:[1,0]\n"
    "v_pk_mul_f32 v[90:91], v[90:91], v[232:233] op_sel:[0,0] op_sel_hi:[1,0]\n"
    "v_pk_mul_f32 v[92:93], v[92:93], v[232:233] op_sel:[0,1] op_sel_hi:[1,1]\n"
    "v_pk_mul_f32 v[94:95], v[94:95], v[232:233] op_sel:[0,1] op_sel_hi:[1,1]\n"
    "v_pk_mul_f32 v[96:97], v[96:97], v[232:233] op_sel:[0,0] op_sel_hi:[1,0]\n"
    "v_pk_mul_f32 v[98:99], v[98:99], v[232:233] op_sel:[0,0] op_sel_hi:[1,0]\n"
    "v_pk_mul_f32 v[100:101], v[100:101], v[232:233] op_sel:[0,1] op_sel_hi:[1,1]\n"
    "v_pk_mul_f32 v[102:103], v[102:103], v[232:233] op_sel:[0,1] op_sel_hi:[1,1]\n"
    "v_pk_mul_f32 v[104:105], v[104:105], v[232:233] op_sel:[0,0] op_sel_hi:[1,0]\n"
    "v_pk_mul_f32 v[106:107], v[106:107], v[232:233] op_sel:[0,0] op_sel_hi:[1,0]\n"
    "v_pk_mul_f32 v[108:109], v[108:109], v[232:233] op_sel:[0,1] op_sel_hi:[1,1]\n"
    "v_pk_mul_f32 v[110:111], v[110:111], v[232:233] op_sel:[0,1] op_sel_hi:[1,1]\n"
    "v_pk_mul_f32 v[112:113], v[112:113], v[232:233] op_sel:[0,0] op_sel_hi:[1,0]\n"
    "v_pk_mul_f32 v[114:115], v[114:115], v[232:233] op_sel:[0,0] op_sel_hi:[1,0]\n"
    "v_pk_mul_f32 v[116:117], v[116:117], v[232:233] op_sel:[0,1] op_sel_hi:[1,1]\n"
    "v_pk_mul_f32 v[118:119], v[118:119], v[232:233] op_sel:[0,1] op_sel_hi:[1,1]\n"
    "v_pk_mul_f32 v[120:121], v[120:121], v[232:233] op_sel:[0,0] op_sel_hi:[1,0]\n"
    "v_pk_mul_f32 v[122:123], v[122:123], v[232:233] op_sel:[0,0] op_sel_hi:[1,0]\n"
    "v_pk_mul_f32 v[124:125], v[124:125], v[232:233] op_sel:[0,1] op_sel_hi:[1,1]\n"
    "v_pk_mul_f32 v[126:127], v[126:127], v[232:233] op_sel:[0,1] op_sel_hi:[1,1]\n"
    "v_exp_f32 v168, v144\n"
    "v_exp_f32 v169, v145\n"
    "v_exp_f32 v170, v146\n"
    "v_exp_f32 v171, v147\n"
    "v_exp_f32 v172, v148\n"
    "v_exp_f32 v173, v149\n"
    "v_exp_f32 v174, v150\n"
    "v_exp_f32 v175, v151\n"
    "v_exp_f32 v176, v152\n"
    "v_exp_f32 v177, v153\n"
    "v_exp_f32 v178, v154\n"
    "v_exp_f32 v179, v155\n"
    "v_exp_f32 v180, v156\n"
    "v_exp_f32 v181, v157\n"
    "v_exp_f32 v182, v158\n"
    "v_exp_f32 v183, v159\n"
    "s_nop 0\n"
    "v_add_f32 v222, v168, v169\n"
    "v_cvt_pk_bf16_f32 v192, v168, v169\n"
    "v_add_f32 v222, v222, v170\n"
    "v_cvt_pk_bf16_f32 v193, v170, v171\n"
    "v_add_f32 v222, v222, v171\n"
    "v_add_f32 v222, v222, v172\n"
    "v_cvt_pk_bf16_f32 v194, v172, v173\n"
    "v_add_f32 v222, v222, v173\n"
    "v_add_f32 v222, v222, v174\n"
    "v_cvt_pk_bf16_f32 v195, v174, v175\n"
    "v_add_f32 v222, v222, v175\n"
    "v_add_f32 v223, v176, v177\n"
    "v_cvt_pk_bf16_f32 v196, v176, v177\n"
    "v_add_f32 v223, v223, v178\n"
    "v_cvt_pk_bf16_f32 v197, v178, v179\n"
    "v_add_f32 v223, v223, v179\n"
    "v_add_f32 v223, v223, v180\n"
    "v_cvt_pk_bf16_f32 v198, v180, v181\n"
    "v_add_f32 v223, v223, v181\n"
    "v_add_f32 v223, v223, v182\n"
    "v_cvt_pk_bf16_f32 v199, v182, v183\n"
    "v_add_f32 v223, v223, v183\n"
    "v_add_f32 v224, v222, v223\n"
    "s_nop 1\n"
    "s_cmp_eq_u32 s62, 1\n"
    "s_cbranch_scc1 L_ret1\n"
    "s_cmp_eq_u32 s62, 3\n"
    "s_cbranch_scc1 L_ret3\n"
    "s_branch L_ret4\n"
    "L_end:\n"
    : "={v[0:15]}"(ro[0]), "={v[16:31]}"(ro[1]), "={v[32:47]}"(ro[2]), "={v[48:63]}"(ro[3]), "={v[128:143]}"(ro[4]), "={v[144:159]}"(ro[5]), "={v[160:175]}"(ro[6]), "={v[176:191]}"(ro[7])
    : "s"((unsigned)(size_t)wq), "s"((unsigned)((size_t)wq >> 32)), "s"((unsigned)(size_t)Kc), "s"((unsigned)((size_t)Kc >> 32)),
       "s"((unsigned)(size_t)Vc), "s"((unsigned)((size_t)Vc >> 32)), "s"((unsigned)(size_t)wo), "s"((unsigned)((size_t)wo >> 32)),
       "s"((unsigned)(size_t)ws), "s"((unsigned)((size_t)ws >> 32)),
       "s"(w), "s"(kvh), "s"(rb), "s"(l), "s"(wg), "s"(wu), "s"(wd), "s"(x)
    : "v64","v65","v66","v67","v68","v69","v70","v71","v72","v73","v74","v75","v76","v77","v78","v79","v80","v81","v82","v83","v84","v85","v86","v87","v88","v89","v90","v91","v92","v93","v94","v95","v96","v97","v98","v99","v100","v101","v102","v103","v104","v105","v106","v107","v108","v109","v110","v111","v112","v113","v114","v115","v116","v117","v118","v119","v120","v121","v122","v123","v124","v125","v126","v127","v192","v193","v194","v195","v196","v197","v198","v199","v200","v201","v202","v203","v204","v205","v206","v207","v208","v209","v210","v211","v212","v213","v214","v215","v216","v217","v218","v219","v220","v221","v222","v223","v224","v225","v226","v227","v228","v229","v230","v231","v232","v233","v234","v235","v236","v237","v238","v239","v240","v241","v242","v243","v244","v245","v246","v247","v248","v249","v250","v251","v252","v253","v254","v255","a0","a1","a2","a3","a4","a5","a6","a7","a8","a9","a10","a11","a12","a13","a14","a15","a16","a17","a18","a19","a20","a21","a22","a23","a24","a25","a26","a27","a28","a29","a30","a31","a32","a33","a34","a35","a36","a37","a38","a39","a40","a41","a42","a43","a44","a45","a46","a47","a48","a49","a50","a51","a52","a53","a54","a55","a56","a57","a58","a59","a60","a61","a62","a63","a64","a65","a66","a67","a68","a69","a70","a71","a72","a73","a74","a75","a76","a77","a78","a79","a80","a81","a82","a83","a84","a85","a86","a87","a88","a89","a90","a91","a92","a93","a94","a95","a96","a97","a98","a99","a100","a101","a102","a103","a104","a105","a106","a107","a108","a109","a110","a111","a112","a113","a114","a115","a116","a117","a118","a119","a120","a121","a122","a123","a124","a125","a126","a127","a128","a129","a130","a131","a132","a133","a134","a135","a136","a137","a138","a139","a140","a141","a142","a143","a144","a145","a146","a147","a148","a149","a150","a151","a152","a153","a154","a155","a156","a157","a158","a159","a160","a161","a162","a163","a164","a165","a166","a167","a168","a169","a170","a171","a172","a173","a174","a175","a176","a177","a178","a179","a180","a181","a182","a183","a184","a185","a186","a187","a188","a189","a190","a191","a192","a193","a194","a195","a196","a197","a198","a199","a200","a201","a202","a203","a204","a205","a206","a207","a208","a209","a210","a211","a212","a213","a214","a215","a216","a217","a218","a219","a220","a221","a222","a223","a224","a225","a226","a227","a228","a229","a230","a231","a232","a233","a234","a235","a236","a237","a238","a239","a240","a241","a242","a243","a244","a245","a246","a247","a248","a249","a250","a251","a252","a253","a254","a255","s40","s41","s42","s43","s44","s45","s46","s47","s48","s49","s50","s51","s52","s53","s54","s55","s56","s57","s58","s59","s60","s61","s62","s63","s64","s65","s66","s67","s68","s69","s70","s71","s72","s73","s74","s75","s76","s77","s78","s79","s80","s81","s82","s83","s84","s85","s86","s87","s88","s89","s90","s91","s92","s93","s94","s95","s96","s97","s98","s99","vcc","m0","scc","memory");

  {
    float (*red)[7][8] = (float (*)[7][8])(smem + 256);
    const int lane = __builtin_amdgcn_mbcnt_hi(~0u, __builtin_amdgcn_mbcnt_lo(~0u, 0u));
    const int rg = blockIdx.x >> 1, half = blockIdx.x & 1;
    const int q8 = lane & 7, g = (lane >> 3) & 3;
    u32x4 p[2][8]; u32x2 xa[2], xb[2]; f32x4 md[2][6]; unsigned fo[2]; int vw[2];
#pragma unroll
    for (int i = 0; i < 2; ++i) {
      vw[i] = i ? (w + 4 < 6 ? w + 4 : 6) : w;
      const int s = 2 * vw[i] + (lane >> 5);
      fo[i] = ((rg * 14 + s) * 64 + g * 16 + half * 8 + q8) * 16;
#pragma unroll
      for (int k = 0; k < 8; ++k)
#pragma unroll
        for (int j = 0; j < 4; ++j) p[i][k][j] = ro[2 * i + (k >> 2)][4 * (k & 3) + j];
#pragma unroll
      for (int k = 0; k < 6; ++k)
#pragma unroll
        for (int j = 0; j < 4; ++j) md[i][k][j] = __uint_as_float(ro[4 + 2 * i + (k >> 2)][4 * (k & 3) + j]);
      xa[i] = u32x2{ro[5 + 2 * i][8], ro[5 + 2 * i][9]}; xb[i] = u32x2{ro[5 + 2 * i][10], ro[5 + 2 * i][11]};
    }
    float xv[2][8];
#pragma unroll
    for (int i = 0; i < 2; ++i) {
      float a[8] = {0, 0, 0, 0, 0, 0, 0, 0};
#pragma unroll
      for (int k = 0; k < 8; ++k)
#pragma unroll
        for (int j = 0; j < 4; ++j) { a[2 * j] += __uint_as_float(p[i][k][j] << 16); a[2 * j + 1] += __uint_as_float(p[i][k][j] & 0xffff0000u); }
      const float xi[8] = {__uint_as_float(xa[i][0] << 16), __uint_as_float(xa[i][0] & 0xffff0000u), __uint_as_float(xa[i][1] << 16), __uint_as_float(xa[i][1] & 0xffff0000u),
                           __uint_as_float(xb[i][0] << 16), __uint_as_float(xb[i][0] & 0xffff0000u), __uint_as_float(xb[i][1] << 16), __uint_as_float(xb[i][1] & 0xffff0000u)};
      float sum = 0.f;
#pragma unroll
      for (int j = 0; j < 8; ++j) { xv[i][j] = rbf(xi[j] + md[i][j >> 2][j & 3] * a[j]); sum += xv[i][j]; }
      u32x4 o = {cvtpk(xv[i][0], xv[i][1]), cvtpk(xv[i][2], xv[i][3]), cvtpk(xv[i][4], xv[i][5]), cvtpk(xv[i][6], xv[i][7])};
      if (i == 0 || w < 3) st16(ws + (20 << 20) + fo[i], o);
      const float lm = red8up(sum) * (1.f / 64.f);
      float q = 0.f;
#pragma unroll
      for (int j = 0; j < 8; ++j) q += (xv[i][j] - lm) * (xv[i][j] - lm);
      q = red8up(q);
      if (lane < 8 && (i == 0 || w < 3)) { red[0][vw[i]][lane] = lm; red[1][vw[i]][lane] = q; }
    }
    LBAR
    float lms[7], t = 0.f, mean = 0.f;
#pragma unroll
    for (int k = 0; k < 7; ++k) { lms[k] = red[0][k][q8]; mean += lms[k]; t += red[1][k][q8]; }
    mean *= 1.f / 7.f;
    float dv = 0.f;
#pragma unroll
    for (int k = 0; k < 7; ++k) dv += (lms[k] - mean) * (lms[k] - mean);
    t += 64.f * dv;
#pragma unroll
    for (int i = 0; i < 2; ++i)
#pragma unroll
      for (int j = 0; j < 8; ++j) xv[i][j] -= mean;
    const float rs = 1.f / sqrtf(t * (1.f / DM) + 1e-5f);
#pragma unroll
    for (int i = 0; i < 2; ++i) {
      float hh[8];
#pragma unroll
      for (int j = 0; j < 8; ++j) hh[j] = xv[i][j] * rs * rbf(1.f + md[i][4 + (j >> 2)][j & 3]) + md[i][2 + (j >> 2)][j & 3];
      u32x4 o = {cvtpk(hh[0], hh[1]), cvtpk(hh[2], hh[3]), cvtpk(hh[4], hh[5]), cvtpk(hh[6], hh[7])};
      if (i == 0 || w < 3) st16(ws + (22 << 20) + fo[i], o);
    }
  }
}

__global__ __launch_bounds__(256, 1) void kf(const u16* __restrict__ p0, const u16* __restrict__ p1, char* __restrict__ ws, const u16* __restrict__ p2, u16* __restrict__ p3) {
  __shared__ __attribute__((aligned(16))) char smem[163840];
  const unsigned w = __builtin_amdgcn_readfirstlane(threadIdx.x >> 6);
  const unsigned l = (unsigned)(size_t)smem;
  asm volatile(
    "F_0:\n"
    "s_mov_b32 s40, %0\n"
    "s_and_b32 s41, %1, 0xffff\n"
    "s_mov_b32 s44, %2\n"
    "s_and_b32 s45, %3, 0xffff\n"
    "s_mov_b32 s48, %4\n"
    "s_and_b32 s49, %5, 0xffff\n"
    "s_mov_b32 s52, %6\n"
    "s_mov_b32 s61, %7\n"
    "s_mov_b32 s64, %9\n"
    "s_and_b32 s65, %10, 0xffff\n"
    "s_mov_b32 s68, %11\n"
    "s_and_b32 s69, %12, 0xffff\n"
    ".p2align 3\n"
    "s_mov_b32 s42, 1605632\n"
    "s_mov_b32 s43, 0x20000\n"
    "s_mov_b32 s46, 1605632\n"
    "s_mov_b32 s47, 0x20000\n"
    "s_mov_b32 s50, 33554432\n"
    "s_mov_b32 s51, 0x20000\n"
    "s_lshr_b32 s58, s61, 3\n"
    "s_bfe_u32 s53, s61, 0x20001\n"
    "s_lshl_b32 s53, s53, 2\n"
    "s_and_b32 s59, s58, 3\n"
    "s_add_u32 s53, s53, s59\n"
    "s_and_b32 s76, s61, 1\n"
    "s_mul_i32 s54, s76, 7\n"
    "s_lshr_b32 s75, s58, 2\n"
    "s_add_u32 s54, s54, s75\n"
    "F_B:\n"
    "v_mbcnt_lo_u32_b32 v148, -1, 0\n"
    "v_mbcnt_hi_u32_b32 v148, -1, v148\n"
    "v_lshlrev_b32 v146, 4, v148\n"
    "v_add_u32 v147, 14336, v146\n"
    "s_lshl_b32 s58, s53, 3\n"
    "s_lshl_b32 s59, s52, 1\n"
    "s_add_u32 s58, s58, s59\n"
    "s_mul_i32 s56, s58, 14336\n"
    "s_add_u32 s56, s56, 23068672\n"
    "s_mul_i32 s60, s58, 56\n"
    "s_lshl_b32 s59, s54, 2\n"
    "s_add_u32 s60, s60, s59\n"
    "s_lshl_b32 s60, s60, 10\n"
    "s_add_u32 s60, s60, 25165824\n"
    "s_mul_i32 s55, s52, 28672\n"
    "s_lshl_b32 s59, s54, 8\n"
    "s_add_u32 s55, s55, s59\n"
    "s_lshl_b32 s57, s52, 11\n"
    "v_lshrrev_b32 v164, 4, v148\n"
    "v_and_b32 v165, 15, v148\n"
    "v_mul_u32_u24 v166, 3584, v164\n"
    "v_lshlrev_b32 v167, 1, v164\n"
    "v_xor_b32 v167, v167, v165\n"
    "v_lshl_add_u32 v144, v167, 4, v166\n"
    "v_xor_b32 v167, 8, v167\n"
    "v_lshl_add_u32 v145, v167, 4, v166\n"
    "v_add_u32 v145, 13312, v145\n"
    "buffer_load_dwordx4 a[128:131], v146, s[48:51], s56 offen offset:0\n"
    "buffer_load_dwordx4 a[132:135], v147, s[48:51], s56 offen offset:0\n"
    "s_add_u32 m0, s57, 0\n"
    "s_nop 0\n"
    "buffer_load_dwordx4 v144, s[40:43], s55 offen offset:0 lds\n"
    "buffer_load_dwordx4 v145, s[40:43], s55 offen offset:1024 lds\n"
    "s_add_u32 m0, s57, 8192\n"
    "s_nop 0\n"
    "buffer_load_dwordx4 v144, s[44:47], s55 offen offset:0 lds\n"
    "buffer_load_dwordx4 v145, s[44:47], s55 offen offset:1024 lds\n"
    "s_getpc_b64 s[84:85]\n"
    "F_pc:\n"
    "s_lshr_b32 s83, s61, 3\n"
    "s_lshl_b32 s83, s83, 2\n"
    "s_add_u32 s83, s83, s52\n"
    "s_mul_i32 s83, s83, 192\n"
    "s_min_u32 s83, s83, F_end-F_pc-192\n"
    "s_add_u32 s84, s84, s83\n"
    "s_addc_u32 s85, s85, 0\n"
    "s_load_dword s88, s[84:85], 0x0\n"
    "s_load_dword s88, s[84:85], 0x40\n"
    "s_load_dword s88, s[84:85], 0x80\n"
    "v_lshrrev_b32 v164, 2, v148\n"
    "v_and_b32 v165, 7, v164\n"
    "v_bfe_u32 v166, v148, 1, 1\n"
    "v_lshl_add_u32 v165, v165, 1, v166\n"
    "v_lshlrev_b32 v164, 8, v164\n"
    "v_and_b32 v166, 1, v148\n"
    "v_lshl_add_u32 v164, v166, 3, v164\n"
    "s_mov_b32 s62, 0xbfb8aa3b\n"
    "v_xor_b32 v166, 0, v165\n"
    "v_lshl_add_u32 v128, v166, 4, v164\n"
    "v_add_u32 v136, 0x10000, v128\n"
    "v_xor_b32 v166, 2, v165\n"
    "v_lshl_add_u32 v129, v166, 4, v164\n"
    "v_add_u32 v137, 0x10000, v129\n"
    "v_xor_b32 v166, 4, v165\n"
    "v_lshl_add_u32 v130, v166, 4, v164\n"
    "v_add_u32 v138, 0x10000, v130\n"
    "v_xor_b32 v166, 6, v165\n"
    "v_lshl_add_u32 v131, v166, 4, v164\n"
    "v_add_u32 v139, 0x10000, v131\n"
    "v_xor_b32 v166, 8, v165\n"
    "v_lshl_add_u32 v132, v166, 4, v164\n"
    "v_add_u32 v140, 0x10000, v132\n"
    "v_xor_b32 v166, 10, v165\n"
    "v_lshl_add_u32 v133, v166, 4, v164\n"
    "v_add_u32 v141, 0x10000, v133\n"
    "v_xor_b32 v166, 12, v165\n"
    "v_lshl_add_u32 v134, v166, 4, v164\n"
    "v_add_u32 v142, 0x10000, v134\n"
    "v_xor_b32 v166, 14, v165\n"
    "v_lshl_add_u32 v135, v166, 4, v164\n"
    "v_add_u32 v143, 0x10000, v135\n"
    "buffer_load_dwordx4 a[136:139], v146, s[48:51], s56 offen offset:1024\n"
    "buffer_load_dwordx4 a[140:143], v147, s[48:51], s56 offen offset:1024\n"
    "s_add_u32 s55, s55, 114688\n"
    "s_add_u32 m0, s57, 16384\n"
    "s_nop 0\n"
    "buffer_load_dwordx4 v144, s[40:43], s55 offen offset:0 lds\n"
    "buffer_load_dwordx4 v145, s[40:43], s55 offen offset:1024 lds\n"
    "s_add_u32 m0, s57, 24576\n"
    "s_nop 0\n"
    "buffer_load_dwordx4 v144, s[44:47], s55 offen offset:0 lds\n"
    "buffer_load_dwordx4 v145, s[44:47], s55 offen offset:1024 lds\n"
    "buffer_load_dwordx4 a[144:147], v146, s[48:51], s56 offen offset:2048\n"
    "buffer_load_dwordx4 a[148:151], v147, s[48:51], s56 offen offset:2048\n"
    "s_add_u32 s55, s55, 114688\n"
    "s_add_u32 m0, s57, 32768\n"
    "s_nop 0\n"
    "buffer_load_dwordx4 v144, s[40:43], s55 offen offset:0 lds\n"
    "buffer_load_dwordx4 v145, s[40:43], s55 offen offset:1024 lds\n"
    "s_add_u32 m0, s57, 40960\n"
    "s_nop 0\n"
    "buffer_load_dwordx4 v144, s[44:47], s55 offen offset:0 lds\n"
    "buffer_load_dwordx4 v145, s[44:47], s55 offen offset:1024 lds\n"
    "buffer_load_dwordx4 a[152:155], v146, s[48:51], s56 offen offset:3072\n"
    "buffer_load_dwordx4 a[156:159], v147, s[48:51], s56 offen offset:3072\n"
    "s_add_u32 s55, s55, 114688\n"
    "s_add_u32 m0, s57, 49152\n"
    "s_nop 0\n"
    "buffer_load_dwordx4 v144, s[40:43], s55 offen offset:0 lds\n"
    "buffer_load_dwordx4 v145, s[40:43], s55 offen offset:1024 lds\n"
    "s_add_u32 m0, s57, 57344\n"
    "s_nop 0\n"
    "buffer_load_dwordx4 v144, s[44:47], s55 offen offset:0 lds\n"
    "buffer_load_dwordx4 v145, s[44:47], s55 offen offset:1024 lds\n"
    "s_mov_b32 s66, 1605632\n"
    "s_mov_b32 s67, 0x20000\n"
    "s_mov_b32 s70, 1835008\n"
    "s_mov_b32 s71, 0x20000\n"
    "s_mul_i32 s72, s52, 401408\n"
    "s_lshl_b32 s59, s75, 7\n"
    "s_add_u32 s72, s72, s59\n"
    "s_lshl_b32 s73, s52, 12\n"
    "v_lshrrev_b32 v166, 3, v148\n"
    "v_mul_u32_u24 v166, 896, v166\n"
    "v_and_b32 v167, 7, v148\n"
    "v_lshrrev_b32 v165, 4, v148\n"
    "v_lshlrev_b32 v164, 1, v165\n"
    "v_xor_b32 v167, v167, v164\n"
    "v_lshl_add_u32 v149, v167, 4, v166\n"
    "v_add_u32 v150, 6144, v149\n"
    "v_add_u32 v151, 12288, v149\n"
    "v_add_u32 v152, 18432, v149\n"
    "s_add_u32 s59, s73, 98304\n"
    "v_lshrrev_b32 v166, 2, v148\n"
    "v_lshlrev_b32 v166, 7, v166\n"
    "v_and_b32 v167, 1, v148\n"
    "v_lshl_add_u32 v166, v167, 3, v166\n"
    "v_add_u32 v166, s59, v166\n"
    "v_bfe_u32 v167, v148, 3, 2\n"
    "v_lshlrev_b32 v167, 1, v167\n"
    "v_bfe_u32 v164, v148, 1, 1\n"
    "v_xor_b32 v167, v167, v164\n"
    "v_xor_b32 v164, 0, v167\n"
    "v_lshl_add_u32 v160, v164, 4, v166\n"
    "v_xor_b32 v164, 2, v167\n"
    "v_lshl_add_u32 v161, v164, 4, v166\n"
    "v_xor_b32 v164, 4, v167\n"
    "v_lshl_add_u32 v162, v164, 4, v166\n"
    "v_xor_b32 v164, 6, v167\n"
    "v_lshl_add_u32 v163, v164, 4, v166\n"
    "s_waitcnt vmcnt(18)\n"
    "s_barrier\n"
    "ds_read_b64_tr_b16 a[0:1], v128 offset:0\n"
    "ds_read_b64_tr_b16 a[2:3], v128 offset:4096\n"
    "ds_read_b64_tr_b16 a[4:5], v128 offset:8192\n"
    "ds_read_b64_tr_b16 a[6:7], v128 offset:12288\n"
    "ds_read_b64_tr_b16 a[8:9], v129 offset:0\n"
    "ds_read_b64_tr_b16 a[10:11], v129 offset:4096\n"
    "ds_read_b64_tr_b16 a[12:13], v129 offset:8192\n"
    "ds_read_b64_tr_b16 a[14:15], v129 offset:12288\n"
    "ds_read_b64_tr_b16 a[16:17], v130 offset:0\n"
    "ds_read_b64_tr_b16 a[18:19], v130 offset:4096\n"
    "ds_read_b64_tr_b16 a[20:21], v130 offset:8192\n"
    "ds_read_b64_tr_b16 a[22:23], v130 offset:12288\n"
    "ds_read_b64_tr_b16 a[24:25], v131 offset:0\n"
    "ds_read_b64_tr_b16 a[26:27], v131 offset:4096\n"
    "ds_read_b64_tr_b16 a[28:29], v131 offset:8192\n"
    "ds_read_b64_tr_b16 a[30:31], v131 offset:12288\n"
    "ds_read_b64_tr_b16 a[32:33], v132 offset:0\n"
    "ds_read_b64_tr_b16 a[34:35], v132 offset:4096\n"
    "ds_read_b64_tr_b16 a[36:37], v132 offset:8192\n"
    "ds_read_b64_tr_b16 a[38:39], v132 offset:12288\n"
    "ds_read_b64_tr_b16 a[40:41], v133 offset:0\n"
    "ds_read_b64_tr_b16 a[42:43], v133 offset:4096\n"
    "ds_read_b64_tr_b16 a[44:45], v133 offset:8192\n"
    "ds_read_b64_tr_b16 a[46:47], v133 offset:12288\n"
    "ds_read_b64_tr_b16 a[48:49], v134 offset:0\n"
    "ds_read_b64_tr_b16 a[50:51], v134 offset:4096\n"
    "ds_read_b64_tr_b16 a[52:53], v134 offset:8192\n"
    "ds_read_b64_tr_b16 a[54:55], v134 offset:12288\n"
    "ds_read_b64_tr_b16 a[56:57], v135 offset:0\n"
    "ds_read_b64_tr_b16 a[58:59], v135 offset:4096\n"
    "ds_read_b64_tr_b16 a[60:61], v135 offset:8192\n"
    "ds_read_b64_tr_b16 a[62:63], v135 offset:12288\n"
    "s_waitcnt lgkmcnt(15)\n"
    "v_mfma_f32_16x16x32_bf16 v[0:3], a[0:3], a[128:131], 0\n"
    "v_mfma_f32_16x16x32_bf16 v[4:7], a[0:3], a[132:135], 0\n"
    "s_waitcnt vmcnt(12) lgkmcnt(0)\n"
    "s_barrier\n"
    "v_mfma_f32_16x16x32_bf16 v[8:11], a[4:7], a[128:131], 0\n"
    "ds_read_b64_tr_b16 a[64:65], v128 offset:16384\n"
    "ds_read_b64_tr_b16 a[66:67], v128 offset:20480\n"
    "v_mfma_f32_16x16x32_bf16 v[12:15], a[4:7], a[132:135], 0\n"
    "ds_read_b64_tr_b16 a[68:69], v128 offset:24576\n"
    "ds_read_b64_tr_b16 a[70:71], v128 offset:28672\n"
    "s_add_u32 s56, s56, 4096\n"
    "v_mfma_f32_16x16x32_bf16 v[16:19], a[8:11], a[128:131], 0\n"
    "buffer_load_dwordx4 a[160:163], v146, s[48:51], s56 offen offset:0\n"
    "ds_read_b64_tr_b16 a[72:73], v129 offset:16384\n"
    "ds_read_b64_tr_b16 a[74:75], v129 offset:20480\n"
    "v_mfma_f32_16x16x32_bf16 v[20:23], a[8:11], a[132:135], 0\n"
    "ds_read_b64_tr_b16 a[76:77], v129 offset:24576\n"
    "ds_read_b64_tr_b16 a[78:79], v129 offset:28672\n"
    "v_mfma_f32_16x16x32_bf16 v[24:27], a[12:15], a[128:131], 0\n"
    "ds_read_b64_tr_b16 a[80:81], v130 offset:16384\n"
    "ds_read_b64_tr_b16 a[82:83], v130 offset:20480\n"
    "v_mfma_f32_16x16x32_bf16 v[28:31], a[12:15], a[132:135], 0\n"
    "ds_read_b64_tr_b16 a[84:85], v130 offset:24576\n"
    "ds_read_b64_tr_b16 a[86:87], v130 offset:28672\n"
    "v_mfma_f32_16x16x32_bf16 v[32:35], a[16:19], a[128:131], 0\n"
    "buffer_load_dwordx4 a[164:167], v147, s[48:51], s56 offen offset:0\n"
    "ds_read_b64_tr_b16 a[88:89], v131 offset:16384\n"
    "v_mfma_f32_16x16x32_bf16 v[36:39], a[16:19], a[132:135], 0\n"
    "ds_read_b64_tr_b16 a[90:91], v131 offset:20480\n"
    "ds_read_b64_tr_b16 a[92:93], v131 offset:24576\n"
    "v_mfma_f32_16x16x32_bf16 v[40:43], a[20:23], a[128:131], 0\n"
    "ds_read_b64_tr_b16 a[94:95], v131 offset:28672\n"
    "ds_read_b64_tr_b16 a[96:97], v132 offset:16384\n"
    "v_mfma_f32_16x16x32_bf16 v[44:47], a[20:23], a[132:135], 0\n"
    "ds_read_b64_tr_b16 a[98:99], v132 offset:20480\n"
    "ds_read_b64_tr_b16 a[100:101], v132 offset:24576\n"
    "v_mfma_f32_16x16x32_bf16 v[48:51], a[24:27], a[128:131], 0\n"
    "ds_read_b64_tr_b16 a[102:103], v132 offset:28672\n"
    "s_add_u32 s55, s55, 114688\n"
    "s_add_u32 m0, s57, 65536\n"
    "v_mfma_f32_16x16x32_bf16 v[52:55], a[24:27], a[132:135], 0\n"
    "buffer_load_dwordx4 v144, s[40:43], s55 offen offset:0 lds\n"
    "ds_read_b64_tr_b16 a[104:105], v133 offset:16384\n"
    "v_mfma_f32_16x16x32_bf16 v[56:59], a[28:31], a[128:131], 0\n"
    "ds_read_b64_tr_b16 a[106:107], v133 offset:20480\n"
    "ds_read_b64_tr_b16 a[108:109], v133 offset:24576\n"
    "v_mfma_f32_16x16x32_bf16 v[60:63], a[28:31], a[132:135], 0\n"
    "ds_read_b64_tr_b16 a[110:111], v133 offset:28672\n"
    "ds_read_b64_tr_b16 a[112:113], v134 offset:16384\n"
    "v_mfma_f32_16x16x32_bf16 v[64:67], a[32:35], a[128:131], 0\n"
    "ds_read_b64_tr_b16 a[114:115], v134 offset:20480\n"
    "ds_read_b64_tr_b16 a[116:117], v134 offset:24576\n"
    "v_mfma_f32_16x16x32_bf16 v[68:71], a[32:35], a[132:135], 0\n"
    "buffer_load_dwordx4 v145, s[40:43], s55 offen offset:1024 lds\n"
    "ds_read_b64_tr_b16 a[118:119], v134 offset:28672\n"
    "v_mfma_f32_16x16x32_bf16 v[72:75], a[36:39], a[128:131], 0\n"
    "ds_read_b64_tr_b16 a[120:121], v135 offset:16384\n"
    "ds_read_b64_tr_b16 a[122:123], v135 offset:20480\n"
    "v_mfma_f32_16x16x32_bf16 v[76:79], a[36:39], a[132:135], 0\n"
    "ds_read_b64_tr_b16 a[124:125], v135 offset:24576\n"
    "ds_read_b64_tr_b16 a[126:127], v135 offset:28672\n"
    "v_mfma_f32_16x16x32_bf16 v[80:83], a[40:43], a[128:131], 0\n"
    "v_mfma_f32_16x16x32_bf16 v[84:87], a[40:43], a[132:135], 0\n"
    "s_add_u32 m0, s57, 73728\n"
    "v_mfma_f32_16x16x32_bf16 v[88:91], a[44:47], a[128:131], 0\n"
    "buffer_load_dwordx4 v144, s[44:47], s55 offen offset:0 lds\n"
    "v_mfma_f32_16x16x32_bf16 v[92:95], a[44:47], a[132:135], 0\n"
    "v_mfma_f32_16x16x32_bf16 v[96:99], a[48:51], a[128:131], 0\n"
    "v_mfma_f32_16x16x32_bf16 v[100:103], a[48:51], a[132:135], 0\n"
    "v_mfma_f32_16x16x32_bf16 v[104:107], a[52:55], a[128:131], 0\n"
    "buffer_load_dwordx4 v145, s[44:47], s55 offen offset:1024 lds\n"
    "v_mfma_f32_16x16x32_bf16 v[108:111], a[52:55], a[132:135], 0\n"
    "v_mfma_f32_16x16x32_bf16 v[112:115], a[56:59], a[128:131], 0\n"
    "v_mfma_f32_16x16x32_bf16 v[116:119], a[56:59], a[132:135], 0\n"
    "v_mfma_f32_16x16x32_bf16 v[120:123], a[60:63], a[128:131], 0\n"
    "v_mfma_f32_16x16x32_bf16 v[124:127], a[60:63], a[132:135], 0\n"
    "s_waitcnt lgkmcnt(15)\n"
    "s_nop 0\n"
    "v_mfma_f32_16x16x32_bf16 v[0:3], a[64:67], a[136:139], v[0:3]\n"
    "v_mfma_f32_16x16x32_bf16 v[4:7], a[64:67], a[140:143], v[4:7]\n"
    "s_waitcnt vmcnt(12) lgkmcnt(0)\n"
    "s_barrier\n"
    "v_mfma_f32_16x16x32_bf16 v[8:11], a[68:71], a[136:139], v[8:11]\n"
    "ds_read_b64_tr_b16 a[0:1], v128 offset:32768\n"
    "ds_read_b64_tr_b16 a[2:3], v128 offset:36864\n"
    "v_mfma_f32_16x16x32_bf16 v[12:15], a[68:71], a[140:143], v[12:15]\n"
    "ds_read_b64_tr_b16 a[4:5], v128 offset:40960\n"
    "ds_read_b64_tr_b16 a[6:7], v128 offset:45056\n"
    "v_mfma_f32_16x16x32_bf16 v[16:19], a[72:75], a[136:139], v[16:19]\n"
    "buffer_load_dwordx4 a[168:171], v146, s[48:51], s56 offen offset:1024\n"
    "ds_read_b64_tr_b16 a[8:9], v129 offset:32768\n"
    "ds_read_b64_tr_b16 a[10:11], v129 offset:36864\n"
    "v_mfma_f32_16x16x32_bf16 v[20:23], a[72:75], a[140:143], v[20:23]\n"
    "ds_read_b64_tr_b16 a[12:13], v129 offset:40960\n"
    "ds_read_b64_tr_b16 a[14:15], v129 offset:45056\n"
    "v_mfma_f32_16x16x32_bf16 v[24:27], a[76:79], a[136:139], v[24:27]\n"
    "ds_read_b64_tr_b16 a[16:17], v130 offset:32768\n"
    "ds_read_b64_tr_b16 a[18:19], v130 offset:36864\n"
    "v_mfma_f32_16x16x32_bf16 v[28:31], a[76:79], a[140:143], v[28:31]\n"
    "ds_read_b64_tr_b16 a[20:21], v130 offset:40960\n"
    "ds_read_b64_tr_b16 a[22:23], v130 offset:45056\n"
    "v_mfma_f32_16x16x32_bf16 v[32:35], a[80:83], a[136:139], v[32:35]\n"
    "buffer_load_dwordx4 a[172:175], v147, s[48:51], s56 offen offset:1024\n"
    "ds_read_b64_tr_b16 a[24:25], v131 offset:32768\n"
    "v_mfma_f32_16x16x32_bf16 v[36:39], a[80:83], a[140:143], v[36:39]\n"
    "ds_read_b64_tr_b16 a[26:27], v131 offset:36864\n"
    "ds_read_b64_tr_b16 a[28:29], v131 offset:40960\n"
    "v_mfma_f32_16x16x32_bf16 v[40:43], a[84:87], a[136:139], v[40:43]\n"
    "ds_read_b64_tr_b16 a[30:31], v131 offset:45056\n"
    "ds_read_b64_tr_b16 a[32:33], v132 offset:32768\n"
    "v_mfma_f32_16x16x32_bf16 v[44:47], a[84:87], a[140:143], v[44:47]\n"
    "ds_read_b64_tr_b16 a[34:35], v132 offset:36864\n"
    "ds_read_b64_tr_b16 a[36:37], v132 offset:40960\n"
    "v_mfma_f32_16x16x32_bf16 v[48:51], a[88:91], a[136:139], v[48:51]\n"
    "ds_read_b64_tr_b16 a[38:39], v132 offset:45056\n"
    "s_add_u32 s55, s55, 114688\n"
    "s_add_u32 m0, s57, 81920\n"
    "v_mfma_f32_16x16x32_bf16 v[52:55], a[88:91], a[140:143], v[52:55]\n"
    "buffer_load_dwordx4 v144, s[40:43], s55 offen offset:0 lds\n"
    "ds_read_b64_tr_b16 a[40:41], v133 offset:32768\n"
    "v_mfma_f32_16x16x32_bf16 v[56:59], a[92:95], a[136:139], v[56:59]\n"
    "ds_read_b64_tr_b16 a[42:43], v133 offset:36864\n"
    "ds_read_b64_tr_b16 a[44:45], v133 offset:40960\n"
    "v_mfma_f32_16x16x32_bf16 v[60:63], a[92:95], a[140:143], v[60:63]\n"
    "ds_read_b64_tr_b16 a[46:47], v133 offset:45056\n"
    "ds_read_b64_tr_b16 a[48:49], v134 offset:32768\n"
    "v_mfma_f32_16x16x32_bf16 v[64:67], a[96:99], a[136:139], v[64:67]\n"
    "ds_read_b64_tr_b16 a[50:51], v134 offset:36864\n"
    "ds_read_b64_tr_b16 a[52:53], v134 offset:40960\n"
    "v_mfma_f32_16x16x32_bf16 v[68:71], a[96:99], a[140:143], v[68:71]\n"
    "buffer_load_dwordx4 v145, s[40:43], s55 offen offset:1024 lds\n"
    "ds_read_b64_tr_b16 a[54:55], v134 offset:45056\n"
    "v_mfma_f32_16x16x32_bf16 v[72:75], a[100:103], a[136:139], v[72:75]\n"
    "ds_read_b64_tr_b16 a[56:57], v135 offset:32768\n"
    "ds_read_b64_tr_b16 a[58:59], v135 offset:36864\n"
    "v_mfma_f32_16x16x32_bf16 v[76:79], a[100:103], a[140:143], v[76:79]\n"
    "ds_read_b64_tr_b16 a[60:61], v135 offset:40960\n"
    "ds_read_b64_tr_b16 a[62:63], v135 offset:45056\n"
    "v_mfma_f32_16x16x32_bf16 v[80:83], a[104:107], a[136:139], v[80:83]\n"
    "v_mfma_f32_16x16x32_bf16 v[84:87], a[104:107], a[140:143], v[84:87]\n"
    "s_add_u32 m0, s57, 90112\n"
    "v_mfma_f32_16x16x32_bf16 v[88:91], a[108:111], a[136:139], v[88:91]\n"
    "buffer_load_dwordx4 v144, s[44:47], s55 offen offset:0 lds\n"
    "v_mfma_f32_16x16x32_bf16 v[92:95], a[108:111], a[140:143], v[92:95]\n"
    "v_mfma_f32_16x16x32_bf16 v[96:99], a[112:115], a[136:139], v[96:99]\n"
    "v_mfma_f32_16x16x32_bf16 v[100:103], a[112:115], a[140:143], v[100:103]\n"
    "v_mfma_f32_16x16x32_bf16 v[104:107], a[116:119], a[136:139], v[104:107]\n"
    "buffer_load_dwordx4 v145, s[44:47], s55 offen offset:1024 lds\n"
    "v_mfma_f32_16x16x32_bf16 v[108:111], a[116:119], a[140:143], v[108:111]\n"
    "v_mfma_f32_16x16x32_bf16 v[112:115], a[120:123], a[136:139], v[112:115]\n"
    "v_mfma_f32_16x16x32_bf16 v[116:119], a[120:123], a[140:143], v[116:119]\n"
    "v_mfma_f32_16x16x32_bf16 v[120:123], a[124:127], a[136:139], v[120:123]\n"
    "v_mfma_f32_16x16x32_bf16 v[124:127], a[124:127], a[140:143], v[124:127]\n"
    "s_waitcnt lgkmcnt(15)\n"
    "s_nop 0\n"
    "v_mfma_f32_16x16x32_bf16 v[0:3], a[0:3], a[144:147], v[0:3]\n"
    "v_mfma_f32_16x16x32_bf16 v[4:7], a[0:3], a[148:151], v[4:7]\n"
    "s_waitcnt vmcnt(12) lgkmcnt(0)\n"
    "s_barrier\n"
    "v_mfma_f32_16x16x32_bf16 v[8:11], a[4:7], a[144:147], v[8:11]\n"
    "ds_read_b64_tr_b16 a[64:65], v128 offset:49152\n"
    "ds_read_b64_tr_b16 a[66:67], v128 offset:53248\n"
    "v_mfma_f32_16x16x32_bf16 v[12:15], a[4:7], a[148:151], v[12:15]\n"
    "ds_read_b64_tr_b16 a[68:69], v128 offset:57344\n"
    "ds_read_b64_tr_b16 a[70:71], v128 offset:61440\n"
    "v_mfma_f32_16x16x32_bf16 v[16:19], a[8:11], a[144:147], v[16:19]\n"
    "buffer_load_dwordx4 a[176:179], v146, s[48:51], s56 offen offset:2048\n"
    "ds_read_b64_tr_b16 a[72:73], v129 offset:49152\n"
    "ds_read_b64_tr_b16 a[74:75], v129 offset:53248\n"
    "v_mfma_f32_16x16x32_bf16 v[20:23], a[8:11], a[148:151], v[20:23]\n"
    "ds_read_b64_tr_b16 a[76:77], v129 offset:57344\n"
    "ds_read_b64_tr_b16 a[78:79], v129 offset:61440\n"
    "v_mfma_f32_16x16x32_bf16 v[24:27], a[12:15], a[144:147], v[24:27]\n"
    "ds_read_b64_tr_b16 a[80:81], v130 offset:49152\n"
    "ds_read_b64_tr_b16 a[82:83], v130 offset:53248\n"
    "v_mfma_f32_16x16x32_bf16 v[28:31], a[12:15], a[148:151], v[28:31]\n"
    "ds_read_b64_tr_b16 a[84:85], v130 offset:57344\n"
    "ds_read_b64_tr_b16 a[86:87], v130 offset:61440\n"
    "v_mfma_f32_16x16x32_bf16 v[32:35], a[16:19], a[144:147], v[32:35]\n"
    "buffer_load_dwordx4 a[180:183], v147, s[48:51], s56 offen offset:2048\n"
    "ds_read_b64_tr_b16 a[88:89], v131 offset:49152\n"
    "v_mfma_f32_16x16x32_bf16 v[36:39], a[16:19], a[148:151], v[36:39]\n"
    "ds_read_b64_tr_b16 a[90:91], v131 offset:53248\n"
    "ds_read_b64_tr_b16 a[92:93], v131 offset:57344\n"
    "v_mfma_f32_16x16x32_bf16 v[40:43], a[20:23], a[144:147], v[40:43]\n"
    "ds_read_b64_tr_b16 a[94:95], v131 offset:61440\n"
    "ds_read_b64_tr_b16 a[96:97], v132 offset:49152\n"
    "v_mfma_f32_16x16x32_bf16 v[44:47], a[20:23], a[148:151], v[44:47]\n"
    "ds_read_b64_tr_b16 a[98:99], v132 offset:53248\n"
    "ds_read_b64_tr_b16 a[100:101], v132 offset:57344\n"
    "v_mfma_f32_16x16x32_bf16 v[48:51], a[24:27], a[144:147], v[48:51]\n"
    "ds_read_b64_tr_b16 a[102:103], v132 offset:61440\n"
    "s_add_u32 s55, s55, 114688\n"
    "s_add_u32 m0, s57, 98304\n"
    "v_mfma_f32_16x16x32_bf16 v[52:55], a[24:27], a[148:151], v[52:55]\n"
    "buffer_load_dwordx4 v144, s[40:43], s55 offen offset:0 lds\n"
    "ds_read_b64_tr_b16 a[104:105], v133 offset:49152\n"
    "v_mfma_f32_16x16x32_bf16 v[56:59], a[28:31], a[144:147], v[56:59]\n"
    "ds_read_b64_tr_b16 a[106:107], v133 offset:53248\n"
    "ds_read_b64_tr_b16 a[108:109], v133 offset:57344\n"
    "v_mfma_f32_16x16x32_bf16 v[60:63], a[28:31], a[148:151], v[60:63]\n"
    "ds_read_b64_tr_b16 a[110:111], v133 offset:61440\n"
    "ds_read_b64_tr_b16 a[112:113], v134 offset:49152\n"
    "v_mfma_f32_16x16x32_bf16 v[64:67], a[32:35], a[144:147], v[64:67]\n"
    "ds_read_b64_tr_b16 a[114:115], v134 offset:53248\n"
    "ds_read_b64_tr_b16 a[116:117], v134 offset:57344\n"
    "v_mfma_f32_16x16x32_bf16 v[68:71], a[32:35], a[148:151], v[68:71]\n"
    "buffer_load_dwordx4 v145, s[40:43], s55 offen offset:1024 lds\n"
    "ds_read_b64_tr_b16 a[118:119], v134 offset:61440\n"
    "v_mfma_f32_16x16x32_bf16 v[72:75], a[36:39], a[144:147], v[72:75]\n"
    "ds_read_b64_tr_b16 a[120:121], v135 offset:49152\n"
    "ds_read_b64_tr_b16 a[122:123], v135 offset:53248\n"
    "v_mfma_f32_16x16x32_bf16 v[76:79], a[36:39], a[148:151], v[76:79]\n"
    "ds_read_b64_tr_b16 a[124:125], v135 offset:57344\n"
    "ds_read_b64_tr_b16 a[126:127], v135 offset:61440\n"
    "v_mfma_f32_16x16x32_bf16 v[80:83], a[40:43], a[144:147], v[80:83]\n"
    "v_mfma_f32_16x16x32_bf16 v[84:87], a[40:43], a[148:151], v[84:87]\n"
    "s_add_u32 m0, s57, 106496\n"
    "v_mfma_f32_16x16x32_bf16 v[88:91], a[44:47], a[144:147], v[88:91]\n"
    "buffer_load_dwordx4 v144, s[44:47], s55 offen offset:0 lds\n"
    "v_mfma_f32_16x16x32_bf16 v[92:95], a[44:47], a[148:151], v[92:95]\n"
    "v_mfma_f32_16x16x32_bf16 v[96:99], a[48:51], a[144:147], v[96:99]\n"
    "v_mfma_f32_16x16x32_bf16 v[100:103], a[48:51], a[148:151], v[100:103]\n"
    "v_mfma_f32_16x16x32_bf16 v[104:107], a[52:55], a[144:147], v[104:107]\n"
    "buffer_load_dwordx4 v145, s[44:47], s55 offen offset:1024 lds\n"
    "v_mfma_f32_16x16x32_bf16 v[108:111], a[52:55], a[148:151], v[108:111]\n"
    "v_mfma_f32_16x16x32_bf16 v[112:115], a[56:59], a[144:147], v[112:115]\n"
    "v_mfma_f32_16x16x32_bf16 v[116:119], a[56:59], a[148:151], v[116:119]\n"
    "v_mfma_f32_16x16x32_bf16 v[120:123], a[60:63], a[144:147], v[120:123]\n"
    "v_mfma_f32_16x16x32_bf16 v[124:127], a[60:63], a[148:151], v[124:127]\n"
    "s_waitcnt lgkmcnt(15)\n"
    "s_nop 0\n"
    "v_mfma_f32_16x16x32_bf16 v[0:3], a[64:67], a[152:155], v[0:3]\n"
    "v_mfma_f32_16x16x32_bf16 v[4:7], a[64:67], a[156:159], v[4:7]\n"
    "s_waitcnt vmcnt(12) lgkmcnt(0)\n"
    "s_barrier\n"
    "v_mfma_f32_16x16x32_bf16 v[8:11], a[68:71], a[152:155], v[8:11]\n"
    "ds_read_b64_tr_b16 a[0:1], v136 offset:0\n"
    "ds_read_b64_tr_b16 a[2:3], v136 offset:4096\n"
    "v_mfma_f32_16x16x32_bf16 v[12:15], a[68:71], a[156:159], v[12:15]\n"
    "ds_read_b64_tr_b16 a[4:5], v136 offset:8192\n"
    "ds_read_b64_tr_b16 a[6:7], v136 offset:12288\n"
    "v_mfma_f32_16x16x32_bf16 v[16:19], a[72:75], a[152:155], v[16:19]\n"
    "buffer_load_dwordx4 a[184:187], v146, s[48:51], s56 offen offset:3072\n"
    "ds_read_b64_tr_b16 a[8:9], v137 offset:0\n"
    "ds_read_b64_tr_b16 a[10:11], v137 offset:4096\n"
    "v_mfma_f32_16x16x32_bf16 v[20:23], a[72:75], a[156:159], v[20:23]\n"
    "ds_read_b64_tr_b16 a[12:13], v137 offset:8192\n"
    "ds_read_b64_tr_b16 a[14:15], v137 offset:12288\n"
    "v_mfma_f32_16x16x32_bf16 v[24:27], a[76:79], a[152:155], v[24:27]\n"
    "ds_read_b64_tr_b16 a[16:17], v138 offset:0\n"
    "ds_read_b64_tr_b16 a[18:19], v138 offset:4096\n"
    "v_mfma_f32_16x16x32_bf16 v[28:31], a[76:79], a[156:159], v[28:31]\n"
    "ds_read_b64_tr_b16 a[20:21], v138 offset:8192\n"
    "ds_read_b64_tr_b16 a[22:23], v138 offset:12288\n"
    "v_mfma_f32_16x16x32_bf16 v[32:35], a[80:83], a[152:155], v[32:35]\n"
    "buffer_load_dwordx4 a[188:191], v147, s[48:51], s56 offen offset:3072\n"
    "ds_read_b64_tr_b16 a[24:25], v139 offset:0\n"
    "v_mfma_f32_16x16x32_bf16 v[36:39], a[80:83], a[156:159], v[36:39]\n"
    "ds_read_b64_tr_b16 a[26:27], v139 offset:4096\n"
    "ds_read_b64_tr_b16 a[28:29], v139 offset:8192\n"
    "v_mfma_f32_16x16x32_bf16 v[40:43], a[84:87], a[152:155], v[40:43]\n"
    "ds_read_b64_tr_b16 a[30:31], v139 offset:12288\n"
    "ds_read_b64_tr_b16 a[32:33], v140 offset:0\n"
    "v_mfma_f32_16x16x32_bf16 v[44:47], a[84:87], a[156:159], v[44:47]\n"
    "ds_read_b64_tr_b16 a[34:35], v140 offset:4096\n"
    "ds_read_b64_tr_b16 a[36:37], v140 offset:8192\n"
    "v_mfma_f32_16x16x32_bf16 v[48:51], a[88:91], a[152:155], v[48:51]\n"
    "ds_read_b64_tr_b16 a[38:39], v140 offset:12288\n"
    "s_add_u32 s55, s55, 114688\n"
    "s_add_u32 m0, s57, 114688\n"
    "v_mfma_f32_16x16x32_bf16 v[52:55], a[88:91], a[156:159], v[52:55]\n"
    "buffer_load_dwordx4 v144, s[40:43], s55 offen offset:0 lds\n"
    "ds_read_b64_tr_b16 a[40:41], v141 offset:0\n"
    "v_mfma_f32_16x16x32_bf16 v[56:59], a[92:95], a[152:155], v[56:59]\n"
    "ds_read_b64_tr_b16 a[42:43], v141 offset:4096\n"
    "ds_read_b64_tr_b16 a[44:45], v141 offset:8192\n"
    "v_mfma_f32_16x16x32_bf16 v[60:63], a[92:95], a[156:159], v[60:63]\n"
    "ds_read_b64_tr_b16 a[46:47], v141 offset:12288\n"
    "ds_read_b64_tr_b16 a[48:49], v142 offset:0\n"
    "v_mfma_f32_16x16x32_bf16 v[64:67], a[96:99], a[152:155], v[64:67]\n"
    "ds_read_b64_tr_b16 a[50:51], v142 offset:4096\n"
    "ds_read_b64_tr_b16 a[52:53], v142 offset:8192\n"
    "v_mfma_f32_16x16x32_bf16 v[68:71], a[96:99], a[156:159], v[68:71]\n"
    "buffer_load_dwordx4 v145, s[40:43], s55 offen offset:1024 lds\n"
    "ds_read_b64_tr_b16 a[54:55], v142 offset:12288\n"
    "v_mfma_f32_16x16x32_bf16 v[72:75], a[100:103], a[152:155], v[72:75]\n"
    "ds_read_b64_tr_b16 a[56:57], v143 offset:0\n"
    "ds_read_b64_tr_b16 a[58:59], v143 offset:4096\n"
    "v_mfma_f32_16x16x32_bf16 v[76:79], a[100:103], a[156:159], v[76:79]\n"
    "ds_read_b64_tr_b16 a[60:61], v143 offset:8192\n"
    "ds_read_b64_tr_b16 a[62:63], v143 offset:12288\n"
    "v_mfma_f32_16x16x32_bf16 v[80:83], a[104:107], a[152:155], v[80:83]\n"
    "v_mfma_f32_16x16x32_bf16 v[84:87], a[104:107], a[156:159], v[84:87]\n"
    "s_add_u32 m0, s57, 122880\n"
    "v_mfma_f32_16x16x32_bf16 v[88:91], a[108:111], a[152:155], v[88:91]\n"
    "buffer_load_dwordx4 v144, s[44:47], s55 offen offset:0 lds\n"
    "v_mfma_f32_16x16x32_bf16 v[92:95], a[108:111], a[156:159], v[92:95]\n"
    "v_mfma_f32_16x16x32_bf16 v[96:99], a[112:115], a[152:155], v[96:99]\n"
    "v_mfma_f32_16x16x32_bf16 v[100:103], a[112:115], a[156:159], v[100:103]\n"
    "v_mfma_f32_16x16x32_bf16 v[104:107], a[116:119], a[152:155], v[104:107]\n"
    "buffer_load_dwordx4 v145, s[44:47], s55 offen offset:1024 lds\n"
    "v_mfma_f32_16x16x32_bf16 v[108:111], a[116:119], a[156:159], v[108:111]\n"
    "v_mfma_f32_16x16x32_bf16 v[112:115], a[120:123], a[152:155], v[112:115]\n"
    "v_mfma_f32_16x16x32_bf16 v[116:119], a[120:123], a[156:159], v[116:119]\n"
    "v_mfma_f32_16x16x32_bf16 v[120:123], a[124:127], a[152:155], v[120:123]\n"
    "v_mfma_f32_16x16x32_bf16 v[124:127], a[124:127], a[156:159], v[124:127]\n"
    "s_waitcnt lgkmcnt(15)\n"
    "s_nop 0\n"
    "v_mfma_f32_16x16x32_bf16 v[0:3], a[0:3], a[160:163], v[0:3]\n"
    "v_mfma_f32_16x16x32_bf16 v[4:7], a[0:3], a[164:167], v[4:7]\n"
    "s_waitcnt vmcnt(12) lgkmcnt(0)\n"
    "s_barrier\n"
    "v_mfma_f32_16x16x32_bf16 v[8:11], a[4:7], a[160:163], v[8:11]\n"
    "ds_read_b64_tr_b16 a[64:65], v136 offset:16384\n"
    "ds_read_b64_tr_b16 a[66:67], v136 offset:20480\n"
    "v_mfma_f32_16x16x32_bf16 v[12:15], a[4:7], a[164:167], v[12:15]\n"
    "ds_read_b64_tr_b16 a[68:69], v136 offset:24576\n"
    "ds_read_b64_tr_b16 a[70:71], v136 offset:28672\n"
    "s_add_u32 s56, s56, 4096\n"
    "v_mfma_f32_16x16x32_bf16 v[16:19], a[8:11], a[160:163], v[16:19]\n"
    "buffer_load_dwordx4 a[192:195], v146, s[48:51], s56 offen offset:0\n"
    "ds_read_b64_tr_b16 a[72:73], v137 offset:16384\n"
    "ds_read_b64_tr_b16 a[74:75], v137 offset:20480\n"
    "v_mfma_f32_16x16x32_bf16 v[20:23], a[8:11], a[164:167], v[20:23]\n"
    "ds_read_b64_tr_b16 a[76:77], v137 offset:24576\n"
    "ds_read_b64_tr_b16 a[78:79], v137 offset:28672\n"
    "v_mfma_f32_16x16x32_bf16 v[24:27], a[12:15], a[160:163], v[24:27]\n"
    "ds_read_b64_tr_b16 a[80:81], v138 offset:16384\n"
    "ds_read_b64_tr_b16 a[82:83], v138 offset:20480\n"
    "v_mfma_f32_16x16x32_bf16 v[28:31], a[12:15], a[164:167], v[28:31]\n"
    "ds_read_b64_tr_b16 a[84:85], v138 offset:24576\n"
    "ds_read_b64_tr_b16 a[86:87], v138 offset:28672\n"
    "v_mfma_f32_16x16x32_bf16 v[32:35], a[16:19], a[160:163], v[32:35]\n"
    "buffer_load_dwordx4 a[196:199], v147, s[48:51], s56 offen offset:0\n"
    "ds_read_b64_tr_b16 a[88:89], v139 offset:16384\n"
    "v_mfma_f32_16x16x32_bf16 v[36:39], a[16:19], a[164:167], v[36:39]\n"
    "ds_read_b64_tr_b16 a[90:91], v139 offset:20480\n"
    "ds_read_b64_tr_b16 a[92:93], v139 offset:24576\n"
    "v_mfma_f32_16x16x32_bf16 v[40:43], a[20:23], a[160:163], v[40:43]\n"
    "ds_read_b64_tr_b16 a[94:95], v139 offset:28672\n"
    "ds_read_b64_tr_b16 a[96:97], v140 offset:16384\n"
    "v_mfma_f32_16x16x32_bf16 v[44:47], a[20:23], a[164:167], v[44:47]\n"
    "ds_read_b64_tr_b16 a[98:99], v140 offset:20480\n"
    "ds_read_b64_tr_b16 a[100:101], v140 offset:24576\n"
    "v_mfma_f32_16x16x32_bf16 v[48:51], a[24:27], a[160:163], v[48:51]\n"
    "ds_read_b64_tr_b16 a[102:103], v140 offset:28672\n"
    "s_add_u32 s55, s55, 114688\n"
    "s_add_u32 m0, s57, 0\n"
    "v_mfma_f32_16x16x32_bf16 v[52:55], a[24:27], a[164:167], v[52:55]\n"
    "buffer_load_dwordx4 v144, s[40:43], s55 offen offset:0 lds\n"
    "ds_read_b64_tr_b16 a[104:105], v141 offset:16384\n"
    "v_mfma_f32_16x16x32_bf16 v[56:59], a[28:31], a[160:163], v[56:59]\n"
    "ds_read_b64_tr_b16 a[106:107], v141 offset:20480\n"
    "ds_read_b64_tr_b16 a[108:109], v141 offset:24576\n"
    "v_mfma_f32_16x16x32_bf16 v[60:63], a[28:31], a[164:167], v[60:63]\n"
    "ds_read_b64_tr_b16 a[110:111], v141 offset:28672\n"
    "ds_read_b64_tr_b16 a[112:113], v142 offset:16384\n"
    "v_mfma_f32_16x16x32_bf16 v[64:67], a[32:35], a[160:163], v[64:67]\n"
    "ds_read_b64_tr_b16 a[114:115], v142 offset:20480\n"
    "ds_read_b64_tr_b16 a[116:117], v142 offset:24576\n"
    "v_mfma_f32_16x16x32_bf16 v[68:71], a[32:35], a[164:167], v[68:71]\n"
    "buffer_load_dwordx4 v145, s[40:43], s55 offen offset:1024 lds\n"
    "ds_read_b64_tr_b16 a[118:119], v142 offset:28672\n"
    "v_mfma_f32_16x16x32_bf16 v[72:75], a[36:39], a[160:163], v[72:75]\n"
    "ds_read_b64_tr_b16 a[120:121], v143 offset:16384\n"
    "ds_read_b64_tr_b16 a[122:123], v143 offset:20480\n"
    "v_mfma_f32_16x16x32_bf16 v[76:79], a[36:39], a[164:167], v[76:79]\n"
    "ds_read_b64_tr_b16 a[124:125], v143 offset:24576\n"
    "ds_read_b64_tr_b16 a[126:127], v143 offset:28672\n"
    "v_mfma_f32_16x16x32_bf16 v[80:83], a[40:43], a[160:163], v[80:83]\n"
    "v_mfma_f32_16x16x32_bf16 v[84:87], a[40:43], a[164:167], v[84:87]\n"
    "s_add_u32 m0, s57, 8192\n"
    "v_mfma_f32_16x16x32_bf16 v[88:91], a[44:47], a[160:163], v[88:91]\n"
    "buffer_load_dwordx4 v144, s[44:47], s55 offen offset:0 lds\n"
    "v_mfma_f32_16x16x32_bf16 v[92:95], a[44:47], a[164:167], v[92:95]\n"
    "v_mfma_f32_16x16x32_bf16 v[96:99], a[48:51], a[160:163], v[96:99]\n"
    "v_mfma_f32_16x16x32_bf16 v[100:103], a[48:51], a[164:167], v[100:103]\n"
    "v_mfma_f32_16x16x32_bf16 v[104:107], a[52:55], a[160:163], v[104:107]\n"
    "buffer_load_dwordx4 v145, s[44:47], s55 offen offset:1024 lds\n"
    "v_mfma_f32_16x16x32_bf16 v[108:111], a[52:55], a[164:167], v[108:111]\n"
    "v_mfma_f32_16x16x32_bf16 v[112:115], a[56:59], a[160:163], v[112:115]\n"
    "v_mfma_f32_16x16x32_bf16 v[116:119], a[56:59], a[164:167], v[116:119]\n"
    "v_mfma_f32_16x16x32_bf16 v[120:123], a[60:63], a[160:163], v[120:123]\n"
    "v_mfma_f32_16x16x32_bf16 v[124:127], a[60:63], a[164:167], v[124:127]\n"
    "s_waitcnt lgkmcnt(15)\n"
    "v_mfma_f32_16x16x32_bf16 v[0:3], a[64:67], a[168:171], v[0:3]\n"
    "v_mfma_f32_16x16x32_bf16 v[4:7], a[64:67], a[172:175], v[4:7]\n"
    "s_waitcnt vmcnt(12) lgkmcnt(0)\n"
    "s_barrier\n"
    "v_mfma_f32_16x16x32_bf16 v[8:11], a[68:71], a[168:171], v[8:11]\n"
    "ds_read_b64_tr_b16 a[0:1], v136 offset:32768\n"
    "ds_read_b64_tr_b16 a[2:3], v136 offset:36864\n"
    "v_mfma_f32_16x16x32_bf16 v[12:15], a[68:71], a[172:175], v[12:15]\n"
    "ds_read_b64_tr_b16 a[4:5], v136 offset:40960\n"
    "ds_read_b64_tr_b16 a[6:7], v136 offset:45056\n"
    "v_mfma_f32_16x16x32_bf16 v[16:19], a[72:75], a[168:171], v[16:19]\n"
    "buffer_load_dwordx4 a[200:203], v146, s[48:51], s56 offen offset:1024\n"
    "ds_read_b64_tr_b16 a[8:9], v137 offset:32768\n"
    "ds_read_b64_tr_b16 a[10:11], v137 offset:36864\n"
    "v_mfma_f32_16x16x32_bf16 v[20:23], a[72:75], a[172:175], v[20:23]\n"
    "ds_read_b64_tr_b16 a[12:13], v137 offset:40960\n"
    "ds_read_b64_tr_b16 a[14:15], v137 offset:45056\n"
    "v_mfma_f32_16x16x32_bf16 v[24:27], a[76:79], a[168:171], v[24:27]\n"
    "ds_read_b64_tr_b16 a[16:17], v138 offset:32768\n"
    "ds_read_b64_tr_b16 a[18:19], v138 offset:36864\n"
    "v_mfma_f32_16x16x32_bf16 v[28:31], a[76:79], a[172:175], v[28:31]\n"
    "ds_read_b64_tr_b16 a[20:21], v138 offset:40960\n"
    "ds_read_b64_tr_b16 a[22:23], v138 offset:45056\n"
    "v_mfma_f32_16x16x32_bf16 v[32:35], a[80:83], a[168:171], v[32:35]\n"
    "buffer_load_dwordx4 a[204:207], v147, s[48:51], s56 offen offset:1024\n"
    "ds_read_b64_tr_b16 a[24:25], v139 offset:32768\n"
    "v_mfma_f32_16x16x32_bf16 v[36:39], a[80:83], a[172:175], v[36:39]\n"
    "ds_read_b64_tr_b16 a[26:27], v139 offset:36864\n"
    "ds_read_b64_tr_b16 a[28:29], v139 offset:40960\n"
    "v_mfma_f32_16x16x32_bf16 v[40:43], a[84:87], a[168:171], v[40:43]\n"
    "ds_read_b64_tr_b16 a[30:31], v139 offset:45056\n"
    "ds_read_b64_tr_b16 a[32:33], v140 offset:32768\n"
    "v_mfma_f32_16x16x32_bf16 v[44:47], a[84:87], a[172:175], v[44:47]\n"
    "ds_read_b64_tr_b16 a[34:35], v140 offset:36864\n"
    "ds_read_b64_tr_b16 a[36:37], v140 offset:40960\n"
    "v_mfma_f32_16x16x32_bf16 v[48:51], a[88:91], a[168:171], v[48:51]\n"
    "ds_read_b64_tr_b16 a[38:39], v140 offset:45056\n"
    "s_add_u32 s55, s55, 114688\n"
    "s_add_u32 m0, s57, 16384\n"
    "v_mfma_f32_16x16x32_bf16 v[52:55], a[88:91], a[172:175], v[52:55]\n"
    "buffer_load_dwordx4 v144, s[40:43], s55 offen offset:0 lds\n"
    "ds_read_b64_tr_b16 a[40:41], v141 offset:32768\n"
    "v_mfma_f32_16x16x32_bf16 v[56:59], a[92:95], a[168:171], v[56:59]\n"
    "ds_read_b64_tr_b16 a[42:43], v141 offset:36864\n"
    "ds_read_b64_tr_b16 a[44:45], v141 offset:40960\n"
    "v_mfma_f32_16x16x32_bf16 v[60:63], a[92:95], a[172:175], v[60:63]\n"
    "ds_read_b64_tr_b16 a[46:47], v141 offset:45056\n"
    "ds_read_b64_tr_b16 a[48:49], v142 offset:32768\n"
    "v_mfma_f32_16x16x32_bf16 v[64:67], a[96:99], a[168:171], v[64:67]\n"
    "ds_read_b64_tr_b16 a[50:51], v142 offset:36864\n"
    "ds_read_b64_tr_b16 a[52:53], v142 offset:40960\n"
    "v_mfma_f32_16x16x32_bf16 v[68:71], a[96:99], a[172:175], v[68:71]\n"
    "buffer_load_dwordx4 v145, s[40:43], s55 offen offset:1024 lds\n"
    "ds_read_b64_tr_b16 a[54:55], v142 offset:45056\n"
    "v_mfma_f32_16x16x32_bf16 v[72:75], a[100:103], a[168:171], v[72:75]\n"
    "ds_read_b64_tr_b16 a[56:57], v143 offset:32768\n"
    "ds_read_b64_tr_b16 a[58:59], v143 offset:36864\n"
    "v_mfma_f32_16x16x32_bf16 v[76:79], a[100:103], a[172:175], v[76:79]\n"
    "ds_read_b64_tr_b16 a[60:61], v143 offset:40960\n"
    "ds_read_b64_tr_b16 a[62:63], v143 offset:45056\n"
    "v_mfma_f32_16x16x32_bf16 v[80:83], a[104:107], a[168:171], v[80:83]\n"
    "v_mfma_f32_16x16x32_bf16 v[84:87], a[104:107], a[172:175], v[84:87]\n"
    "s_add_u32 m0, s57, 24576\n"
    "v_mfma_f32_16x16x32_bf16 v[88:91], a[108:111], a[168:171], v[88:91]\n"
    "buffer_load_dwordx4 v144, s[44:47], s55 offen offset:0 lds\n"
    "v_mfma_f32_16x16x32_bf16 v[92:95], a[108:111], a[172:175], v[92:95]\n"
    "v_mfma_f32_16x16x32_bf16 v[96:99], a[112:115], a[168:171], v[96:99]\n"
    "v_mfma_f32_16x16x32_bf16 v[100:103], a[112:115], a[172:175], v[100:103]\n"
    "v_mfma_f32_16x16x32_bf16 v[104:107], a[116:119], a[168:171], v[104:107]\n"
    "buffer_load_dwordx4 v145, s[44:47], s55 offen offset:1024 lds\n"
    "v_mfma_f32_16x16x32_bf16 v[108:111], a[116:119], a[172:175], v[108:111]\n"
    "v_mfma_f32_16x16x32_bf16 v[112:115], a[120:123], a[168:171], v[112:115]\n"
    "v_mfma_f32_16x16x32_bf16 v[116:119], a[120:123], a[172:175], v[116:119]\n"
    "v_mfma_f32_16x16x32_bf16 v[120:123], a[124:127], a[168:171], v[120:123]\n"
    "v_mfma_f32_16x16x32_bf16 v[124:127], a[124:127], a[172:175], v[124:127]\n"
    "s_waitcnt lgkmcnt(15)\n"
    "s_nop 0\n"
    "v_mfma_f32_16x16x32_bf16 v[0:3], a[0:3], a[176:179], v[0:3]\n"
    "v_mfma_f32_16x16x32_bf16 v[4:7], a[0:3], a[180:183], v[4:7]\n"
    "s_waitcnt vmcnt(12) lgkmcnt(0)\n"
    "s_barrier\n"
    "v_mfma_f32_16x16x32_bf16 v[8:11], a[4:7], a[176:179], v[8:11]\n"
    "ds_read_b64_tr_b16 a[64:65], v136 offset:49152\n"
    "ds_read_b64_tr_b16 a[66:67], v136 offset:53248\n"
    "v_mfma_f32_16x16x32_bf16 v[12:15], a[4:7], a[180:183], v[12:15]\n"
    "ds_read_b64_tr_b16 a[68:69], v136 offset:57344\n"
    "ds_read_b64_tr_b16 a[70:71], v136 offset:61440\n"
    "v_mfma_f32_16x16x32_bf16 v[16:19], a[8:11], a[176:179], v[16:19]\n"
    "buffer_load_dwordx4 a[208:211], v146, s[48:51], s56 offen offset:2048\n"
    "ds_read_b64_tr_b16 a[72:73], v137 offset:49152\n"
    "ds_read_b64_tr_b16 a[74:75], v137 offset:53248\n"
    "v_mfma_f32_16x16x32_bf16 v[20:23], a[8:11], a[180:183], v[20:23]\n"
    "ds_read_b64_tr_b16 a[76:77], v137 offset:57344\n"
    "ds_read_b64_tr_b16 a[78:79], v137 offset:61440\n"
    "v_mfma_f32_16x16x32_bf16 v[24:27], a[12:15], a[176:179], v[24:27]\n"
    "ds_read_b64_tr_b16 a[80:81], v138 offset:49152\n"
    "ds_read_b64_tr_b16 a[82:83], v138 offset:53248\n"
    "v_mfma_f32_16x16x32_bf16 v[28:31], a[12:15], a[180:183], v[28:31]\n"
    "ds_read_b64_tr_b16 a[84:85], v138 offset:57344\n"
    "ds_read_b64_tr_b16 a[86:87], v138 offset:61440\n"
    "v_mfma_f32_16x16x32_bf16 v[32:35], a[16:19], a[176:179], v[32:35]\n"
    "buffer_load_dwordx4 a[212:215], v147, s[48:51], s56 offen offset:2048\n"
    "ds_read_b64_tr_b16 a[88:89], v139 offset:49152\n"
    "v_mfma_f32_16x16x32_bf16 v[36:39], a[16:19], a[180:183], v[36:39]\n"
    "ds_read_b64_tr_b16 a[90:91], v139 offset:53248\n"
    "ds_read_b64_tr_b16 a[92:93], v139 offset:57344\n"
    "v_mfma_f32_16x16x32_bf16 v[40:43], a[20:23], a[176:179], v[40:43]\n"
    "ds_read_b64_tr_b16 a[94:95], v139 offset:61440\n"
    "ds_read_b64_tr_b16 a[96:97], v140 offset:49152\n"
    "v_mfma_f32_16x16x32_bf16 v[44:47], a[20:23], a[180:183], v[44:47]\n"
    "ds_read_b64_tr_b16 a[98:99], v140 offset:53248\n"
    "ds_read_b64_tr_b16 a[100:101], v140 offset:57344\n"
    "v_mfma_f32_16x16x32_bf16 v[48:51], a[24:27], a[176:179], v[48:51]\n"
    "ds_read_b64_tr_b16 a[102:103], v140 offset:61440\n"
    "s_add_u32 s55, s55, 114688\n"
    "s_add_u32 m0, s57, 32768\n"
    "v_mfma_f32_16x16x32_bf16 v[52:55], a[24:27], a[180:183], v[52:55]\n"
    "buffer_load_dwordx4 v144, s[40:43], s55 offen offset:0 lds\n"
    "ds_read_b64_tr_b16 a[104:105], v141 offset:49152\n"
    "v_mfma_f32_16x16x32_bf16 v[56:59], a[28:31], a[176:179], v[56:59]\n"
    "ds_read_b64_tr_b16 a[106:107], v141 offset:53248\n"
    "ds_read_b64_tr_b16 a[108:109], v141 offset:57344\n"
    "v_mfma_f32_16x16x32_bf16 v[60:63], a[28:31], a[180:183], v[60:63]\n"
    "ds_read_b64_tr_b16 a[110:111], v141 offset:61440\n"
    "ds_read_b64_tr_b16 a[112:113], v142 offset:49152\n"
    "v_mfma_f32_16x16x32_bf16 v[64:67], a[32:35], a[176:179], v[64:67]\n"
    "ds_read_b64_tr_b16 a[114:115], v142 offset:53248\n"
    "ds_read_b64_tr_b16 a[116:117], v142 offset:57344\n"
    "v_mfma_f32_16x16x32_bf16 v[68:71], a[32:35], a[180:183], v[68:71]\n"
    "buffer_load_dwordx4 v145, s[40:43], s55 offen offset:1024 lds\n"
    "ds_read_b64_tr_b16 a[118:119], v142 offset:61440\n"
    "v_mfma_f32_16x16x32_bf16 v[72:75], a[36:39], a[176:179], v[72:75]\n"
    "ds_read_b64_tr_b16 a[120:121], v143 offset:49152\n"
    "ds_read_b64_tr_b16 a[122:123], v143 offset:53248\n"
    "v_mfma_f32_16x16x32_bf16 v[76:79], a[36:39], a[180:183], v[76:79]\n"
    "ds_read_b64_tr_b16 a[124:125], v143 offset:57344\n"
    "ds_read_b64_tr_b16 a[126:127], v143 offset:61440\n"
    "v_mfma_f32_16x16x32_bf16 v[80:83], a[40:43], a[176:179], v[80:83]\n"
    "v_mfma_f32_16x16x32_bf16 v[84:87], a[40:43], a[180:183], v[84:87]\n"
    "s_add_u32 m0, s57, 40960\n"
    "v_mfma_f32_16x16x32_bf16 v[88:91], a[44:47], a[176:179], v[88:91]\n"
    "buffer_load_dwordx4 v144, s[44:47], s55 offen offset:0 lds\n"
    "v_mfma_f32_16x16x32_bf16 v[92:95], a[44:47], a[180:183], v[92:95]\n"
    "v_mfma_f32_16x16x32_bf16 v[96:99], a[48:51], a[176:179], v[96:99]\n"
    "v_mfma_f32_16x16x32_bf16 v[100:103], a[48:51], a[180:183], v[100:103]\n"
    "v_mfma_f32_16x16x32_bf16 v[104:107], a[52:55], a[176:179], v[104:107]\n"
    "buffer_load_dwordx4 v145, s[44:47], s55 offen offset:1024 lds\n"
    "v_mfma_f32_16x16x32_bf16 v[108:111], a[52:55], a[180:183], v[108:111]\n"
    "v_mfma_f32_16x16x32_bf16 v[112:115], a[56:59], a[176:179], v[112:115]\n"
    "v_mfma_f32_16x16x32_bf16 v[116:119], a[56:59], a[180:183], v[116:119]\n"
    "v_mfma_f32_16x16x32_bf16 v[120:123], a[60:63], a[176:179], v[120:123]\n"
    "v_mfma_f32_16x16x32_bf16 v[124:127], a[60:63], a[180:183], v[124:127]\n"
    "s_waitcnt lgkmcnt(15)\n"
    "s_nop 0\n"
    "v_mfma_f32_16x16x32_bf16 v[0:3], a[64:67], a[184:187], v[0:3]\n"
    "v_mfma_f32_16x16x32_bf16 v[4:7], a[64:67], a[188:191], v[4:7]\n"
    "s_waitcnt vmcnt(12) lgkmcnt(0)\n"
    "s_barrier\n"
    "v_mfma_f32_16x16x32_bf16 v[8:11], a[68:71], a[184:187], v[8:11]\n"
    "ds_read_b64_tr_b16 a[0:1], v128 offset:0\n"
    "ds_read_b64_tr_b16 a[2:3], v128 offset:4096\n"
    "v_mfma_f32_16x16x32_bf16 v[12:15], a[68:71], a[188:191], v[12:15]\n"
    "ds_read_b64_tr_b16 a[4:5], v128 offset:8192\n"
    "ds_read_b64_tr_b16 a[6:7], v128 offset:12288\n"
    "v_mfma_f32_16x16x32_bf16 v[16:19], a[72:75], a[184:187], v[16:19]\n"
    "buffer_load_dwordx4 a[216:219], v146, s[48:51], s56 offen offset:3072\n"
    "ds_read_b64_tr_b16 a[8:9], v129 offset:0\n"
    "ds_read_b64_tr_b16 a[10:11], v129 offset:4096\n"
    "v_mfma_f32_16x16x32_bf16 v[20:23], a[72:75], a[188:191], v[20:23]\n"
    "ds_read_b64_tr_b16 a[12:13], v129 offset:8192\n"
    "ds_read_b64_tr_b16 a[14:15], v129 offset:12288\n"
    "v_mfma_f32_16x16x32_bf16 v[24:27], a[76:79], a[184:187], v[24:27]\n"
    "ds_read_b64_tr_b16 a[16:17], v130 offset:0\n"
    "ds_read_b64_tr_b16 a[18:19], v130 offset:4096\n"
    "v_mfma_f32_16x16x32_bf16 v[28:31], a[76:79], a[188:191], v[28:31]\n"
    "ds_read_b64_tr_b16 a[20:21], v130 offset:8192\n"
    "ds_read_b64_tr_b16 a[22:23], v130 offset:12288\n"
    "v_mfma_f32_16x16x32_bf16 v[32:35], a[80:83], a[184:187], v[32:35]\n"
    "buffer_load_dwordx4 a[220:223], v147, s[48:51], s56 offen offset:3072\n"
    "ds_read_b64_tr_b16 a[24:25], v131 offset:0\n"
    "v_mfma_f32_16x16x32_bf16 v[36:39], a[80:83], a[188:191], v[36:39]\n"
    "ds_read_b64_tr_b16 a[26:27], v131 offset:4096\n"
    "ds_read_b64_tr_b16 a[28:29], v131 offset:8192\n"
    "v_mfma_f32_16x16x32_bf16 v[40:43], a[84:87], a[184:187], v[40:43]\n"
    "ds_read_b64_tr_b16 a[30:31], v131 offset:12288\n"
    "ds_read_b64_tr_b16 a[32:33], v132 offset:0\n"
    "v_mfma_f32_16x16x32_bf16 v[44:47], a[84:87], a[188:191], v[44:47]\n"
    "ds_read_b64_tr_b16 a[34:35], v132 offset:4096\n"
    "ds_read_b64_tr_b16 a[36:37], v132 offset:8192\n"
    "v_mfma_f32_16x16x32_bf16 v[48:51], a[88:91], a[184:187], v[48:51]\n"
    "ds_read_b64_tr_b16 a[38:39], v132 offset:12288\n"
    "s_add_u32 s55, s55, 114688\n"
    "s_add_u32 m0, s57, 49152\n"
    "v_mfma_f32_16x16x32_bf16 v[52:55], a[88:91], a[188:191], v[52:55]\n"
    "buffer_load_dwordx4 v144, s[40:43], s55 offen offset:0 lds\n"
    "ds_read_b64_tr_b16 a[40:41], v133 offset:0\n"
    "v_mfma_f32_16x16x32_bf16 v[56:59], a[92:95], a[184:187], v[56:59]\n"
    "ds_read_b64_tr_b16 a[42:43], v133 offset:4096\n"
    "ds_read_b64_tr_b16 a[44:45], v133 offset:8192\n"
    "v_mfma_f32_16x16x32_bf16 v[60:63], a[92:95], a[188:191], v[60:63]\n"
    "ds_read_b64_tr_b16 a[46:47], v133 offset:12288\n"
    "ds_read_b64_tr_b16 a[48:49], v134 offset:0\n"
    "v_mfma_f32_16x16x32_bf16 v[64:67], a[96:99], a[184:187], v[64:67]\n"
    "ds_read_b64_tr_b16 a[50:51], v134 offset:4096\n"
    "ds_read_b64_tr_b16 a[52:53], v134 offset:8192\n"
    "v_mfma_f32_16x16x32_bf16 v[68:71], a[96:99], a[188:191], v[68:71]\n"
    "buffer_load_dwordx4 v145, s[40:43], s55 offen offset:1024 lds\n"
    "ds_read_b64_tr_b16 a[54:55], v134 offset:12288\n"
    "v_mfma_f32_16x16x32_bf16 v[72:75], a[100:103], a[184:187], v[72:75]\n"
    "ds_read_b64_tr_b16 a[56:57], v135 offset:0\n"
    "ds_read_b64_tr_b16 a[58:59], v135 offset:4096\n"
    "v_mfma_f32_16x16x32_bf16 v[76:79], a[100:103], a[188:191], v[76:79]\n"
    "ds_read_b64_tr_b16 a[60:61], v135 offset:8192\n"
    "ds_read_b64_tr_b16 a[62:63], v135 offset:12288\n"
    "v_mfma_f32_16x16x32_bf16 v[80:83], a[104:107], a[184:187], v[80:83]\n"
    "v_mfma_f32_16x16x32_bf16 v[84:87], a[104:107], a[188:191], v[84:87]\n"
    "s_add_u32 m0, s57, 57344\n"
    "v_mfma_f32_16x16x32_bf16 v[88:91], a[108:111], a[184:187], v[88:91]\n"
    "buffer_load_dwordx4 v144, s[44:47], s55 offen offset:0 lds\n"
    "v_mfma_f32_16x16x32_bf16 v[92:95], a[108:111], a[188:191], v[92:95]\n"
    "v_mfma_f32_16x16x32_bf16 v[96:99], a[112:115], a[184:187], v[96:99]\n"
    "v_mfma_f32_16x16x32_bf16 v[100:103], a[112:115], a[188:191], v[100:103]\n"
    "v_mfma_f32_16x16x32_bf16 v[104:107], a[116:119], a[184:187], v[104:107]\n"
    "buffer_load_dwordx4 v145, s[44:47], s55 offen offset:1024 lds\n"
    "v_mfma_f32_16x16x32_bf16 v[108:111], a[116:119], a[188:191], v[108:111]\n"
    "v_mfma_f32_16x16x32_bf16 v[112:115], a[120:123], a[184:187], v[112:115]\n"
    "v_mfma_f32_16x16x32_bf16 v[116:119], a[120:123], a[188:191], v[116:119]\n"
    "v_mfma_f32_16x16x32_bf16 v[120:123], a[124:127], a[184:187], v[120:123]\n"
    "v_mfma_f32_16x16x32_bf16 v[124:127], a[124:127], a[188:191], v[124:127]\n"
    "s_waitcnt lgkmcnt(15)\n"
    "s_nop 0\n"
    "v_mfma_f32_16x16x32_bf16 v[0:3], a[0:3], a[192:195], v[0:3]\n"
    "v_mfma_f32_16x16x32_bf16 v[4:7], a[0:3], a[196:199], v[4:7]\n"
    "s_waitcnt vmcnt(12) lgkmcnt(0)\n"
    "s_barrier\n"
    "v_mfma_f32_16x16x32_bf16 v[8:11], a[4:7], a[192:195], v[8:11]\n"
    "ds_read_b64_tr_b16 a[64:65], v128 offset:16384\n"
    "ds_read_b64_tr_b16 a[66:67], v128 offset:20480\n"
    "v_mfma_f32_16x16x32_bf16 v[12:15], a[4:7], a[196:199], v[12:15]\n"
    "ds_read_b64_tr_b16 a[68:69], v128 offset:24576\n"
    "ds_read_b64_tr_b16 a[70:71], v128 offset:28672\n"
    "s_add_u32 s56, s56, 4096\n"
    "v_mfma_f32_16x16x32_bf16 v[16:19], a[8:11], a[192:195], v[16:19]\n"
    "buffer_load_dwordx4 a[224:227], v146, s[48:51], s56 offen offset:0\n"
    "ds_read_b64_tr_b16 a[72:73], v129 offset:16384\n"
    "ds_read_b64_tr_b16 a[74:75], v129 offset:20480\n"
    "v_mfma_f32_16x16x32_bf16 v[20:23], a[8:11], a[196:199], v[20:23]\n"
    "ds_read_b64_tr_b16 a[76:77], v129 offset:24576\n"
    "ds_read_b64_tr_b16 a[78:79], v129 offset:28672\n"
    "v_mfma_f32_16x16x32_bf16 v[24:27], a[12:15], a[192:195], v[24:27]\n"
    "ds_read_b64_tr_b16 a[80:81], v130 offset:16384\n"
    "ds_read_b64_tr_b16 a[82:83], v130 offset:20480\n"
    "v_mfma_f32_16x16x32_bf16 v[28:31], a[12:15], a[196:199], v[28:31]\n"
    "ds_read_b64_tr_b16 a[84:85], v130 offset:24576\n"
    "ds_read_b64_tr_b16 a[86:87], v130 offset:28672\n"
    "v_mfma_f32_16x16x32_bf16 v[32:35], a[16:19], a[192:195], v[32:35]\n"
    "buffer_load_dwordx4 a[228:231], v147, s[48:51], s56 offen offset:0\n"
    "ds_read_b64_tr_b16 a[88:89], v131 offset:16384\n"
    "v_mfma_f32_16x16x32_bf16 v[36:39], a[16:19], a[196:199], v[36:39]\n"
    "ds_read_b64_tr_b16 a[90:91], v131 offset:20480\n"
    "ds_read_b64_tr_b16 a[92:93], v131 offset:24576\n"
    "v_mfma_f32_16x16x32_bf16 v[40:43], a[20:23], a[192:195], v[40:43]\n"
    "ds_read_b64_tr_b16 a[94:95], v131 offset:28672\n"
    "ds_read_b64_tr_b16 a[96:97], v132 offset:16384\n"
    "v_mfma_f32_16x16x32_bf16 v[44:47], a[20:23], a[196:199], v[44:47]\n"
    "ds_read_b64_tr_b16 a[98:99], v132 offset:20480\n"
    "ds_read_b64_tr_b16 a[100:101], v132 offset:24576\n"
    "v_mfma_f32_16x16x32_bf16 v[48:51], a[24:27], a[192:195], v[48:51]\n"
    "ds_read_b64_tr_b16 a[102:103], v132 offset:28672\n"
    "s_add_u32 s55, s55, 114688\n"
    "s_add_u32 m0, s57, 65536\n"
    "v_mfma_f32_16x16x32_bf16 v[52:55], a[24:27], a[196:199], v[52:55]\n"
    "buffer_load_dwordx4 v144, s[40:43], s55 offen offset:0 lds\n"
    "ds_read_b64_tr_b16 a[104:105], v133 offset:16384\n"
    "v_mfma_f32_16x16x32_bf16 v[56:59], a[28:31], a[192:195], v[56:59]\n"
    "ds_read_b64_tr_b16 a[106:107], v133 offset:20480\n"
    "ds_read_b64_tr_b16 a[108:109], v133 offset:24576\n"
    "v_mfma_f32_16x16x32_bf16 v[60:63], a[28:31], a[196:199], v[60:63]\n"
    "ds_read_b64_tr_b16 a[110:111], v133 offset:28672\n"
    "ds_read_b64_tr_b16 a[112:113], v134 offset:16384\n"
    "v_mfma_f32_16x16x32_bf16 v[64:67], a[32:35], a[192:195], v[64:67]\n"
    "ds_read_b64_tr_b16 a[114:115], v134 offset:20480\n"
    "ds_read_b64_tr_b16 a[116:117], v134 offset:24576\n"
    "v_mfma_f32_16x16x32_bf16 v[68:71], a[32:35], a[196:199], v[68:71]\n"
    "buffer_load_dwordx4 v145, s[40:43], s55 offen offset:1024 lds\n"
    "ds_read_b64_tr_b16 a[118:119], v134 offset:28672\n"
    "v_mfma_f32_16x16x32_bf16 v[72:75], a[36:39], a[192:195], v[72:75]\n"
    "ds_read_b64_tr_b16 a[120:121], v135 offset:16384\n"
    "ds_read_b64_tr_b16 a[122:123], v135 offset:20480\n"
    "v_mfma_f32_16x16x32_bf16 v[76:79], a[36:39], a[196:199], v[76:79]\n"
    "ds_read_b64_tr_b16 a[124:125], v135 offset:24576\n"
    "ds_read_b64_tr_b16 a[126:127], v135 offset:28672\n"
    "v_mfma_f32_16x16x32_bf16 v[80:83], a[40:43], a[192:195], v[80:83]\n"
    "v_mfma_f32_16x16x32_bf16 v[84:87], a[40:43], a[196:199], v[84:87]\n"
    "s_add_u32 m0, s57, 73728\n"
    "v_mfma_f32_16x16x32_bf16 v[88:91], a[44:47], a[192:195], v[88:91]\n"
    "buffer_load_dwordx4 v144, s[44:47], s55 offen offset:0 lds\n"
    "v_mfma_f32_16x16x32_bf16 v[92:95], a[44:47], a[196:199], v[92:95]\n"
    "v_mfma_f32_16x16x32_bf16 v[96:99], a[48:51], a[192:195], v[96:99]\n"
    "v_mfma_f32_16x16x32_bf16 v[100:103], a[48:51], a[196:199], v[100:103]\n"
    "v_mfma_f32_16x16x32_bf16 v[104:107], a[52:55], a[192:195], v[104:107]\n"
    "buffer_load_dwordx4 v145, s[44:47], s55 offen offset:1024 lds\n"
    "v_mfma_f32_16x16x32_bf16 v[108:111], a[52:55], a[196:199], v[108:111]\n"
    "v_mfma_f32_16x16x32_bf16 v[112:115], a[56:59], a[192:195], v[112:115]\n"
    "v_mfma_f32_16x16x32_bf16 v[116:119], a[56:59], a[196:199], v[116:119]\n"
    "v_mfma_f32_16x16x32_bf16 v[120:123], a[60:63], a[192:195], v[120:123]\n"
    "v_mfma_f32_16x16x32_bf16 v[124:127], a[60:63], a[196:199], v[124:127]\n"
    "s_waitcnt lgkmcnt(15)\n"
    "s_nop 0\n"
    "v_mfma_f32_16x16x32_bf16 v[0:3], a[64:67], a[200:203], v[0:3]\n"
    "v_mfma_f32_16x16x32_bf16 v[4:7], a[64:67], a[204:207], v[4:7]\n"
    "s_waitcnt vmcnt(12) lgkmcnt(0)\n"
    "s_barrier\n"
    "v_mfma_f32_16x16x32_bf16 v[8:11], a[68:71], a[200:203], v[8:11]\n"
    "ds_read_b64_tr_b16 a[0:1], v128 offset:32768\n"
    "ds_read_b64_tr_b16 a[2:3], v128 offset:36864\n"
    "v_mfma_f32_16x16x32_bf16 v[12:15], a[68:71], a[204:207], v[12:15]\n"
    "ds_read_b64_tr_b16 a[4:5], v128 offset:40960\n"
    "ds_read_b64_tr_b16 a[6:7], v128 offset:45056\n"
    "v_mfma_f32_16x16x32_bf16 v[16:19], a[72:75], a[200:203], v[16:19]\n"
    "buffer_load_dwordx4 a[232:235], v146, s[48:51], s56 offen offset:1024\n"
    "ds_read_b64_tr_b16 a[8:9], v129 offset:32768\n"
    "ds_read_b64_tr_b16 a[10:11], v129 offset:36864\n"
    "v_mfma_f32_16x16x32_bf16 v[20:23], a[72:75], a[204:207], v[20:23]\n"
    "ds_read_b64_tr_b16 a[12:13], v129 offset:40960\n"
    "ds_read_b64_tr_b16 a[14:15], v129 offset:45056\n"
    "v_mfma_f32_16x16x32_bf16 v[24:27], a[76:79], a[200:203], v[24:27]\n"
    "ds_read_b64_tr_b16 a[16:17], v130 offset:32768\n"
    "ds_read_b64_tr_b16 a[18:19], v130 offset:36864\n"
    "v_mfma_f32_16x16x32_bf16 v[28:31], a[76:79], a[204:207], v[28:31]\n"
    "ds_read_b64_tr_b16 a[20:21], v130 offset:40960\n"
    "ds_read_b64_tr_b16 a[22:23], v130 offset:45056\n"
    "v_mfma_f32_16x16x32_bf16 v[32:35], a[80:83], a[200:203], v[32:35]\n"
    "buffer_load_dwordx4 a[236:239], v147, s[48:51], s56 offen offset:1024\n"
    "ds_read_b64_tr_b16 a[24:25], v131 offset:32768\n"
    "v_mfma_f32_16x16x32_bf16 v[36:39], a[80:83], a[204:207], v[36:39]\n"
    "ds_read_b64_tr_b16 a[26:27], v131 offset:36864\n"
    "ds_read_b64_tr_b16 a[28:29], v131 offset:40960\n"
    "v_mfma_f32_16x16x32_bf16 v[40:43], a[84:87], a[200:203], v[40:43]\n"
    "ds_read_b64_tr_b16 a[30:31], v131 offset:45056\n"
    "ds_read_b64_tr_b16 a[32:33], v132 offset:32768\n"
    "v_mfma_f32_16x16x32_bf16 v[44:47], a[84:87], a[204:207], v[44:47]\n"
    "ds_read_b64_tr_b16 a[34:35], v132 offset:36864\n"
    "ds_read_b64_tr_b16 a[36:37], v132 offset:40960\n"
    "v_mfma_f32_16x16x32_bf16 v[48:51], a[88:91], a[200:203], v[48:51]\n"
    "ds_read_b64_tr_b16 a[38:39], v132 offset:45056\n"
    "s_add_u32 s55, s55, 114688\n"
    "s_add_u32 m0, s57, 81920\n"
    "v_mfma_f32_16x16x32_bf16 v[52:55], a[88:91], a[204:207], v[52:55]\n"
    "buffer_load_dwordx4 v144, s[40:43], s55 offen offset:0 lds\n"
    "ds_read_b64_tr_b16 a[40:41], v133 offset:32768\n"
    "v_mfma_f32_16x16x32_bf16 v[56:59], a[92:95], a[200:203], v[56:59]\n"
    "ds_read_b64_tr_b16 a[42:43], v133 offset:36864\n"
    "ds_read_b64_tr_b16 a[44:45], v133 offset:40960\n"
    "v_mfma_f32_16x16x32_bf16 v[60:63], a[92:95], a[204:207], v[60:63]\n"
    "ds_read_b64_tr_b16 a[46:47], v133 offset:45056\n"
    "ds_read_b64_tr_b16 a[48:49], v134 offset:32768\n"
    "v_mfma_f32_16x16x32_bf16 v[64:67], a[96:99], a[200:203], v[64:67]\n"
    "ds_read_b64_tr_b16 a[50:51], v134 offset:36864\n"
    "ds_read_b64_tr_b16 a[52:53], v134 offset:40960\n"
    "v_mfma_f32_16x16x32_bf16 v[68:71], a[96:99], a[204:207], v[68:71]\n"
    "buffer_load_dwordx4 v145, s[40:43], s55 offen offset:1024 lds\n"
    "ds_read_b64_tr_b16 a[54:55], v134 offset:45056\n"
    "v_mfma_f32_16x16x32_bf16 v[72:75], a[100:103], a[200:203], v[72:75]\n"
    "ds_read_b64_tr_b16 a[56:57], v135 offset:32768\n"
    "ds_read_b64_tr_b16 a[58:59], v135 offset:36864\n"
    "v_mfma_f32_16x16x32_bf16 v[76:79], a[100:103], a[204:207], v[76:79]\n"
    "ds_read_b64_tr_b16 a[60:61], v135 offset:40960\n"
    "ds_read_b64_tr_b16 a[62:63], v135 offset:45056\n"
    "v_mfma_f32_16x16x32_bf16 v[80:83], a[104:107], a[200:203], v[80:83]\n"
    "v_mfma_f32_16x16x32_bf16 v[84:87], a[104:107], a[204:207], v[84:87]\n"
    "s_add_u32 m0, s57, 90112\n"
    "v_mfma_f32_16x16x32_bf16 v[88:91], a[108:111], a[200:203], v[88:91]\n"
    "buffer_load_dwordx4 v144, s[44:47], s55 offen offset:0 lds\n"
    "v_mfma_f32_16x16x32_bf16 v[92:95], a[108:111], a[204:207], v[92:95]\n"
    "v_mfma_f32_16x16x32_bf16 v[96:99], a[112:115], a[200:203], v[96:99]\n"
    "v_mfma_f32_16x16x32_bf16 v[100:103], a[112:115], a[204:207], v[100:103]\n"
    "v_mfma_f32_16x16x32_bf16 v[104:107], a[116:119], a[200:203], v[104:107]\n"
    "buffer_load_dwordx4 v145, s[44:47], s55 offen offset:1024 lds\n"
    "v_mfma_f32_16x16x32_bf16 v[108:111], a[116:119], a[204:207], v[108:111]\n"
    "v_mfma_f32_16x16x32_bf16 v[112:115], a[120:123], a[200:203], v[112:115]\n"
    "v_mfma_f32_16x16x32_bf16 v[116:119], a[120:123], a[204:207], v[116:119]\n"
    "v_mfma_f32_16x16x32_bf16 v[120:123], a[124:127], a[200:203], v[120:123]\n"
    "v_mfma_f32_16x16x32_bf16 v[124:127], a[124:127], a[204:207], v[124:127]\n"
    "s_waitcnt lgkmcnt(15)\n"
    "s_nop 0\n"
    "v_mfma_f32_16x16x32_bf16 v[0:3], a[0:3], a[208:211], v[0:3]\n"
    "v_mfma_f32_16x16x32_bf16 v[4:7], a[0:3], a[212:215], v[4:7]\n"
    "s_waitcnt vmcnt(12) lgkmcnt(0)\n"
    "s_barrier\n"
    "v_mfma_f32_16x16x32_bf16 v[8:11], a[4:7], a[208:211], v[8:11]\n"
    "ds_read_b64_tr_b16 a[64:65], v128 offset:49152\n"
    "ds_read_b64_tr_b16 a[66:67], v128 offset:53248\n"
    "s_add_u32 s74, s72, 0\n"
    "s_add_u32 m0, s73, 98304\n"
    "v_mfma_f32_16x16x32_bf16 v[12:15], a[4:7], a[212:215], v[12:15]\n"
    "buffer_load_dwordx4 v149, s[64:67], s74 offen offset:0 lds\n"
    "ds_read_b64_tr_b16 a[68:69], v128 offset:57344\n"
    "ds_read_b64_tr_b16 a[70:71], v128 offset:61440\n"
    "v_mfma_f32_16x16x32_bf16 v[16:19], a[8:11], a[208:211], v[16:19]\n"
    "ds_read_b64_tr_b16 a[72:73], v129 offset:49152\n"
    "ds_read_b64_tr_b16 a[74:75], v129 offset:53248\n"
    "v_mfma_f32_16x16x32_bf16 v[20:23], a[8:11], a[212:215], v[20:23]\n"
    "ds_read_b64_tr_b16 a[76:77], v129 offset:57344\n"
    "ds_read_b64_tr_b16 a[78:79], v129 offset:61440\n"
    "v_mfma_f32_16x16x32_bf16 v[24:27], a[12:15], a[208:211], v[24:27]\n"
    "ds_read_b64_tr_b16 a[80:81], v130 offset:49152\n"
    "ds_read_b64_tr_b16 a[82:83], v130 offset:53248\n"
    "v_mfma_f32_16x16x32_bf16 v[28:31], a[12:15], a[212:215], v[28:31]\n"
    "buffer_load_dwordx4 v150, s[64:67], s74 offen offset:1024 lds\n"
    "ds_read_b64_tr_b16 a[84:85], v130 offset:57344\n"
    "v_mfma_f32_16x16x32_bf16 v[32:35], a[16:19], a[208:211], v[32:35]\n"
    "ds_read_b64_tr_b16 a[86:87], v130 offset:61440\n"
    "ds_read_b64_tr_b16 a[88:89], v131 offset:49152\n"
    "v_mfma_f32_16x16x32_bf16 v[36:39], a[16:19], a[212:215], v[36:39]\n"
    "ds_read_b64_tr_b16 a[90:91], v131 offset:53248\n"
    "ds_read_b64_tr_b16 a[92:93], v131 offset:57344\n"
    "v_mfma_f32_16x16x32_bf16 v[40:43], a[20:23], a[208:211], v[40:43]\n"
    "buffer_load_dwordx4 v151, s[64:67], s74 offen offset:2048 lds\n"
    "ds_read_b64_tr_b16 a[94:95], v131 offset:61440\n"
    "v_mfma_f32_16x16x32_bf16 v[44:47], a[20:23], a[212:215], v[44:47]\n"
    "ds_read_b64_tr_b16 a[96:97], v132 offset:49152\n"
    "ds_read_b64_tr_b16 a[98:99], v132 offset:53248\n"
    "v_mfma_f32_16x16x32_bf16 v[48:51], a[24:27], a[208:211], v[48:51]\n"
    "ds_read_b64_tr_b16 a[100:101], v132 offset:57344\n"
    "ds_read_b64_tr_b16 a[102:103], v132 offset:61440\n"
    "v_mfma_f32_16x16x32_bf16 v[52:55], a[24:27], a[212:215], v[52:55]\n"
    "ds_read_b64_tr_b16 a[104:105], v133 offset:49152\n"
    "ds_read_b64_tr_b16 a[106:107], v133 offset:53248\n"
    "v_mfma_f32_16x16x32_bf16 v[56:59], a[28:31], a[208:211], v[56:59]\n"
    "buffer_load_dwordx4 v152, s[64:67], s74 offen offset:3072 lds\n"
    "ds_read_b64_tr_b16 a[108:109], v133 offset:57344\n"
    "v_mfma_f32_16x16x32_bf16 v[60:63], a[28:31], a[212:215], v[60:63]\n"
    "ds_read_b64_tr_b16 a[110:111], v133 offset:61440\n"
    "ds_read_b64_tr_b16 a[112:113], v134 offset:49152\n"
    "v_mfma_f32_16x16x32_bf16 v[64:67], a[32:35], a[208:211], v[64:67]\n"
    "ds_read_b64_tr_b16 a[114:115], v134 offset:53248\n"
    "ds_read_b64_tr_b16 a[116:117], v134 offset:57344\n"
    "v_mfma_f32_16x16x32_bf16 v[68:71], a[32:35], a[212:215], v[68:71]\n"
    "ds_read_b64_tr_b16 a[118:119], v134 offset:61440\n"
    "s_add_u32 s74, s72, 28672\n"
    "s_add_u32 m0, s73, 114688\n"
    "v_mfma_f32_16x16x32_bf16 v[72:75], a[36:39], a[208:211], v[72:75]\n"
    "buffer_load_dwordx4 v149, s[64:67], s74 offen offset:0 lds\n"
    "ds_read_b64_tr_b16 a[120:121], v135 offset:49152\n"
    "v_mfma_f32_16x16x32_bf16 v[76:79], a[36:39], a[212:215], v[76:79]\n"
    "ds_read_b64_tr_b16 a[122:123], v135 offset:53248\n"
    "ds_read_b64_tr_b16 a[124:125], v135 offset:57344\n"
    "v_mfma_f32_16x16x32_bf16 v[80:83], a[40:43], a[208:211], v[80:83]\n"
    "ds_read_b64_tr_b16 a[126:127], v135 offset:61440\n"
    "v_mfma_f32_16x16x32_bf16 v[84:87], a[40:43], a[212:215], v[84:87]\n"
    "buffer_load_dwordx4 v150, s[64:67], s74 offen offset:1024 lds\n"
    "v_mfma_f32_16x16x32_bf16 v[88:91], a[44:47], a[208:211], v[88:91]\n"
    "v_mfma_f32_16x16x32_bf16 v[92:95], a[44:47], a[212:215], v[92:95]\n"
    "v_mfma_f32_16x16x32_bf16 v[96:99], a[48:51], a[208:211], v[96:99]\n"
    "v_mfma_f32_16x16x32_bf16 v[100:103], a[48:51], a[212:215], v[100:103]\n"
    "buffer_load_dwordx4 v151, s[64:67], s74 offen offset:2048 lds\n"
    "v_mfma_f32_16x16x32_bf16 v[104:107], a[52:55], a[208:211], v[104:107]\n"
    "v_mfma_f32_16x16x32_bf16 v[108:111], a[52:55], a[212:215], v[108:111]\n"
    "v_mfma_f32_16x16x32_bf16 v[112:115], a[56:59], a[208:211], v[112:115]\n"
    "buffer_load_dwordx4 v152, s[64:67], s74 offen offset:3072 lds\n"
    "v_mfma_f32_16x16x32_bf16 v[116:119], a[56:59], a[212:215], v[116:119]\n"
    "v_mfma_f32_16x16x32_bf16 v[120:123], a[60:63], a[208:211], v[120:123]\n"
    "v_mfma_f32_16x16x32_bf16 v[124:127], a[60:63], a[212:215], v[124:127]\n"
    "s_waitcnt lgkmcnt(15)\n"
    "v_mfma_f32_16x16x32_bf16 v[0:3], a[64:67], a[216:219], v[0:3]\n"
    "v_mfma_f32_16x16x32_bf16 v[4:7], a[64:67], a[220:223], v[4:7]\n"
    "s_waitcnt vmcnt(14) lgkmcnt(0)\n"
    "s_barrier\n"
    "v_mfma_f32_16x16x32_bf16 v[8:11], a[68:71], a[216:219], v[8:11]\n"
    "ds_read_b64_tr_b16 a[0:1], v136 offset:0\n"
    "ds_read_b64_tr_b16 a[2:3], v136 offset:4096\n"
    "s_add_u32 s74, s72, 57344\n"
    "s_add_u32 m0, s73, 131072\n"
    "v_mfma_f32_16x16x32_bf16 v[12:15], a[68:71], a[220:223], v[12:15]\n"
    "buffer_load_dwordx4 v149, s[64:67], s74 offen offset:0 lds\n"
    "ds_read_b64_tr_b16 a[4:5], v136 offset:8192\n"
    "ds_read_b64_tr_b16 a[6:7], v136 offset:12288\n"
    "v_mfma_f32_16x16x32_bf16 v[16:19], a[72:75], a[216:219], v[16:19]\n"
    "ds_read_b64_tr_b16 a[8:9], v137 offset:0\n"
    "ds_read_b64_tr_b16 a[10:11], v137 offset:4096\n"
    "v_mfma_f32_16x16x32_bf16 v[20:23], a[72:75], a[220:223], v[20:23]\n"
    "ds_read_b64_tr_b16 a[12:13], v137 offset:8192\n"
    "ds_read_b64_tr_b16 a[14:15], v137 offset:12288\n"
    "v_mfma_f32_16x16x32_bf16 v[24:27], a[76:79], a[216:219], v[24:27]\n"
    "ds_read_b64_tr_b16 a[16:17], v138 offset:0\n"
    "ds_read_b64_tr_b16 a[18:19], v138 offset:4096\n"
    "v_mfma_f32_16x16x32_bf16 v[28:31], a[76:79], a[220:223], v[28:31]\n"
    "buffer_load_dwordx4 v150, s[64:67], s74 offen offset:1024 lds\n"
    "ds_read_b64_tr_b16 a[20:21], v138 offset:8192\n"
    "v_mfma_f32_16x16x32_bf16 v[32:35], a[80:83], a[216:219], v[32:35]\n"
    "ds_read_b64_tr_b16 a[22:23], v138 offset:12288\n"
    "ds_read_b64_tr_b16 a[24:25], v139 offset:0\n"
    "v_mfma_f32_16x16x32_bf16 v[36:39], a[80:83], a[220:223], v[36:39]\n"
    "ds_read_b64_tr_b16 a[26:27], v139 offset:4096\n"
    "ds_read_b64_tr_b16 a[28:29], v139 offset:8192\n"
    "v_mfma_f32_16x16x32_bf16 v[40:43], a[84:87], a[216:219], v[40:43]\n"
    "buffer_load_dwordx4 v151, s[64:67], s74 offen offset:2048 lds\n"
    "ds_read_b64_tr_b16 a[30:31], v139 offset:12288\n"
    "v_mfma_f32_16x16x32_bf16 v[44:47], a[84:87], a[220:223], v[44:47]\n"
    "ds_read_b64_tr_b16 a[32:33], v140 offset:0\n"
    "ds_read_b64_tr_b16 a[34:35], v140 offset:4096\n"
    "v_mfma_f32_16x16x32_bf16 v[48:51], a[88:91], a[216:219], v[48:51]\n"
    "ds_read_b64_tr_b16 a[36:37], v140 offset:8192\n"
    "ds_read_b64_tr_b16 a[38:39], v140 offset:12288\n"
    "v_mfma_f32_16x16x32_bf16 v[52:55], a[88:91], a[220:223], v[52:55]\n"
    "ds_read_b64_tr_b16 a[40:41], v141 offset:0\n"
    "ds_read_b64_tr_b16 a[42:43], v141 offset:4096\n"
    "v_mfma_f32_16x16x32_bf16 v[56:59], a[92:95], a[216:219], v[56:59]\n"
    "buffer_load_dwordx4 v152, s[64:67], s74 offen offset:3072 lds\n"
    "ds_read_b64_tr_b16 a[44:45], v141 offset:8192\n"
    "v_mfma_f32_16x16x32_bf16 v[60:63], a[92:95], a[220:223], v[60:63]\n"
    "ds_read_b64_tr_b16 a[46:47], v141 offset:12288\n"
    "ds_read_b64_tr_b16 a[48:49], v142 offset:0\n"
    "v_mfma_f32_16x16x32_bf16 v[64:67], a[96:99], a[216:219], v[64:67]\n"
    "ds_read_b64_tr_b16 a[50:51], v142 offset:4096\n"
    "ds_read_b64_tr_b16 a[52:53], v142 offset:8192\n"
    "v_mfma_f32_16x16x32_bf16 v[68:71], a[96:99], a[220:223], v[68:71]\n"
    "ds_read_b64_tr_b16 a[54:55], v142 offset:12288\n"
    "s_add_u32 s74, s72, 86016\n"
    "s_add_u32 m0, s73, 147456\n"
    "v_mfma_f32_16x16x32_bf16 v[72:75], a[100:103], a[216:219], v[72:75]\n"
    "buffer_load_dwordx4 v149, s[64:67], s74 offen offset:0 lds\n"
    "ds_read_b64_tr_b16 a[56:57], v143 offset:0\n"
    "v_mfma_f32_16x16x32_bf16 v[76:79], a[100:103], a[220:223], v[76:79]\n"
    "ds_read_b64_tr_b16 a[58:59], v143 offset:4096\n"
    "ds_read_b64_tr_b16 a[60:61], v143 offset:8192\n"
    "v_mfma_f32_16x16x32_bf16 v[80:83], a[104:107], a[216:219], v[80:83]\n"
    "ds_read_b64_tr_b16 a[62:63], v143 offset:12288\n"
    "v_mfma_f32_16x16x32_bf16 v[84:87], a[104:107], a[220:223], v[84:87]\n"
    "buffer_load_dwordx4 v150, s[64:67], s74 offen offset:1024 lds\n"
    "v_mfma_f32_16x16x32_bf16 v[88:91], a[108:111], a[216:219], v[88:91]\n"
    "v_mfma_f32_16x16x32_bf16 v[92:95], a[108:111], a[220:223], v[92:95]\n"
    "v_mfma_f32_16x16x32_bf16 v[96:99], a[112:115], a[216:219], v[96:99]\n"
    "v_mfma_f32_16x16x32_bf16 v[100:103], a[112:115], a[220:223], v[100:103]\n"
    "buffer_load_dwordx4 v151, s[64:67], s74 offen offset:2048 lds\n"
    "v_mfma_f32_16x16x32_bf16 v[104:107], a[116:119], a[216:219], v[104:107]\n"
    "v_mfma_f32_16x16x32_bf16 v[108:111], a[116:119], a[220:223], v[108:111]\n"
    "v_mfma_f32_16x16x32_bf16 v[112:115], a[120:123], a[216:219], v[112:115]\n"
    "buffer_load_dwordx4 v152, s[64:67], s74 offen offset:3072 lds\n"
    "v_mfma_f32_16x16x32_bf16 v[116:119], a[120:123], a[220:223], v[116:119]\n"
    "v_mfma_f32_16x16x32_bf16 v[120:123], a[124:127], a[216:219], v[120:123]\n"
    "v_mfma_f32_16x16x32_bf16 v[124:127], a[124:127], a[220:223], v[124:127]\n"
    "s_waitcnt lgkmcnt(15)\n"
    "s_nop 0\n"
    "v_mfma_f32_16x16x32_bf16 v[0:3], a[0:3], a[224:227], v[0:3]\n"
    "v_mfma_f32_16x16x32_bf16 v[4:7], a[0:3], a[228:231], v[4:7]\n"
    "s_waitcnt vmcnt(16) lgkmcnt(0)\n"
    "s_barrier\n"
    "v_mfma_f32_16x16x32_bf16 v[8:11], a[4:7], a[224:227], v[8:11]\n"
    "ds_read_b64_tr_b16 a[64:65], v136 offset:16384\n"
    "ds_read_b64_tr_b16 a[66:67], v136 offset:20480\n"
    "s_add_u32 s74, s72, 114688\n"
    "s_add_u32 m0, s73, 0\n"
    "v_mfma_f32_16x16x32_bf16 v[12:15], a[4:7], a[228:231], v[12:15]\n"
    "buffer_load_dwordx4 v149, s[64:67], s74 offen offset:0 lds\n"
    "ds_read_b64_tr_b16 a[68:69], v136 offset:24576\n"
    "ds_read_b64_tr_b16 a[70:71], v136 offset:28672\n"
    "v_mfma_f32_16x16x32_bf16 v[16:19], a[8:11], a[224:227], v[16:19]\n"
    "ds_read_b64_tr_b16 a[72:73], v137 offset:16384\n"
    "ds_read_b64_tr_b16 a[74:75], v137 offset:20480\n"
    "v_mfma_f32_16x16x32_bf16 v[20:23], a[8:11], a[228:231], v[20:23]\n"
    "ds_read_b64_tr_b16 a[76:77], v137 offset:24576\n"
    "ds_read_b64_tr_b16 a[78:79], v137 offset:28672\n"
    "v_mfma_f32_16x16x32_bf16 v[24:27], a[12:15], a[224:227], v[24:27]\n"
    "ds_read_b64_tr_b16 a[80:81], v138 offset:16384\n"
    "ds_read_b64_tr_b16 a[82:83], v138 offset:20480\n"
    "v_mfma_f32_16x16x32_bf16 v[28:31], a[12:15], a[228:231], v[28:31]\n"
    "buffer_load_dwordx4 v150, s[64:67], s74 offen offset:1024 lds\n"
    "ds_read_b64_tr_b16 a[84:85], v138 offset:24576\n"
    "v_mfma_f32_16x16x32_bf16 v[32:35], a[16:19], a[224:227], v[32:35]\n"
    "ds_read_b64_tr_b16 a[86:87], v138 offset:28672\n"
    "ds_read_b64_tr_b16 a[88:89], v139 offset:16384\n"
    "v_mfma_f32_16x16x32_bf16 v[36:39], a[16:19], a[228:231], v[36:39]\n"
    "ds_read_b64_tr_b16 a[90:91], v139 offset:20480\n"
    "ds_read_b64_tr_b16 a[92:93], v139 offset:24576\n"
    "v_mfma_f32_16x16x32_bf16 v[40:43], a[20:23], a[224:227], v[40:43]\n"
    "buffer_load_dwordx4 v151, s[64:67], s74 offen offset:2048 lds\n"
    "ds_read_b64_tr_b16 a[94:95], v139 offset:28672\n"
    "v_mfma_f32_16x16x32_bf16 v[44:47], a[20:23], a[228:231], v[44:47]\n"
    "ds_read_b64_tr_b16 a[96:97], v140 offset:16384\n"
    "ds_read_b64_tr_b16 a[98:99], v140 offset:20480\n"
    "v_mfma_f32_16x16x32_bf16 v[48:51], a[24:27], a[224:227], v[48:51]\n"
    "ds_read_b64_tr_b16 a[100:101], v140 offset:24576\n"
    "ds_read_b64_tr_b16 a[102:103], v140 offset:28672\n"
    "v_mfma_f32_16x16x32_bf16 v[52:55], a[24:27], a[228:231], v[52:55]\n"
    "ds_read_b64_tr_b16 a[104:105], v141 offset:16384\n"
    "ds_read_b64_tr_b16 a[106:107], v141 offset:20480\n"
    "v_mfma_f32_16x16x32_bf16 v[56:59], a[28:31], a[224:227], v[56:59]\n"
    "buffer_load_dwordx4 v152, s[64:67], s74 offen offset:3072 lds\n"
    "ds_read_b64_tr_b16 a[108:109], v141 offset:24576\n"
    "v_mfma_f32_16x16x32_bf16 v[60:63], a[28:31], a[228:231], v[60:63]\n"
    "ds_read_b64_tr_b16 a[110:111], v141 offset:28672\n"
    "ds_read_b64_tr_b16 a[112:113], v142 offset:16384\n"
    "v_mfma_f32_16x16x32_bf16 v[64:67], a[32:35], a[224:227], v[64:67]\n"
    "ds_read_b64_tr_b16 a[114:115], v142 offset:20480\n"
    "ds_read_b64_tr_b16 a[116:117], v142 offset:24576\n"
    "v_mfma_f32_16x16x32_bf16 v[68:71], a[32:35], a[228:231], v[68:71]\n"
    "ds_read_b64_tr_b16 a[118:119], v142 offset:28672\n"
    "s_add_u32 s74, s72, 143360\n"
    "s_add_u32 m0, s73, 16384\n"
    "v_mfma_f32_16x16x32_bf16 v[72:75], a[36:39], a[224:227], v[72:75]\n"
    "buffer_load_dwordx4 v149, s[64:67], s74 offen offset:0 lds\n"
    "ds_read_b64_tr_b16 a[120:121], v143 offset:16384\n"
    "v_mfma_f32_16x16x32_bf16 v[76:79], a[36:39], a[228:231], v[76:79]\n"
    "ds_read_b64_tr_b16 a[122:123], v143 offset:20480\n"
    "ds_read_b64_tr_b16 a[124:125], v143 offset:24576\n"
    "v_mfma_f32_16x16x32_bf16 v[80:83], a[40:43], a[224:227], v[80:83]\n"
    "ds_read_b64_tr_b16 a[126:127], v143 offset:28672\n"
    "v_mfma_f32_16x16x32_bf16 v[84:87], a[40:43], a[228:231], v[84:87]\n"
    "buffer_load_dwordx4 v150, s[64:67], s74 offen offset:1024 lds\n"
    "v_mfma_f32_16x16x32_bf16 v[88:91], a[44:47], a[224:227], v[88:91]\n"
    "v_mfma_f32_16x16x32_bf16 v[92:95], a[44:47], a[228:231], v[92:95]\n"
    "v_mfma_f32_16x16x32_bf16 v[96:99], a[48:51], a[224:227], v[96:99]\n"
    "v_mfma_f32_16x16x32_bf16 v[100:103], a[48:51], a[228:231], v[100:103]\n"
    "buffer_load_dwordx4 v151, s[64:67], s74 offen offset:2048 lds\n"
    "v_mfma_f32_16x16x32_bf16 v[104:107], a[52:55], a[224:227], v[104:107]\n"
    "v_mfma_f32_16x16x32_bf16 v[108:111], a[52:55], a[228:231], v[108:111]\n"
    "v_mfma_f32_16x16x32_bf16 v[112:115], a[56:59], a[224:227], v[112:115]\n"
    "buffer_load_dwordx4 v152, s[64:67], s74 offen offset:3072 lds\n"
    "v_mfma_f32_16x16x32_bf16 v[116:119], a[56:59], a[228:231], v[116:119]\n"
    "v_mfma_f32_16x16x32_bf16 v[120:123], a[60:63], a[224:227], v[120:123]\n"
    "v_mfma_f32_16x16x32_bf16 v[124:127], a[60:63], a[228:231], v[124:127]\n"
    "s_waitcnt lgkmcnt(15)\n"
    "v_mfma_f32_16x16x32_bf16 v[0:3], a[64:67], a[232:235], v[0:3]\n"
    "v_mfma_f32_16x16x32_bf16 v[4:7], a[64:67], a[236:239], v[4:7]\n"
    "s_waitcnt vmcnt(8) lgkmcnt(0)\n"
    "s_barrier\n"
    "v_mfma_f32_16x16x32_bf16 v[8:11], a[68:71], a[232:235], v[8:11]\n"
    "ds_read_b64_tr_b16 v[192:193], v160 offset:0\n"
    "ds_read_b64_tr_b16 v[194:195], v160 offset:2048\n"
    "s_add_u32 s74, s72, 286720\n"
    "s_add_u32 m0, s73, 32768\n"
    "v_mfma_f32_16x16x32_bf16 v[12:15], a[68:71], a[236:239], v[12:15]\n"
    "buffer_load_dwordx4 v149, s[64:67], s74 offen offset:0 lds\n"
    "ds_read_b64_tr_b16 v[196:197], v161 offset:0\n"
    "ds_read_b64_tr_b16 v[198:199], v161 offset:2048\n"
    "v_mfma_f32_16x16x32_bf16 v[16:19], a[72:75], a[232:235], v[16:19]\n"
    "ds_read_b64_tr_b16 v[200:201], v162 offset:0\n"
    "ds_read_b64_tr_b16 v[202:203], v162 offset:2048\n"
    "v_mov_b32_e64 v128, v160\n"
    "v_mfma_f32_16x16x32_bf16 v[20:23], a[72:75], a[236:239], v[20:23]\n"
    "ds_read_b64_tr_b16 v[204:205], v163 offset:0\n"
    "ds_read_b64_tr_b16 v[206:207], v163 offset:2048\n"
    "v_subrev_u32 v132, 98304, v160\n"
    "v_mfma_f32_16x16x32_bf16 v[24:27], a[76:79], a[232:235], v[24:27]\n"
    "ds_read_b64_tr_b16 v[208:209], v160 offset:16384\n"
    "ds_read_b64_tr_b16 v[210:211], v160 offset:18432\n"
    "v_subrev_u32 v136, 32768, v160\n"
    "v_mfma_f32_16x16x32_bf16 v[28:31], a[76:79], a[236:239], v[28:31]\n"
    "buffer_load_dwordx4 v150, s[64:67], s74 offen offset:1024 lds\n"
    "ds_read_b64_tr_b16 v[212:213], v161 offset:16384\n"
    "v_mov_b32_e64 v129, v161\n"
    "v_mfma_f32_16x16x32_bf16 v[32:35], a[80:83], a[232:235], v[32:35]\n"
    "ds_read_b64_tr_b16 v[214:215], v161 offset:18432\n"
    "ds_read_b64_tr_b16 v[216:217], v162 offset:16384\n"
    "v_subrev_u32 v133, 98304, v161\n"
    "v_mfma_f32_16x16x32_bf16 v[36:39], a[80:83], a[236:239], v[36:39]\n"
    "ds_read_b64_tr_b16 v[218:219], v162 offset:18432\n"
    "ds_read_b64_tr_b16 v[220:221], v163 offset:16384\n"
    "v_subrev_u32 v137, 32768, v161\n"
    "v_mfma_f32_16x16x32_bf16 v[40:43], a[84:87], a[232:235], v[40:43]\n"
    "buffer_load_dwordx4 v151, s[64:67], s74 offen offset:2048 lds\n"
    "ds_read_b64_tr_b16 v[222:223], v163 offset:18432\n"
    "v_mov_b32_e64 v130, v162\n"
    "v_mfma_f32_16x16x32_bf16 v[44:47], a[84:87], a[236:239], v[44:47]\n"
    "ds_read_b64_tr_b16 v[224:225], v160 offset:32768\n"
    "ds_read_b64_tr_b16 v[226:227], v160 offset:34816\n"
    "v_subrev_u32 v134, 98304, v162\n"
    "v_mfma_f32_16x16x32_bf16 v[48:51], a[88:91], a[232:235], v[48:51]\n"
    "ds_read_b64_tr_b16 v[228:229], v161 offset:32768\n"
    "ds_read_b64_tr_b16 v[230:231], v161 offset:34816\n"
    "v_subrev_u32 v138, 32768, v162\n"
    "v_mfma_f32_16x16x32_bf16 v[52:55], a[88:91], a[236:239], v[52:55]\n"
    "ds_read_b64_tr_b16 v[232:233], v162 offset:32768\n"
    "ds_read_b64_tr_b16 v[234:235], v162 offset:34816\n"
    "v_mov_b32_e64 v131, v163\n"
    "v_mfma_f32_16x16x32_bf16 v[56:59], a[92:95], a[232:235], v[56:59]\n"
    "buffer_load_dwordx4 v152, s[64:67], s74 offen offset:3072 lds\n"
    "ds_read_b64_tr_b16 v[236:237], v163 offset:32768\n"
    "v_subrev_u32 v135, 98304, v163\n"
    "v_mfma_f32_16x16x32_bf16 v[60:63], a[92:95], a[236:239], v[60:63]\n"
    "ds_read_b64_tr_b16 v[238:239], v163 offset:34816\n"
    "ds_read_b64_tr_b16 v[240:241], v160 offset:49152\n"
    "v_subrev_u32 v139, 32768, v163\n"
    "v_mfma_f32_16x16x32_bf16 v[64:67], a[96:99], a[232:235], v[64:67]\n"
    "ds_read_b64_tr_b16 v[242:243], v160 offset:51200\n"
    "ds_read_b64_tr_b16 v[244:245], v161 offset:49152\n"
    "v_mfma_f32_16x16x32_bf16 v[68:71], a[96:99], a[236:239], v[68:71]\n"
    "ds_read_b64_tr_b16 v[246:247], v161 offset:51200\n"
    "s_add_u32 s74, s72, 315392\n"
    "s_add_u32 m0, s73, 49152\n"
    "v_mfma_f32_16x16x32_bf16 v[72:75], a[100:103], a[232:235], v[72:75]\n"
    "buffer_load_dwordx4 v149, s[64:67], s74 offen offset:0 lds\n"
    "ds_read_b64_tr_b16 v[248:249], v162 offset:49152\n"
    "v_mfma_f32_16x16x32_bf16 v[76:79], a[100:103], a[236:239], v[76:79]\n"
    "ds_read_b64_tr_b16 v[250:251], v162 offset:51200\n"
    "ds_read_b64_tr_b16 v[252:253], v163 offset:49152\n"
    "v_mfma_f32_16x16x32_bf16 v[80:83], a[104:107], a[232:235], v[80:83]\n"
    "ds_read_b64_tr_b16 v[254:255], v163 offset:51200\n"
    "v_mfma_f32_16x16x32_bf16 v[84:87], a[104:107], a[236:239], v[84:87]\n"
    "buffer_load_dwordx4 v150, s[64:67], s74 offen offset:1024 lds\n"
    "v_mfma_f32_16x16x32_bf16 v[88:91], a[108:111], a[232:235], v[88:91]\n"
    "v_mfma_f32_16x16x32_bf16 v[92:95], a[108:111], a[236:239], v[92:95]\n"
    "v_mfma_f32_16x16x32_bf16 v[96:99], a[112:115], a[232:235], v[96:99]\n"
    "v_mfma_f32_16x16x32_bf16 v[100:103], a[112:115], a[236:239], v[100:103]\n"
    "buffer_load_dwordx4 v151, s[64:67], s74 offen offset:2048 lds\n"
    "v_mfma_f32_16x16x32_bf16 v[104:107], a[116:119], a[232:235], v[104:107]\n"
    "v_mfma_f32_16x16x32_bf16 v[108:111], a[116:119], a[236:239], v[108:111]\n"
    "v_mfma_f32_16x16x32_bf16 v[112:115], a[120:123], a[232:235], v[112:115]\n"
    "buffer_load_dwordx4 v152, s[64:67], s74 offen offset:3072 lds\n"
    "v_mfma_f32_16x16x32_bf16 v[116:119], a[120:123], a[236:239], v[116:119]\n"
    "v_mfma_f32_16x16x32_bf16 v[120:123], a[124:127], a[232:235], v[120:123]\n"
    "v_mfma_f32_16x16x32_bf16 v[124:127], a[124:127], a[236:239], v[124:127]\n"
    "s_waitcnt vmcnt(12) lgkmcnt(0)\n"
    "ds_read_b64_tr_b16 a[240:241], v132 offset:0\n"
    "ds_read_b64_tr_b16 a[242:243], v132 offset:2048\n"
    "ds_read_b64_tr_b16 a[244:245], v133 offset:0\n"
    "ds_read_b64_tr_b16 a[246:247], v133 offset:2048\n"
    "ds_read_b64_tr_b16 a[248:249], v134 offset:0\n"
    "ds_read_b64_tr_b16 a[250:251], v134 offset:2048\n"
    "ds_read_b64_tr_b16 a[252:253], v135 offset:0\n"
    "ds_read_b64_tr_b16 a[254:255], v135 offset:2048\n"
    "s_add_u32 s74, s72, 344064\n"
    "s_add_u32 m0, s73, 65536\n"
    "s_nop 0\n"
    "buffer_load_dwordx4 v149, s[64:67], s74 offen offset:0 lds\n"
    "buffer_load_dwordx4 v150, s[64:67], s74 offen offset:1024 lds\n"
    "buffer_load_dwordx4 v151, s[64:67], s74 offen offset:2048 lds\n"
    "buffer_load_dwordx4 v152, s[64:67], s74 offen offset:3072 lds\n"
    "s_add_u32 s74, s72, 372736\n"
    "s_add_u32 m0, s73, 81920\n"
    "s_nop 0\n"
    "buffer_load_dwordx4 v149, s[64:67], s74 offen offset:0 lds\n"
    "buffer_load_dwordx4 v150, s[64:67], s74 offen offset:1024 lds\n"
    "buffer_load_dwordx4 v151, s[64:67], s74 offen offset:2048 lds\n"
    "buffer_load_dwordx4 v152, s[64:67], s74 offen offset:3072 lds\n"
    "s_add_u32 s74, s72, 172032\n"
    "s_add_u32 m0, s73, 98304\n"
    "s_nop 0\n"
    "buffer_load_dwordx4 v149, s[64:67], s74 offen offset:0 lds\n"
    "buffer_load_dwordx4 v150, s[64:67], s74 offen offset:1024 lds\n"
    "buffer_load_dwordx4 v151, s[64:67], s74 offen offset:2048 lds\n"
    "buffer_load_dwordx4 v152, s[64:67], s74 offen offset:3072 lds\n"
    "s_add_u32 s74, s72, 200704\n"
    "s_add_u32 m0, s73, 114688\n"
    "s_nop 0\n"
    "buffer_load_dwordx4 v149, s[64:67], s74 offen offset:0 lds\n"
    "buffer_load_dwordx4 v150, s[64:67], s74 offen offset:1024 lds\n"
    "buffer_load_dwordx4 v151, s[64:67], s74 offen offset:2048 lds\n"
    "buffer_load_dwordx4 v152, s[64:67], s74 offen offset:3072 lds\n"
    "v_mov_b32 v154, s62\n"
    "v_mov_b32 v155, s62\n"
    "v_mov_b32 v156, 1.0\n"
    "v_mov_b32 v157, 1.0\n"
    "v_pk_mul_f32 v[160:161], v[0:1], v[154:155]\n"
    "v_pk_mul_f32 v[162:163], v[2:3], v[154:155]\n"
    "v_exp_f32 v160, v160\n"
    "v_exp_f32 v161, v161\n"
    "v_exp_f32 v162, v162\n"
    "v_exp_f32 v163, v163\n"
    "v_pk_mul_f32 v[0:1], v[0:1], v[8:9]\n"
    "v_pk_mul_f32 v[2:3], v[2:3], v[10:11]\n"
    "v_pk_add_f32 v[160:161], v[160:161], v[156:157]\n"
    "v_pk_add_f32 v[162:163], v[162:163], v[156:157]\n"
    "v_rcp_f32 v160, v160\n"
    "v_rcp_f32 v161, v161\n"
    "v_rcp_f32 v162, v162\n"
    "v_rcp_f32 v163, v163\n"
    "s_nop 0\n"
    "v_pk_mul_f32 v[0:1], v[0:1], v[160:161]\n"
    "v_pk_mul_f32 v[2:3], v[2:3], v[162:163]\n"
    "v_cvt_pk_bf16_f32 v176, v0, v1\n"
    "v_cvt_pk_bf16_f32 v177, v2, v3\n"
    "v_pk_mul_f32 v[164:165], v[16:17], v[154:155]\n"
    "v_pk_mul_f32 v[166:167], v[18:19], v[154:155]\n"
    "v_exp_f32 v164, v164\n"
    "v_exp_f32 v165, v165\n"
    "v_exp_f32 v166, v166\n"
    "v_exp_f32 v167, v167\n"
    "v_pk_mul_f32 v[16:17], v[16:17], v[24:25]\n"
    "v_pk_mul_f32 v[18:19], v[18:19], v[26:27]\n"
    "v_pk_add_f32 v[164:165], v[164:165], v[156:157]\n"
    "v_pk_add_f32 v[166:167], v[166:167], v[156:157]\n"
    "v_rcp_f32 v164, v164\n"
    "v_rcp_f32 v165, v165\n"
    "v_rcp_f32 v166, v166\n"
    "v_rcp_f32 v167, v167\n"
    "s_nop 0\n"
    "v_pk_mul_f32 v[16:17], v[16:17], v[164:165]\n"
    "v_pk_mul_f32 v[18:19], v[18:19], v[166:167]\n"
    "v_cvt_pk_bf16_f32 v178, v16, v17\n"
    "v_cvt_pk_bf16_f32 v179, v18, v19\n"
    "buffer_store_dwordx4 v[176:179], v146, s[48:51], s60 offen offset:0 sc1\n"
    "v_pk_mul_f32 v[168:169], v[4:5], v[154:155]\n"
    "v_pk_mul_f32 v[170:171], v[6:7], v[154:155]\n"
    "v_exp_f32 v168, v168\n"
    "v_exp_f32 v169, v169\n"
    "v_exp_f32 v170, v170\n"
    "v_exp_f32 v171, v171\n"
    "v_pk_mul_f32 v[4:5], v[4:5], v[12:13]\n"
    "v_pk_mul_f32 v[6:7], v[6:7], v[14:15]\n"
    "v_pk_add_f32 v[168:169], v[168:169], v[156:157]\n"
    "v_pk_add_f32 v[170:171], v[170:171], v[156:157]\n"
    "v_rcp_f32 v168, v168\n"
    "v_rcp_f32 v169, v169\n"
    "v_rcp_f32 v170, v170\n"
    "v_rcp_f32 v171, v171\n"
    "s_nop 0\n"
    "v_pk_mul_f32 v[4:5], v[4:5], v[168:169]\n"
    "v_pk_mul_f32 v[6:7], v[6:7], v[170:171]\n"
    "v_cvt_pk_bf16_f32 v180, v4, v5\n"
    "v_cvt_pk_bf16_f32 v181, v6, v7\n"
    "v_pk_mul_f32 v[172:173], v[20:21], v[154:155]\n"
    "v_pk_mul_f32 v[174:175], v[22:23], v[154:155]\n"
    "v_exp_f32 v172, v172\n"
    "v_exp_f32 v173, v173\n"
    "v_exp_f32 v174, v174\n"
    "v_exp_f32 v175, v175\n"
    "v_pk_mul_f32 v[20:21], v[20:21], v[28:29]\n"
    "v_pk_mul_f32 v[22:23], v[22:23], v[30:31]\n"
    "v_pk_add_f32 v[172:173], v[172:173], v[156:157]\n"
    "v_pk_add_f32 v[174:175], v[174:175], v[156:157]\n"
    "v_rcp_f32 v172, v172\n"
    "v_rcp_f32 v173, v173\n"
    "v_rcp_f32 v174, v174\n"
    "v_rcp_f32 v175, v175\n"
    "s_nop 0\n"
    "v_pk_mul_f32 v[20:21], v[20:21], v[172:173]\n"
    "v_pk_mul_f32 v[22:23], v[22:23], v[174:175]\n"
    "v_cvt_pk_bf16_f32 v182, v20, v21\n"
    "v_cvt_pk_bf16_f32 v183, v22, v23\n"
    "v_add_u32 v147, 43008, v147\n"
    "buffer_store_dwordx4 v[180:183], v147, s[48:51], s60 offen offset:0 sc1\n"
    "v_pk_mul_f32 v[160:161], v[32:33], v[154:155]\n"
    "v_pk_mul_f32 v[162:163], v[34:35], v[154:155]\n"
    "v_exp_f32 v160, v160\n"
    "v_exp_f32 v161, v161\n"
    "v_exp_f32 v162, v162\n"
    "v_exp_f32 v163, v163\n"
    "v_pk_mul_f32 v[32:33], v[32:33], v[40:41]\n"
    "v_pk_mul_f32 v[34:35], v[34:35], v[42:43]\n"
    "v_pk_add_f32 v[160:161], v[160:161], v[156:157]\n"
    "v_pk_add_f32 v[162:163], v[162:163], v[156:157]\n"
    "v_rcp_f32 v160, v160\n"
    "v_rcp_f32 v161, v161\n"
    "v_rcp_f32 v162, v162\n"
    "v_rcp_f32 v163, v163\n"
    "s_nop 0\n"
    "v_pk_mul_f32 v[32:33], v[32:33], v[160:161]\n"
    "v_pk_mul_f32 v[34:35], v[34:35], v[162:163]\n"
    "v_cvt_pk_bf16_f32 v184, v32, v33\n"
    "v_cvt_pk_bf16_f32 v185, v34, v35\n"
    "v_pk_mul_f32 v[164:165], v[48:49], v[154:155]\n"
    "v_pk_mul_f32 v[166:167], v[50:51], v[154:155]\n"
    "v_exp_f32 v164, v164\n"
    "v_exp_f32 v165, v165\n"
    "v_exp_f32 v166, v166\n"
    "v_exp_f32 v167, v167\n"
    "v_pk_mul_f32 v[48:49], v[48:49], v[56:57]\n"
    "v_pk_mul_f32 v[50:51], v[50:51], v[58:59]\n"
    "v_pk_add_f32 v[164:165], v[164:165], v[156:157]\n"
    "v_pk_add_f32 v[166:167], v[166:167], v[156:157]\n"
    "v_rcp_f32 v164, v164\n"
    "v_rcp_f32 v165, v165\n"
    "v_rcp_f32 v166, v166\n"
    "v_rcp_f32 v167, v167\n"
    "s_nop 0\n"
    "v_pk_mul_f32 v[48:49], v[48:49], v[164:165]\n"
    "v_pk_mul_f32 v[50:51], v[50:51], v[166:167]\n"
    "v_cvt_pk_bf16_f32 v186, v48, v49\n"
    "v_cvt_pk_bf16_f32 v187, v50, v51\n"
    "buffer_store_dwordx4 v[184:187], v146, s[48:51], s60 offen offset:1024 sc1\n"
    "v_pk_mul_f32 v[168:169], v[36:37], v[154:155]\n"
    "v_pk_mul_f32 v[170:171], v[38:39], v[154:155]\n"
    "v_exp_f32 v168, v168\n"
    "v_exp_f32 v169, v169\n"
    "v_exp_f32 v170, v170\n"
    "v_exp_f32 v171, v171\n"
    "v_pk_mul_f32 v[36:37], v[36:37], v[44:45]\n"
    "v_pk_mul_f32 v[38:39], v[38:39], v[46:47]\n"
    "v_pk_add_f32 v[168:169], v[168:169], v[156:157]\n"
    "v_pk_add_f32 v[170:171], v[170:171], v[156:157]\n"
    "v_rcp_f32 v168, v168\n"
    "v_rcp_f32 v169, v169\n"
    "v_rcp_f32 v170, v170\n"
    "v_rcp_f32 v171, v171\n"
    "s_nop 0\n"
    "v_pk_mul_f32 v[36:37], v[36:37], v[168:169]\n"
    "v_pk_mul_f32 v[38:39], v[38:39], v[170:171]\n"
    "v_cvt_pk_bf16_f32 v188, v36, v37\n"
    "v_cvt_pk_bf16_f32 v189, v38, v39\n"
    "v_pk_mul_f32 v[172:173], v[52:53], v[154:155]\n"
    "v_pk_mul_f32 v[174:175], v[54:55], v[154:155]\n"
    "v_exp_f32 v172, v172\n"
    "v_exp_f32 v173, v173\n"
    "v_exp_f32 v174, v174\n"
    "v_exp_f32 v175, v175\n"
    "v_pk_mul_f32 v[52:53], v[52:53], v[60:61]\n"
    "v_pk_mul_f32 v[54:55], v[54:55], v[62:63]\n"
    "v_pk_add_f32 v[172:173], v[172:173], v[156:157]\n"
    "v_pk_add_f32 v[174:175], v[174:175], v[156:157]\n"
    "v_rcp_f32 v172, v172\n"
    "v_rcp_f32 v173, v173\n"
    "v_rcp_f32 v174, v174\n"
    "v_rcp_f32 v175, v175\n"
    "s_nop 0\n"
    "v_pk_mul_f32 v[52:53], v[52:53], v[172:173]\n"
    "v_pk_mul_f32 v[54:55], v[54:55], v[174:175]\n"
    "v_cvt_pk_bf16_f32 v190, v52, v53\n"
    "v_cvt_pk_bf16_f32 v191, v54, v55\n"
    "buffer_store_dwordx4 v[188:191], v147, s[48:51], s60 offen offset:1024 sc1\n"
    "v_pk_mul_f32 v[160:161], v[64:65], v[154:155]\n"
    "v_pk_mul_f32 v[162:163], v[66:67], v[154:155]\n"
    "v_exp_f32 v160, v160\n"
    "v_exp_f32 v161, v161\n"
    "v_exp_f32 v162, v162\n"
    "v_exp_f32 v163, v163\n"
    "v_pk_mul_f32 v[64:65], v[64:65], v[72:73]\n"
    "v_pk_mul_f32 v[66:67], v[66:67], v[74:75]\n"
    "v_pk_add_f32 v[160:161], v[160:161], v[156:157]\n"
    "v_pk_add_f32 v[162:163], v[162:163], v[156:157]\n"
    "v_rcp_f32 v160, v160\n"
    "v_rcp_f32 v161, v161\n"
    "v_rcp_f32 v162, v162\n"
    "v_rcp_f32 v163, v163\n"
    "s_nop 0\n"
    "v_pk_mul_f32 v[64:65], v[64:65], v[160:161]\n"
    "v_pk_mul_f32 v[66:67], v[66:67], v[162:163]\n"
    "v_cvt_pk_bf16_f32 v176, v64, v65\n"
    "v_cvt_pk_bf16_f32 v177, v66, v67\n"
    "v_pk_mul_f32 v[164:165], v[80:81], v[154:155]\n"
    "v_pk_mul_f32 v[166:167], v[82:83], v[154:155]\n"
    "v_exp_f32 v164, v164\n"
    "v_exp_f32 v165, v165\n"
    "v_exp_f32 v166, v166\n"
    "v_exp_f32 v167, v167\n"
    "v_pk_mul_f32 v[80:81], v[80:81], v[88:89]\n"
    "v_pk_mul_f32 v[82:83], v[82:83], v[90:91]\n"
    "v_pk_add_f32 v[164:165], v[164:165], v[156:157]\n"
    "v_pk_add_f32 v[166:167], v[166:167], v[156:157]\n"
    "v_rcp_f32 v164, v164\n"
    "v_rcp_f32 v165, v165\n"
    "v_rcp_f32 v166, v166\n"
    "v_rcp_f32 v167, v167\n"
    "s_nop 0\n"
    "v_pk_mul_f32 v[80:81], v[80:81], v[164:165]\n"
    "v_pk_mul_f32 v[82:83], v[82:83], v[166:167]\n"
    "v_cvt_pk_bf16_f32 v178, v80, v81\n"
    "v_cvt_pk_bf16_f32 v179, v82, v83\n"
    "buffer_store_dwordx4 v[176:179], v146, s[48:51], s60 offen offset:2048 sc1\n"
    "v_pk_mul_f32 v[168:169], v[68:69], v[154:155]\n"
    "v_pk_mul_f32 v[170:171], v[70:71], v[154:155]\n"
    "v_exp_f32 v168, v168\n"
    "v_exp_f32 v169, v169\n"
    "v_exp_f32 v170, v170\n"
    "v_exp_f32 v171, v171\n"
    "v_pk_mul_f32 v[68:69], v[68:69], v[76:77]\n"
    "v_pk_mul_f32 v[70:71], v[70:71], v[78:79]\n"
    "v_pk_add_f32 v[168:169], v[168:169], v[156:157]\n"
    "v_pk_add_f32 v[170:171], v[170:171], v[156:157]\n"
    "v_rcp_f32 v168, v168\n"
    "v_rcp_f32 v169, v169\n"
    "v_rcp_f32 v170, v170\n"
    "v_rcp_f32 v171, v171\n"
    "s_nop 0\n"
    "v_pk_mul_f32 v[68:69], v[68:69], v[168:169]\n"
    "v_pk_mul_f32 v[70:71], v[70:71], v[170:171]\n"
    "v_cvt_pk_bf16_f32 v180, v68, v69\n"
    "v_cvt_pk_bf16_f32 v181, v70, v71\n"
    "v_pk_mul_f32 v[172:173], v[84:85], v[154:155]\n"
    "v_pk_mul_f32 v[174:175], v[86:87], v[154:155]\n"
    "v_exp_f32 v172, v172\n"
    "v_exp_f32 v173, v173\n"
    "v_exp_f32 v174, v174\n"
    "v_exp_f32 v175, v175\n"
    "v_pk_mul_f32 v[84:85], v[84:85], v[92:93]\n"
    "v_pk_mul_f32 v[86:87], v[86:87], v[94:95]\n"
    "v_pk_add_f32 v[172:173], v[172:173], v[156:157]\n"
    "v_pk_add_f32 v[174:175], v[174:175], v[156:157]\n"
    "v_rcp_f32 v172, v172\n"
    "v_rcp_f32 v173, v173\n"
    "v_rcp_f32 v174, v174\n"
    "v_rcp_f32 v175, v175\n"
    "s_nop 0\n"
    "v_pk_mul_f32 v[84:85], v[84:85], v[172:173]\n"
    "v_pk_mul_f32 v[86:87], v[86:87], v[174:175]\n"
    "v_cvt_pk_bf16_f32 v182, v84, v85\n"
    "v_cvt_pk_bf16_f32 v183, v86, v87\n"
    "buffer_store_dwordx4 v[180:183], v147, s[48:51], s60 offen offset:2048 sc1\n"
    "v_pk_mul_f32 v[160:161], v[96:97], v[154:155]\n"
    "v_pk_mul_f32 v[162:163], v[98:99], v[154:155]\n"
    "v_exp_f32 v160, v160\n"
    "v_exp_f32 v161, v161\n"
    "v_exp_f32 v162, v162\n"
    "v_exp_f32 v163, v163\n"
    "v_pk_mul_f32 v[96:97], v[96:97], v[104:105]\n"
    "v_pk_mul_f32 v[98:99], v[98:99], v[106:107]\n"
    "v_pk_add_f32 v[160:161], v[160:161], v[156:157]\n"
    "v_pk_add_f32 v[162:163], v[162:163], v[156:157]\n"
    "v_rcp_f32 v160, v160\n"
    "v_rcp_f32 v161, v161\n"
    "v_rcp_f32 v162, v162\n"
    "v_rcp_f32 v163, v163\n"
    "s_nop 0\n"
    "v_pk_mul_f32 v[96:97], v[96:97], v[160:161]\n"
    "v_pk_mul_f32 v[98:99], v[98:99], v[162:163]\n"
    "v_cvt_pk_bf16_f32 v184, v96, v97\n"
    "v_cvt_pk_bf16_f32 v185, v98, v99\n"
    "v_pk_mul_f32 v[164:165], v[112:113], v[154:155]\n"
    "v_pk_mul_f32 v[166:167], v[114:115], v[154:155]\n"
    "v_exp_f32 v164, v164\n"
    "v_exp_f32 v165, v165\n"
    "v_exp_f32 v166, v166\n"
    "v_exp_f32 v167, v167\n"
    "v_pk_mul_f32 v[112:113], v[112:113], v[120:121]\n"
    "v_pk_mul_f32 v[114:115], v[114:115], v[122:123]\n"
    "v_pk_add_f32 v[164:165], v[164:165], v[156:157]\n"
    "v_pk_add_f32 v[166:167], v[166:167], v[156:157]\n"
    "v_rcp_f32 v164, v164\n"
    "v_rcp_f32 v165, v165\n"
    "v_rcp_f32 v166, v166\n"
    "v_rcp_f32 v167, v167\n"
    "s_nop 0\n"
    "v_pk_mul_f32 v[112:113], v[112:113], v[164:165]\n"
    "v_pk_mul_f32 v[114:115], v[114:115], v[166:167]\n"
    "v_cvt_pk_bf16_f32 v186, v112, v113\n"
    "v_cvt_pk_bf16_f32 v187, v114, v115\n"
    "buffer_store_dwordx4 v[184:187], v146, s[48:51], s60 offen offset:3072 sc1\n"
    "v_pk_mul_f32 v[168:169], v[100:101], v[154:155]\n"
    "v_pk_mul_f32 v[170:171], v[102:103], v[154:155]\n"
    "v_exp_f32 v168, v168\n"
    "v_exp_f32 v169, v169\n"
    "v_exp_f32 v170, v170\n"
    "v_exp_f32 v171, v171\n"
    "v_pk_mul_f32 v[100:101], v[100:101], v[108:109]\n"
    "v_pk_mul_f32 v[102:103], v[102:103], v[110:111]\n"
    "v_pk_add_f32 v[168:169], v[168:169], v[156:157]\n"
    "v_pk_add_f32 v[170:171], v[170:171], v[156:157]\n"
    "v_rcp_f32 v168, v168\n"
    "v_rcp_f32 v169, v169\n"
    "v_rcp_f32 v170, v170\n"
    "v_rcp_f32 v171, v171\n"
    "s_nop 0\n"
    "v_pk_mul_f32 v[100:101], v[100:101], v[168:169]\n"
    "v_pk_mul_f32 v[102:103], v[102:103], v[170:171]\n"
    "v_cvt_pk_bf16_f32 v188, v100, v101\n"
    "v_cvt_pk_bf16_f32 v189, v102, v103\n"
    "v_pk_mul_f32 v[172:173], v[116:117], v[154:155]\n"
    "v_pk_mul_f32 v[174:175], v[118:119], v[154:155]\n"
    "v_exp_f32 v172, v172\n"
    "v_exp_f32 v173, v173\n"
    "v_exp_f32 v174, v174\n"
    "v_exp_f32 v175, v175\n"
    "v_pk_mul_f32 v[116:117], v[116:117], v[124:125]\n"
    "v_pk_mul_f32 v[118:119], v[118:119], v[126:127]\n"
    "v_pk_add_f32 v[172:173], v[172:173], v[156:157]\n"
    "v_pk_add_f32 v[174:175], v[174:175], v[156:157]\n"
    "v_rcp_f32 v172, v172\n"
    "v_rcp_f32 v173, v173\n"
    "v_rcp_f32 v174, v174\n"
    "v_rcp_f32 v175, v175\n"
    "s_nop 0\n"
    "v_pk_mul_f32 v[116:117], v[116:117], v[172:173]\n"
    "v_pk_mul_f32 v[118:119], v[118:119], v[174:175]\n"
    "v_cvt_pk_bf16_f32 v190, v116, v117\n"
    "v_cvt_pk_bf16_f32 v191, v118, v119\n"
    "buffer_store_dwordx4 v[188:191], v147, s[48:51], s60 offen offset:3072 sc1\n"
    "s_lshl_b32 s81, s53, 11\n"
    "s_add_u32 s81, s81, 33030144\n"
    "s_lshl_b32 s59, s54, 4\n"
    "s_add_u32 s59, s59, s81\n"
    "v_mov_b32 v140, 1\n"
    "v_mov_b32 v127, 0\n"
    "v_min_u32 v141, 13, v148\n"
    "v_lshlrev_b32 v141, 4, v141\n"
    "s_lshl_b32 s58, s53, 1\n"
    "s_add_u32 s58, s58, s76\n"
    "s_mul_i32 s77, s58, 229376\n"
    "s_mul_i32 s78, s52, 14336\n"
    "s_add_u32 s77, s77, s78\n"
    "s_add_u32 s77, s77, 25165824\n"
    "s_lshl_b32 s58, s58, 2\n"
    "s_add_u32 s58, s58, s52\n"
    "s_mov_b32 s80, 0\n"
    "s_cmp_lg_u32 s52, 0\n"
    "s_waitcnt vmcnt(0) lgkmcnt(0)\n"
    "s_barrier\n"
    "s_cbranch_scc1 F_np\n"
    "s_mov_b64 exec, 1\n"
    "buffer_store_dword v140, v127, s[48:51], s59 offen sc1\n"
    "s_mov_b64 exec, -1\n"
    "F_np:\n"
    "s_add_u32 s74, s72, 229376\n"
    "s_add_u32 m0, s73, 131072\n"
    "s_nop 0\n"
    "buffer_load_dwordx4 v149, s[64:67], s74 offen offset:0 lds\n"
    "buffer_load_dwordx4 v150, s[64:67], s74 offen offset:1024 lds\n"
    "buffer_load_dwordx4 v151, s[64:67], s74 offen offset:2048 lds\n"
    "buffer_load_dwordx4 v152, s[64:67], s74 offen offset:3072 lds\n"
    "s_add_u32 s74, s72, 258048\n"
    "s_add_u32 m0, s73, 147456\n"
    "s_nop 0\n"
    "buffer_load_dwordx4 v149, s[64:67], s74 offen offset:0 lds\n"
    "buffer_load_dwordx4 v150, s[64:67], s74 offen offset:1024 lds\n"
    "buffer_load_dwordx4 v151, s[64:67], s74 offen offset:2048 lds\n"
    "buffer_load_dwordx4 v152, s[64:67], s74 offen offset:3072 lds\n"
    "v_lshlrev_b32 v120, 4, v148\n"
    "v_add_u32 v121, 57344, v120\n"
    "v_add_u32 v122, 114688, v120\n"
    "v_add_u32 v123, 172032, v120\n"
    "v_lshrrev_b32 v143, 4, v148\n"
    "s_mul_i32 s59, s58, 14\n"
    "s_lshl_b32 s78, s75, 1\n"
    "s_add_u32 s59, s59, s78\n"
    "s_lshl_b32 s59, s59, 10\n"
    "s_add_u32 s59, s59, 20971520\n"
    "buffer_load_dwordx4 v[96:99], v120, s[48:51], s59 offen\n"
    "buffer_load_dwordx4 v[100:103], v120, s[48:51], s59 offen offset:1024\n"
    "s_lshl_b32 s59, s75, 8\n"
    "s_add_u32 s59, s59, 17152\n"
    "v_lshlrev_b32 v142, 4, v143\n"
    "buffer_load_dwordx4 v[104:107], v142, s[48:51], s59 offen\n"
    "buffer_load_dwordx4 v[108:111], v142, s[48:51], s59 offen offset:64\n"
    "buffer_load_dwordx4 v[112:115], v142, s[48:51], s59 offen offset:128\n"
    "buffer_load_dwordx4 v[116:119], v142, s[48:51], s59 offen offset:192\n"
    "s_mul_i32 s78, s58, 14336\n"
    "s_lshl_b32 s59, s75, 7\n"
    "s_add_u32 s78, s78, s59\n"
    "v_and_b32 v142, 15, v148\n"
    "v_mul_u32_u24 v126, 896, v142\n"
    "v_and_b32 v142, 1, v143\n"
    "v_lshl_add_u32 v126, v142, 5, v126\n"
    "v_lshrrev_b32 v142, 1, v143\n"
    "v_lshl_add_u32 v126, v142, 4, v126\n"
    "s_add_u32 s59, s73, 98304\n"
    "v_add_u32 v124, s59, v120\n"
    "s_lshl_b32 s59, s52, 14\n"
    "s_add_u32 s59, s59, 98304\n"
    "v_add_u32 v125, s59, v120\n"
    "s_mov_b32 s79, 0xffff0000\n"
    "s_cmp_lg_u32 s52, 0\n"
    "s_cbranch_scc1 F_pd\n"
    "F_poll:\n"
    "buffer_load_dword v142, v141, s[48:51], s81 offen sc1\n"
    "s_add_u32 s80, s80, 1\n"
    "s_waitcnt vmcnt(0)\n"
    "v_cmp_ne_u32 vcc, 1, v142\n"
    "s_cbranch_vccz F_ok\n"
    "s_sleep 1\n"
    "s_cmp_lt_u32 s80, 400\n"
    "s_cbranch_scc1 F_poll\n"
    "F_ok:\n"
    "s_nop 1\n"
    "v_mov_b32 v142, vcc_lo\n"
    "s_nop 0\n"
    "ds_write_b32 v127, v142\n"
    "F_pd:\n"
    "s_waitcnt vmcnt(0) lgkmcnt(0)\n"
    "s_barrier\n"
    "ds_read_b32 v142, v127\n"
    "buffer_load_dwordx4 a[0:3], v120, s[48:51], s77 offen offset:0 sc1\n"
    "buffer_load_dwordx4 a[4:7], v121, s[48:51], s77 offen offset:0 sc1\n"
    "buffer_load_dwordx4 a[8:11], v122, s[48:51], s77 offen offset:0 sc1\n"
    "buffer_load_dwordx4 a[12:15], v123, s[48:51], s77 offen offset:0 sc1\n"
    "buffer_load_dwordx4 a[16:19], v120, s[48:51], s77 offen offset:1024 sc1\n"
    "buffer_load_dwordx4 a[20:23], v121, s[48:51], s77 offen offset:1024 sc1\n"
    "buffer_load_dwordx4 a[24:27], v122, s[48:51], s77 offen offset:1024 sc1\n"
    "buffer_load_dwordx4 a[28:31], v123, s[48:51], s77 offen offset:1024 sc1\n"
    "s_waitcnt lgkmcnt(0)\n"
    "v_readfirstlane_b32 s82, v142\n"
    "buffer_load_dwordx4 a[32:35], v120, s[48:51], s77 offen offset:2048 sc1\n"
    "buffer_load_dwordx4 a[36:39], v121, s[48:51], s77 offen offset:2048 sc1\n"
    "buffer_load_dwordx4 a[40:43], v122, s[48:51], s77 offen offset:2048 sc1\n"
    "buffer_load_dwordx4 a[44:47], v123, s[48:51], s77 offen offset:2048 sc1\n"
    "buffer_load_dwordx4 a[48:51], v120, s[48:51], s77 offen offset:3072 sc1\n"
    "buffer_load_dwordx4 a[52:55], v121, s[48:51], s77 offen offset:3072 sc1\n"
    "buffer_load_dwordx4 a[56:59], v122, s[48:51], s77 offen offset:3072 sc1\n"
    "buffer_load_dwordx4 a[60:63], v123, s[48:51], s77 offen offset:3072 sc1\n"
    "s_cmp_lg_u32 s82, 0\n"
    "s_cbranch_scc1 F_fb\n"
    "s_add_u32 s77, s77, 4096\n"
    "buffer_load_dwordx4 a[64:67], v120, s[48:51], s77 offen offset:0 sc1\n"
    "buffer_load_dwordx4 a[68:71], v121, s[48:51], s77 offen offset:0 sc1\n"
    "buffer_load_dwordx4 a[72:75], v122, s[48:51], s77 offen offset:0 sc1\n"
    "buffer_load_dwordx4 a[76:79], v123, s[48:51], s77 offen offset:0 sc1\n"
    "buffer_load_dwordx4 a[80:83], v120, s[48:51], s77 offen offset:1024 sc1\n"
    "buffer_load_dwordx4 a[84:87], v121, s[48:51], s77 offen offset:1024 sc1\n"
    "buffer_load_dwordx4 a[88:91], v122, s[48:51], s77 offen offset:1024 sc1\n"
    "buffer_load_dwordx4 a[92:95], v123, s[48:51], s77 offen offset:1024 sc1\n"
    "buffer_load_dwordx4 a[96:99], v120, s[48:51], s77 offen offset:2048 sc1\n"
    "buffer_load_dwordx4 a[100:103], v121, s[48:51], s77 offen offset:2048 sc1\n"
    "buffer_load_dwordx4 a[104:107], v122, s[48:51], s77 offen offset:2048 sc1\n"
    "buffer_load_dwordx4 a[108:111], v123, s[48:51], s77 offen offset:2048 sc1\n"
    "buffer_load_dwordx4 a[112:115], v120, s[48:51], s77 offen offset:3072 sc1\n"
    "buffer_load_dwordx4 a[116:119], v121, s[48:51], s77 offen offset:3072 sc1\n"
    "buffer_load_dwordx4 a[120:123], v122, s[48:51], s77 offen offset:3072 sc1\n"
    "buffer_load_dwordx4 a[124:127], v123, s[48:51], s77 offen offset:3072 sc1\n"
    "s_add_u32 s77, s77, 4096\n"
    "buffer_load_dwordx4 a[128:131], v120, s[48:51], s77 offen offset:0 sc1\n"
    "buffer_load_dwordx4 a[132:135], v121, s[48:51], s77 offen offset:0 sc1\n"
    "buffer_load_dwordx4 a[136:139], v122, s[48:51], s77 offen offset:0 sc1\n"
    "buffer_load_dwordx4 a[140:143], v123, s[48:51], s77 offen offset:0 sc1\n"
    "buffer_load_dwordx4 a[144:147], v120, s[48:51], s77 offen offset:1024 sc1\n"
    "buffer_load_dwordx4 a[148:151], v121, s[48:51], s77 offen offset:1024 sc1\n"
    "buffer_load_dwordx4 a[152:155], v122, s[48:51], s77 offen offset:1024 sc1\n"
    "buffer_load_dwordx4 a[156:159], v123, s[48:51], s77 offen offset:1024 sc1\n"
    "buffer_load_dwordx4 a[160:163], v120, s[48:51], s77 offen offset:2048 sc1\n"
    "buffer_load_dwordx4 a[164:167], v121, s[48:51], s77 offen offset:2048 sc1\n"
    "buffer_load_dwordx4 a[168:171], v122, s[48:51], s77 offen offset:2048 sc1\n"
    "buffer_load_dwordx4 a[172:175], v123, s[48:51], s77 offen offset:2048 sc1\n"
    "buffer_load_dwordx4 a[176:179], v120, s[48:51], s77 offen offset:3072 sc1\n"
    "buffer_load_dwordx4 a[180:183], v121, s[48:51], s77 offen offset:3072 sc1\n"
    "buffer_load_dwordx4 a[184:187], v122, s[48:51], s77 offen offset:3072 sc1\n"
    "buffer_load_dwordx4 a[188:191], v123, s[48:51], s77 offen offset:3072 sc1\n"
    "s_add_u32 s77, s77, 4096\n"
    "buffer_load_dwordx4 a[192:195], v120, s[48:51], s77 offen offset:0 sc1\n"
    "buffer_load_dwordx4 a[196:199], v121, s[48:51], s77 offen offset:0 sc1\n"
    "buffer_load_dwordx4 a[200:203], v122, s[48:51], s77 offen offset:0 sc1\n"
    "buffer_load_dwordx4 a[204:207], v123, s[48:51], s77 offen offset:0 sc1\n"
    "buffer_load_dwordx4 a[208:211], v120, s[48:51], s77 offen offset:1024 sc1\n"
    "buffer_load_dwordx4 a[212:215], v121, s[48:51], s77 offen offset:1024 sc1\n"
    "buffer_load_dwordx4 a[216:219], v122, s[48:51], s77 offen offset:1024 sc1\n"
    "buffer_load_dwordx4 a[220:223], v123, s[48:51], s77 offen offset:1024 sc1\n"
    "s_waitcnt vmcnt(52) lgkmcnt(0)\n"
    "v_mfma_f32_16x16x32_bf16 v[0:3], v[192:195], a[0:3], 0\n"
    "v_mfma_f32_16x16x32_bf16 v[16:19], v[192:195], a[4:7], 0\n"
    "v_mfma_f32_16x16x32_bf16 v[32:35], v[192:195], a[8:11], 0\n"
    "v_mfma_f32_16x16x32_bf16 v[48:51], v[192:195], a[12:15], 0\n"
    "v_mfma_f32_16x16x32_bf16 v[4:7], v[196:199], a[0:3], 0\n"
    "v_mfma_f32_16x16x32_bf16 v[20:23], v[196:199], a[4:7], 0\n"
    "v_mfma_f32_16x16x32_bf16 v[36:39], v[196:199], a[8:11], 0\n"
    "v_mfma_f32_16x16x32_bf16 v[52:55], v[196:199], a[12:15], 0\n"
    "v_mfma_f32_16x16x32_bf16 v[8:11], v[200:203], a[0:3], 0\n"
    "v_mfma_f32_16x16x32_bf16 v[24:27], v[200:203], a[4:7], 0\n"
    "v_mfma_f32_16x16x32_bf16 v[40:43], v[200:203], a[8:11], 0\n"
    "v_mfma_f32_16x16x32_bf16 v[56:59], v[200:203], a[12:15], 0\n"
    "v_mfma_f32_16x16x32_bf16 v[12:15], v[204:207], a[0:3], 0\n"
    "v_mfma_f32_16x16x32_bf16 v[28:31], v[204:207], a[4:7], 0\n"
    "v_mfma_f32_16x16x32_bf16 v[44:47], v[204:207], a[8:11], 0\n"
    "v_mfma_f32_16x16x32_bf16 v[60:63], v[204:207], a[12:15], 0\n"
    "s_waitcnt vmcnt(48) lgkmcnt(0)\n"
    "v_mfma_f32_16x16x32_bf16 v[0:3], v[208:211], a[16:19], v[0:3]\n"
    "v_mfma_f32_16x16x32_bf16 v[16:19], v[208:211], a[20:23], v[16:19]\n"
    "v_mfma_f32_16x16x32_bf16 v[32:35], v[208:211], a[24:27], v[32:35]\n"
    "v_mfma_f32_16x16x32_bf16 v[48:51], v[208:211], a[28:31], v[48:51]\n"
    "v_mfma_f32_16x16x32_bf16 v[4:7], v[212:215], a[16:19], v[4:7]\n"
    "v_mfma_f32_16x16x32_bf16 v[20:23], v[212:215], a[20:23], v[20:23]\n"
    "v_mfma_f32_16x16x32_bf16 v[36:39], v[212:215], a[24:27], v[36:39]\n"
    "v_mfma_f32_16x16x32_bf16 v[52:55], v[212:215], a[28:31], v[52:55]\n"
    "v_mfma_f32_16x16x32_bf16 v[8:11], v[216:219], a[16:19], v[8:11]\n"
    "v_mfma_f32_16x16x32_bf16 v[24:27], v[216:219], a[20:23], v[24:27]\n"
    "v_mfma_f32_16x16x32_bf16 v[40:43], v[216:219], a[24:27], v[40:43]\n"
    "v_mfma_f32_16x16x32_bf16 v[56:59], v[216:219], a[28:31], v[56:59]\n"
    "v_mfma_f32_16x16x32_bf16 v[12:15], v[220:223], a[16:19], v[12:15]\n"
    "v_mfma_f32_16x16x32_bf16 v[28:31], v[220:223], a[20:23], v[28:31]\n"
    "v_mfma_f32_16x16x32_bf16 v[44:47], v[220:223], a[24:27], v[44:47]\n"
    "v_mfma_f32_16x16x32_bf16 v[60:63], v[220:223], a[28:31], v[60:63]\n"
    "s_waitcnt vmcnt(44) lgkmcnt(0)\n"
    "v_mfma_f32_16x16x32_bf16 v[0:3], v[224:227], a[32:35], v[0:3]\n"
    "v_mfma_f32_16x16x32_bf16 v[16:19], v[224:227], a[36:39], v[16:19]\n"
    "v_mfma_f32_16x16x32_bf16 v[32:35], v[224:227], a[40:43], v[32:35]\n"
    "v_mfma_f32_16x16x32_bf16 v[48:51], v[224:227], a[44:47], v[48:51]\n"
    "v_mfma_f32_16x16x32_bf16 v[4:7], v[228:231], a[32:35], v[4:7]\n"
    "v_mfma_f32_16x16x32_bf16 v[20:23], v[228:231], a[36:39], v[20:23]\n"
    "v_mfma_f32_16x16x32_bf16 v[36:39], v[228:231], a[40:43], v[36:39]\n"
    "v_mfma_f32_16x16x32_bf16 v[52:55], v[228:231], a[44:47], v[52:55]\n"
    "v_mfma_f32_16x16x32_bf16 v[8:11], v[232:235], a[32:35], v[8:11]\n"
    "v_mfma_f32_16x16x32_bf16 v[24:27], v[232:235], a[36:39], v[24:27]\n"
    "v_mfma_f32_16x16x32_bf16 v[40:43], v[232:235], a[40:43], v[40:43]\n"
    "v_mfma_f32_16x16x32_bf16 v[56:59], v[232:235], a[44:47], v[56:59]\n"
    "v_mfma_f32_16x16x32_bf16 v[12:15], v[236:239], a[32:35], v[12:15]\n"
    "v_mfma_f32_16x16x32_bf16 v[28:31], v[236:239], a[36:39], v[28:31]\n"
    "v_mfma_f32_16x16x32_bf16 v[44:47], v[236:239], a[40:43], v[44:47]\n"
    "v_mfma_f32_16x16x32_bf16 v[60:63], v[236:239], a[44:47], v[60:63]\n"
    "s_waitcnt vmcnt(40) lgkmcnt(0)\n"
    "v_mfma_f32_16x16x32_bf16 v[0:3], v[240:243], a[48:51], v[0:3]\n"
    "v_mfma_f32_16x16x32_bf16 v[16:19], v[240:243], a[52:55], v[16:19]\n"
    "v_mfma_f32_16x16x32_bf16 v[32:35], v[240:243], a[56:59], v[32:35]\n"
    "v_mfma_f32_16x16x32_bf16 v[48:51], v[240:243], a[60:63], v[48:51]\n"
    "v_mfma_f32_16x16x32_bf16 v[4:7], v[244:247], a[48:51], v[4:7]\n"
    "v_mfma_f32_16x16x32_bf16 v[20:23], v[244:247], a[52:55], v[20:23]\n"
    "v_mfma_f32_16x16x32_bf16 v[36:39], v[244:247], a[56:59], v[36:39]\n"
    "v_mfma_f32_16x16x32_bf16 v[52:55], v[244:247], a[60:63], v[52:55]\n"
    "v_mfma_f32_16x16x32_bf16 v[8:11], v[248:251], a[48:51], v[8:11]\n"
    "v_mfma_f32_16x16x32_bf16 v[24:27], v[248:251], a[52:55], v[24:27]\n"
    "v_mfma_f32_16x16x32_bf16 v[40:43], v[248:251], a[56:59], v[40:43]\n"
    "v_mfma_f32_16x16x32_bf16 v[56:59], v[248:251], a[60:63], v[56:59]\n"
    "v_mfma_f32_16x16x32_bf16 v[12:15], v[252:255], a[48:51], v[12:15]\n"
    "v_mfma_f32_16x16x32_bf16 v[28:31], v[252:255], a[52:55], v[28:31]\n"
    "v_mfma_f32_16x16x32_bf16 v[44:47], v[252:255], a[56:59], v[44:47]\n"
    "v_mfma_f32_16x16x32_bf16 v[60:63], v[252:255], a[60:63], v[60:63]\n"
    "s_waitcnt vmcnt(36) lgkmcnt(0)\n"
    "v_mfma_f32_16x16x32_bf16 v[0:3], a[240:243], a[64:67], v[0:3]\n"
    "ds_read_b64_tr_b16 v[80:81], v132 offset:16384\n"
    "v_mfma_f32_16x16x32_bf16 v[16:19], a[240:243], a[68:71], v[16:19]\n"
    "ds_read_b64_tr_b16 v[82:83], v132 offset:18432\n"
    "v_mfma_f32_16x16x32_bf16 v[32:35], a[240:243], a[72:75], v[32:35]\n"
    "ds_read_b64_tr_b16 v[84:85], v133 offset:16384\n"
    "v_mfma_f32_16x16x32_bf16 v[48:51], a[240:243], a[76:79], v[48:51]\n"
    "ds_read_b64_tr_b16 v[86:87], v133 offset:18432\n"
    "v_mfma_f32_16x16x32_bf16 v[4:7], a[244:247], a[64:67], v[4:7]\n"
    "ds_read_b64_tr_b16 v[88:89], v134 offset:16384\n"
    "v_mfma_f32_16x16x32_bf16 v[20:23], a[244:247], a[68:71], v[20:23]\n"
    "ds_read_b64_tr_b16 v[90:91], v134 offset:18432\n"
    "v_mfma_f32_16x16x32_bf16 v[36:39], a[244:247], a[72:75], v[36:39]\n"
    "ds_read_b64_tr_b16 v[92:93], v135 offset:16384\n"
    "v_mfma_f32_16x16x32_bf16 v[52:55], a[244:247], a[76:79], v[52:55]\n"
    "ds_read_b64_tr_b16 v[94:95], v135 offset:18432\n"
    "v_mfma_f32_16x16x32_bf16 v[8:11], a[248:251], a[64:67], v[8:11]\n"
    "v_mfma_f32_16x16x32_bf16 v[24:27], a[248:251], a[68:71], v[24:27]\n"
    "v_mfma_f32_16x16x32_bf16 v[40:43], a[248:251], a[72:75], v[40:43]\n"
    "v_mfma_f32_16x16x32_bf16 v[56:59], a[248:251], a[76:79], v[56:59]\n"
    "v_mfma_f32_16x16x32_bf16 v[12:15], a[252:255], a[64:67], v[12:15]\n"
    "v_mfma_f32_16x16x32_bf16 v[28:31], a[252:255], a[68:71], v[28:31]\n"
    "v_mfma_f32_16x16x32_bf16 v[44:47], a[252:255], a[72:75], v[44:47]\n"
    "v_mfma_f32_16x16x32_bf16 v[60:63], a[252:255], a[76:79], v[60:63]\n"
    "s_waitcnt vmcnt(32) lgkmcnt(0)\n"
    "v_mfma_f32_16x16x32_bf16 v[0:3], v[80:83], a[80:83], v[0:3]\n"
    "ds_read_b64_tr_b16 v[64:65], v128 offset:0\n"
    "v_mfma_f32_16x16x32_bf16 v[16:19], v[80:83], a[84:87], v[16:19]\n"
    "ds_read_b64_tr_b16 v[66:67], v128 offset:2048\n"
    "v_mfma_f32_16x16x32_bf16 v[32:35], v[80:83], a[88:91], v[32:35]\n"
    "ds_read_b64_tr_b16 v[68:69], v129 offset:0\n"
    "v_mfma_f32_16x16x32_bf16 v[48:51], v[80:83], a[92:95], v[48:51]\n"
    "ds_read_b64_tr_b16 v[70:71], v129 offset:2048\n"
    "v_mfma_f32_16x16x32_bf16 v[4:7], v[84:87], a[80:83], v[4:7]\n"
    "ds_read_b64_tr_b16 v[72:73], v130 offset:0\n"
    "v_mfma_f32_16x16x32_bf16 v[20:23], v[84:87], a[84:87], v[20:23]\n"
    "ds_read_b64_tr_b16 v[74:75], v130 offset:2048\n"
    "v_mfma_f32_16x16x32_bf16 v[36:39], v[84:87], a[88:91], v[36:39]\n"
    "ds_read_b64_tr_b16 v[76:77], v131 offset:0\n"
    "v_mfma_f32_16x16x32_bf16 v[52:55], v[84:87], a[92:95], v[52:55]\n"
    "ds_read_b64_tr_b16 v[78:79], v131 offset:2048\n"
    "v_mfma_f32_16x16x32_bf16 v[8:11], v[88:91], a[80:83], v[8:11]\n"
    "v_mfma_f32_16x16x32_bf16 v[24:27], v[88:91], a[84:87], v[24:27]\n"
    "v_mfma_f32_16x16x32_bf16 v[40:43], v[88:91], a[88:91], v[40:43]\n"
    "v_mfma_f32_16x16x32_bf16 v[56:59], v[88:91], a[92:95], v[56:59]\n"
    "v_mfma_f32_16x16x32_bf16 v[12:15], v[92:95], a[80:83], v[12:15]\n"
    "v_mfma_f32_16x16x32_bf16 v[28:31], v[92:95], a[84:87], v[28:31]\n"
    "v_mfma_f32_16x16x32_bf16 v[44:47], v[92:95], a[88:91], v[44:47]\n"
    "v_mfma_f32_16x16x32_bf16 v[60:63], v[92:95], a[92:95], v[60:63]\n"
    "s_waitcnt vmcnt(28) lgkmcnt(0)\n"
    "v_mfma_f32_16x16x32_bf16 v[0:3], v[64:67], a[96:99], v[0:3]\n"
    "ds_read_b64_tr_b16 v[80:81], v128 offset:16384\n"
    "v_mfma_f32_16x16x32_bf16 v[16:19], v[64:67], a[100:103], v[16:19]\n"
    "ds_read_b64_tr_b16 v[82:83], v128 offset:18432\n"
    "v_mfma_f32_16x16x32_bf16 v[32:35], v[64:67], a[104:107], v[32:35]\n"
    "ds_read_b64_tr_b16 v[84:85], v129 offset:16384\n"
    "v_mfma_f32_16x16x32_bf16 v[48:51], v[64:67], a[108:111], v[48:51]\n"
    "ds_read_b64_tr_b16 v[86:87], v129 offset:18432\n"
    "v_mfma_f32_16x16x32_bf16 v[4:7], v[68:71], a[96:99], v[4:7]\n"
    "ds_read_b64_tr_b16 v[88:89], v130 offset:16384\n"
    "v_mfma_f32_16x16x32_bf16 v[20:23], v[68:71], a[100:103], v[20:23]\n"
    "ds_read_b64_tr_b16 v[90:91], v130 offset:18432\n"
    "v_mfma_f32_16x16x32_bf16 v[36:39], v[68:71], a[104:107], v[36:39]\n"
    "ds_read_b64_tr_b16 v[92:93], v131 offset:16384\n"
    "v_mfma_f32_16x16x32_bf16 v[52:55], v[68:71], a[108:111], v[52:55]\n"
    "ds_read_b64_tr_b16 v[94:95], v131 offset:18432\n"
    "v_mfma_f32_16x16x32_bf16 v[8:11], v[72:75], a[96:99], v[8:11]\n"
    "v_mfma_f32_16x16x32_bf16 v[24:27], v[72:75], a[100:103], v[24:27]\n"
    "v_mfma_f32_16x16x32_bf16 v[40:43], v[72:75], a[104:107], v[40:43]\n"
    "v_mfma_f32_16x16x32_bf16 v[56:59], v[72:75], a[108:111], v[56:59]\n"
    "v_mfma_f32_16x16x32_bf16 v[12:15], v[76:79], a[96:99], v[12:15]\n"
    "v_mfma_f32_16x16x32_bf16 v[28:31], v[76:79], a[100:103], v[28:31]\n"
    "v_mfma_f32_16x16x32_bf16 v[44:47], v[76:79], a[104:107], v[44:47]\n"
    "v_mfma_f32_16x16x32_bf16 v[60:63], v[76:79], a[108:111], v[60:63]\n"
    "s_waitcnt vmcnt(24) lgkmcnt(0)\n"
    "v_mfma_f32_16x16x32_bf16 v[0:3], v[80:83], a[112:115], v[0:3]\n"
    "ds_read_b64_tr_b16 v[64:65], v128 offset:32768\n"
    "v_mfma_f32_16x16x32_bf16 v[16:19], v[80:83], a[116:119], v[16:19]\n"
    "ds_read_b64_tr_b16 v[66:67], v128 offset:34816\n"
    "v_mfma_f32_16x16x32_bf16 v[32:35], v[80:83], a[120:123], v[32:35]\n"
    "ds_read_b64_tr_b16 v[68:69], v129 offset:32768\n"
    "v_mfma_f32_16x16x32_bf16 v[48:51], v[80:83], a[124:127], v[48:51]\n"
    "ds_read_b64_tr_b16 v[70:71], v129 offset:34816\n"
    "v_mfma_f32_16x16x32_bf16 v[4:7], v[84:87], a[112:115], v[4:7]\n"
    "ds_read_b64_tr_b16 v[72:73], v130 offset:32768\n"
    "v_mfma_f32_16x16x32_bf16 v[20:23], v[84:87], a[116:119], v[20:23]\n"
    "ds_read_b64_tr_b16 v[74:75], v130 offset:34816\n"
    "v_mfma_f32_16x16x32_bf16 v[36:39], v[84:87], a[120:123], v[36:39]\n"
    "ds_read_b64_tr_b16 v[76:77], v131 offset:32768\n"
    "v_mfma_f32_16x16x32_bf16 v[52:55], v[84:87], a[124:127], v[52:55]\n"
    "ds_read_b64_tr_b16 v[78:79], v131 offset:34816\n"
    "v_mfma_f32_16x16x32_bf16 v[8:11], v[88:91], a[112:115], v[8:11]\n"
    "v_mfma_f32_16x16x32_bf16 v[24:27], v[88:91], a[116:119], v[24:27]\n"
    "v_mfma_f32_16x16x32_bf16 v[40:43], v[88:91], a[120:123], v[40:43]\n"
    "v_mfma_f32_16x16x32_bf16 v[56:59], v[88:91], a[124:127], v[56:59]\n"
    "v_mfma_f32_16x16x32_bf16 v[12:15], v[92:95], a[112:115], v[12:15]\n"
    "v_mfma_f32_16x16x32_bf16 v[28:31], v[92:95], a[116:119], v[28:31]\n"
    "v_mfma_f32_16x16x32_bf16 v[44:47], v[92:95], a[120:123], v[44:47]\n"
    "v_mfma_f32_16x16x32_bf16 v[60:63], v[92:95], a[124:127], v[60:63]\n"
    "s_waitcnt vmcnt(20) lgkmcnt(0)\n"
    "v_mfma_f32_16x16x32_bf16 v[0:3], v[64:67], a[128:131], v[0:3]\n"
    "ds_read_b64_tr_b16 v[80:81], v128 offset:49152\n"
    "v_mfma_f32_16x16x32_bf16 v[16:19], v[64:67], a[132:135], v[16:19]\n"
    "ds_read_b64_tr_b16 v[82:83], v128 offset:51200\n"
    "v_mfma_f32_16x16x32_bf16 v[32:35], v[64:67], a[136:139], v[32:35]\n"
    "ds_read_b64_tr_b16 v[84:85], v129 offset:49152\n"
    "v_mfma_f32_16x16x32_bf16 v[48:51], v[64:67], a[140:143], v[48:51]\n"
    "ds_read_b64_tr_b16 v[86:87], v129 offset:51200\n"
    "v_mfma_f32_16x16x32_bf16 v[4:7], v[68:71], a[128:131], v[4:7]\n"
    "ds_read_b64_tr_b16 v[88:89], v130 offset:49152\n"
    "v_mfma_f32_16x16x32_bf16 v[20:23], v[68:71], a[132:135], v[20:23]\n"
    "ds_read_b64_tr_b16 v[90:91], v130 offset:51200\n"
    "v_mfma_f32_16x16x32_bf16 v[36:39], v[68:71], a[136:139], v[36:39]\n"
    "ds_read_b64_tr_b16 v[92:93], v131 offset:49152\n"
    "v_mfma_f32_16x16x32_bf16 v[52:55], v[68:71], a[140:143], v[52:55]\n"
    "ds_read_b64_tr_b16 v[94:95], v131 offset:51200\n"
    "v_mfma_f32_16x16x32_bf16 v[8:11], v[72:75], a[128:131], v[8:11]\n"
    "v_mfma_f32_16x16x32_bf16 v[24:27], v[72:75], a[132:135], v[24:27]\n"
    "v_mfma_f32_16x16x32_bf16 v[40:43], v[72:75], a[136:139], v[40:43]\n"
    "v_mfma_f32_16x16x32_bf16 v[56:59], v[72:75], a[140:143], v[56:59]\n"
    "v_mfma_f32_16x16x32_bf16 v[12:15], v[76:79], a[128:131], v[12:15]\n"
    "v_mfma_f32_16x16x32_bf16 v[28:31], v[76:79], a[132:135], v[28:31]\n"
    "v_mfma_f32_16x16x32_bf16 v[44:47], v[76:79], a[136:139], v[44:47]\n"
    "v_mfma_f32_16x16x32_bf16 v[60:63], v[76:79], a[140:143], v[60:63]\n"
    "s_waitcnt vmcnt(16) lgkmcnt(0)\n"
    "v_mfma_f32_16x16x32_bf16 v[0:3], v[80:83], a[144:147], v[0:3]\n"
    "ds_read_b64_tr_b16 v[64:65], v132 offset:32768\n"
    "v_mfma_f32_16x16x32_bf16 v[16:19], v[80:83], a[148:151], v[16:19]\n"
    "ds_read_b64_tr_b16 v[66:67], v132 offset:34816\n"
    "v_mfma_f32_16x16x32_bf16 v[32:35], v[80:83], a[152:155], v[32:35]\n"
    "ds_read_b64_tr_b16 v[68:69], v133 offset:32768\n"
    "v_mfma_f32_16x16x32_bf16 v[48:51], v[80:83], a[156:159], v[48:51]\n"
    "ds_read_b64_tr_b16 v[70:71], v133 offset:34816\n"
    "v_mfma_f32_16x16x32_bf16 v[4:7], v[84:87], a[144:147], v[4:7]\n"
    "ds_read_b64_tr_b16 v[72:73], v134 offset:32768\n"
    "v_mfma_f32_16x16x32_bf16 v[20:23], v[84:87], a[148:151], v[20:23]\n"
    "ds_read_b64_tr_b16 v[74:75], v134 offset:34816\n"
    "v_mfma_f32_16x16x32_bf16 v[36:39], v[84:87], a[152:155], v[36:39]\n"
    "ds_read_b64_tr_b16 v[76:77], v135 offset:32768\n"
    "v_mfma_f32_16x16x32_bf16 v[52:55], v[84:87], a[156:159], v[52:55]\n"
    "ds_read_b64_tr_b16 v[78:79], v135 offset:34816\n"
    "v_mfma_f32_16x16x32_bf16 v[8:11], v[88:91], a[144:147], v[8:11]\n"
    "v_mfma_f32_16x16x32_bf16 v[24:27], v[88:91], a[148:151], v[24:27]\n"
    "v_mfma_f32_16x16x32_bf16 v[40:43], v[88:91], a[152:155], v[40:43]\n"
    "v_mfma_f32_16x16x32_bf16 v[56:59], v[88:91], a[156:159], v[56:59]\n"
    "v_mfma_f32_16x16x32_bf16 v[12:15], v[92:95], a[144:147], v[12:15]\n"
    "v_mfma_f32_16x16x32_bf16 v[28:31], v[92:95], a[148:151], v[28:31]\n"
    "v_mfma_f32_16x16x32_bf16 v[44:47], v[92:95], a[152:155], v[44:47]\n"
    "v_mfma_f32_16x16x32_bf16 v[60:63], v[92:95], a[156:159], v[60:63]\n"
    "s_waitcnt vmcnt(12) lgkmcnt(0)\n"
    "v_mfma_f32_16x16x32_bf16 v[0:3], v[64:67], a[160:163], v[0:3]\n"
    "ds_read_b64_tr_b16 v[80:81], v132 offset:49152\n"
    "v_mfma_f32_16x16x32_bf16 v[16:19], v[64:67], a[164:167], v[16:19]\n"
    "ds_read_b64_tr_b16 v[82:83], v132 offset:51200\n"
    "v_mfma_f32_16x16x32_bf16 v[32:35], v[64:67], a[168:171], v[32:35]\n"
    "ds_read_b64_tr_b16 v[84:85], v133 offset:49152\n"
    "v_mfma_f32_16x16x32_bf16 v[48:51], v[64:67], a[172:175], v[48:51]\n"
    "ds_read_b64_tr_b16 v[86:87], v133 offset:51200\n"
    "v_mfma_f32_16x16x32_bf16 v[4:7], v[68:71], a[160:163], v[4:7]\n"
    "ds_read_b64_tr_b16 v[88:89], v134 offset:49152\n"
    "v_mfma_f32_16x16x32_bf16 v[20:23], v[68:71], a[164:167], v[20:23]\n"
    "ds_read_b64_tr_b16 v[90:91], v134 offset:51200\n"
    "v_mfma_f32_16x16x32_bf16 v[36:39], v[68:71], a[168:171], v[36:39]\n"
    "ds_read_b64_tr_b16 v[92:93], v135 offset:49152\n"
    "v_mfma_f32_16x16x32_bf16 v[52:55], v[68:71], a[172:175], v[52:55]\n"
    "ds_read_b64_tr_b16 v[94:95], v135 offset:51200\n"
    "v_mfma_f32_16x16x32_bf16 v[8:11], v[72:75], a[160:163], v[8:11]\n"
    "v_mfma_f32_16x16x32_bf16 v[24:27], v[72:75], a[164:167], v[24:27]\n"
    "v_mfma_f32_16x16x32_bf16 v[40:43], v[72:75], a[168:171], v[40:43]\n"
    "v_mfma_f32_16x16x32_bf16 v[56:59], v[72:75], a[172:175], v[56:59]\n"
    "v_mfma_f32_16x16x32_bf16 v[12:15], v[76:79], a[160:163], v[12:15]\n"
    "v_mfma_f32_16x16x32_bf16 v[28:31], v[76:79], a[164:167], v[28:31]\n"
    "v_mfma_f32_16x16x32_bf16 v[44:47], v[76:79], a[168:171], v[44:47]\n"
    "v_mfma_f32_16x16x32_bf16 v[60:63], v[76:79], a[172:175], v[60:63]\n"
    "s_waitcnt vmcnt(8) lgkmcnt(0)\n"
    "v_mfma_f32_16x16x32_bf16 v[0:3], v[80:83], a[176:179], v[0:3]\n"
    "ds_read_b64_tr_b16 v[64:65], v136 offset:0\n"
    "v_mfma_f32_16x16x32_bf16 v[16:19], v[80:83], a[180:183], v[16:19]\n"
    "ds_read_b64_tr_b16 v[66:67], v136 offset:2048\n"
    "v_mfma_f32_16x16x32_bf16 v[32:35], v[80:83], a[184:187], v[32:35]\n"
    "ds_read_b64_tr_b16 v[68:69], v137 offset:0\n"
    "v_mfma_f32_16x16x32_bf16 v[48:51], v[80:83], a[188:191], v[48:51]\n"
    "ds_read_b64_tr_b16 v[70:71], v137 offset:2048\n"
    "v_mfma_f32_16x16x32_bf16 v[4:7], v[84:87], a[176:179], v[4:7]\n"
    "ds_read_b64_tr_b16 v[72:73], v138 offset:0\n"
    "v_mfma_f32_16x16x32_bf16 v[20:23], v[84:87], a[180:183], v[20:23]\n"
    "ds_read_b64_tr_b16 v[74:75], v138 offset:2048\n"
    "v_mfma_f32_16x16x32_bf16 v[36:39], v[84:87], a[184:187], v[36:39]\n"
    "ds_read_b64_tr_b16 v[76:77], v139 offset:0\n"
    "v_mfma_f32_16x16x32_bf16 v[52:55], v[84:87], a[188:191], v[52:55]\n"
    "ds_read_b64_tr_b16 v[78:79], v139 offset:2048\n"
    "v_mfma_f32_16x16x32_bf16 v[8:11], v[88:91], a[176:179], v[8:11]\n"
    "v_mfma_f32_16x16x32_bf16 v[24:27], v[88:91], a[180:183], v[24:27]\n"
    "v_mfma_f32_16x16x32_bf16 v[40:43], v[88:91], a[184:187], v[40:43]\n"
    "v_mfma_f32_16x16x32_bf16 v[56:59], v[88:91], a[188:191], v[56:59]\n"
    "v_mfma_f32_16x16x32_bf16 v[12:15], v[92:95], a[176:179], v[12:15]\n"
    "v_mfma_f32_16x16x32_bf16 v[28:31], v[92:95], a[180:183], v[28:31]\n"
    "v_mfma_f32_16x16x32_bf16 v[44:47], v[92:95], a[184:187], v[44:47]\n"
    "v_mfma_f32_16x16x32_bf16 v[60:63], v[92:95], a[188:191], v[60:63]\n"
    "s_waitcnt vmcnt(4) lgkmcnt(0)\n"
    "v_mfma_f32_16x16x32_bf16 v[0:3], v[64:67], a[192:195], v[0:3]\n"
    "ds_read_b64_tr_b16 v[80:81], v136 offset:16384\n"
    "v_mfma_f32_16x16x32_bf16 v[16:19], v[64:67], a[196:199], v[16:19]\n"
    "ds_read_b64_tr_b16 v[82:83], v136 offset:18432\n"
    "v_mfma_f32_16x16x32_bf16 v[32:35], v[64:67], a[200:203], v[32:35]\n"
    "ds_read_b64_tr_b16 v[84:85], v137 offset:16384\n"
    "v_mfma_f32_16x16x32_bf16 v[48:51], v[64:67], a[204:207], v[48:51]\n"
    "ds_read_b64_tr_b16 v[86:87], v137 offset:18432\n"
    "v_mfma_f32_16x16x32_bf16 v[4:7], v[68:71], a[192:195], v[4:7]\n"
    "ds_read_b64_tr_b16 v[88:89], v138 offset:16384\n"
    "v_mfma_f32_16x16x32_bf16 v[20:23], v[68:71], a[196:199], v[20:23]\n"
    "ds_read_b64_tr_b16 v[90:91], v138 offset:18432\n"
    "v_mfma_f32_16x16x32_bf16 v[36:39], v[68:71], a[200:203], v[36:39]\n"
    "ds_read_b64_tr_b16 v[92:93], v139 offset:16384\n"
    "v_mfma_f32_16x16x32_bf16 v[52:55], v[68:71], a[204:207], v[52:55]\n"
    "ds_read_b64_tr_b16 v[94:95], v139 offset:18432\n"
    "v_mfma_f32_16x16x32_bf16 v[8:11], v[72:75], a[192:195], v[8:11]\n"
    "v_mfma_f32_16x16x32_bf16 v[24:27], v[72:75], a[196:199], v[24:27]\n"
    "v_mfma_f32_16x16x32_bf16 v[40:43], v[72:75], a[200:203], v[40:43]\n"
    "v_mfma_f32_16x16x32_bf16 v[56:59], v[72:75], a[204:207], v[56:59]\n"
    "v_mfma_f32_16x16x32_bf16 v[12:15], v[76:79], a[192:195], v[12:15]\n"
    "v_mfma_f32_16x16x32_bf16 v[28:31], v[76:79], a[196:199], v[28:31]\n"
    "v_mfma_f32_16x16x32_bf16 v[44:47], v[76:79], a[200:203], v[44:47]\n"
    "v_mfma_f32_16x16x32_bf16 v[60:63], v[76:79], a[204:207], v[60:63]\n"
    "s_waitcnt vmcnt(0) lgkmcnt(0)\n"
    "v_mfma_f32_16x16x32_bf16 v[0:3], v[80:83], a[208:211], v[0:3]\n"
    "v_mfma_f32_16x16x32_bf16 v[16:19], v[80:83], a[212:215], v[16:19]\n"
    "v_mfma_f32_16x16x32_bf16 v[32:35], v[80:83], a[216:219], v[32:35]\n"
    "v_mfma_f32_16x16x32_bf16 v[48:51], v[80:83], a[220:223], v[48:51]\n"
    "v_mfma_f32_16x16x32_bf16 v[4:7], v[84:87], a[208:211], v[4:7]\n"
    "v_mfma_f32_16x16x32_bf16 v[20:23], v[84:87], a[212:215], v[20:23]\n"
    "v_mfma_f32_16x16x32_bf16 v[36:39], v[84:87], a[216:219], v[36:39]\n"
    "v_mfma_f32_16x16x32_bf16 v[52:55], v[84:87], a[220:223], v[52:55]\n"
    "v_mfma_f32_16x16x32_bf16 v[8:11], v[88:91], a[208:211], v[8:11]\n"
    "v_mfma_f32_16x16x32_bf16 v[24:27], v[88:91], a[212:215], v[24:27]\n"
    "v_mfma_f32_16x16x32_bf16 v[40:43], v[88:91], a[216:219], v[40:43]\n"
    "v_mfma_f32_16x16x32_bf16 v[56:59], v[88:91], a[220:223], v[56:59]\n"
    "v_mfma_f32_16x16x32_bf16 v[12:15], v[92:95], a[208:211], v[12:15]\n"
    "v_mfma_f32_16x16x32_bf16 v[28:31], v[92:95], a[212:215], v[28:31]\n"
    "v_mfma_f32_16x16x32_bf16 v[44:47], v[92:95], a[216:219], v[44:47]\n"
    "v_mfma_f32_16x16x32_bf16 v[60:63], v[92:95], a[220:223], v[60:63]\n"
    "s_nop 7\n"
    "ds_write_b128 v124, v[0:3] offset:0\n"
    "ds_write_b128 v124, v[4:7] offset:1024\n"
    "ds_write_b128 v124, v[8:11] offset:2048\n"
    "ds_write_b128 v124, v[12:15] offset:3072\n"
    "ds_write_b128 v124, v[16:19] offset:16384\n"
    "ds_write_b128 v124, v[20:23] offset:17408\n"
    "ds_write_b128 v124, v[24:27] offset:18432\n"
    "ds_write_b128 v124, v[28:31] offset:19456\n"
    "ds_write_b128 v124, v[32:35] offset:32768\n"
    "ds_write_b128 v124, v[36:39] offset:33792\n"
    "ds_write_b128 v124, v[40:43] offset:34816\n"
    "ds_write_b128 v124, v[44:47] offset:35840\n"
    "ds_write_b128 v124, v[48:51] offset:49152\n"
    "ds_write_b128 v124, v[52:55] offset:50176\n"
    "ds_write_b128 v124, v[56:59] offset:51200\n"
    "ds_write_b128 v124, v[60:63] offset:52224\n"
    "s_waitcnt lgkmcnt(0)\n"
    "s_barrier\n"
    "ds_read_b128 v[160:163], v125 offset:0\n"
    "ds_read_b128 v[164:167], v125 offset:1024\n"
    "ds_read_b128 v[168:171], v125 offset:2048\n"
    "ds_read_b128 v[172:175], v125 offset:3072\n"
    "ds_read_b128 v[176:179], v125 offset:4096\n"
    "ds_read_b128 v[180:183], v125 offset:5120\n"
    "ds_read_b128 v[184:187], v125 offset:6144\n"
    "ds_read_b128 v[188:191], v125 offset:7168\n"
    "ds_read_b128 v[192:195], v125 offset:8192\n"
    "ds_read_b128 v[196:199], v125 offset:9216\n"
    "ds_read_b128 v[200:203], v125 offset:10240\n"
    "ds_read_b128 v[204:207], v125 offset:11264\n"
    "ds_read_b128 v[208:211], v125 offset:12288\n"
    "ds_read_b128 v[212:215], v125 offset:13312\n"
    "ds_read_b128 v[216:219], v125 offset:14336\n"
    "ds_read_b128 v[220:223], v125 offset:15360\n"
    "v_lshlrev_b32 v0, 16, v96\n"
    "v_and_b32 v1, s79, v96\n"
    "v_lshlrev_b32 v2, 16, v97\n"
    "v_and_b32 v3, s79, v97\n"
    "v_lshlrev_b32 v4, 16, v98\n"
    "v_and_b32 v5, s79, v98\n"
    "v_lshlrev_b32 v6, 16, v99\n"
    "v_and_b32 v7, s79, v99\n"
    "v_lshlrev_b32 v8, 16, v100\n"
    "v_and_b32 v9, s79, v100\n"
    "v_lshlrev_b32 v10, 16, v101\n"
    "v_and_b32 v11, s79, v101\n"
    "v_lshlrev_b32 v12, 16, v102\n"
    "v_and_b32 v13, s79, v102\n"
    "v_lshlrev_b32 v14, 16, v103\n"
    "v_and_b32 v15, s79, v103\n"
    "s_waitcnt lgkmcnt(8)\n"
    "v_pk_add_f32 v[160:161], v[160:161], v[176:177]\n"
    "v_pk_add_f32 v[162:163], v[162:163], v[178:179]\n"
    "v_pk_add_f32 v[164:165], v[164:165], v[180:181]\n"
    "v_pk_add_f32 v[166:167], v[166:167], v[182:183]\n"
    "v_pk_add_f32 v[168:169], v[168:169], v[184:185]\n"
    "v_pk_add_f32 v[170:171], v[170:171], v[186:187]\n"
    "v_pk_add_f32 v[172:173], v[172:173], v[188:189]\n"
    "v_pk_add_f32 v[174:175], v[174:175], v[190:191]\n"
    "s_waitcnt lgkmcnt(0)\n"
    "v_pk_add_f32 v[192:193], v[192:193], v[208:209]\n"
    "v_pk_add_f32 v[194:195], v[194:195], v[210:211]\n"
    "v_pk_add_f32 v[196:197], v[196:197], v[212:213]\n"
    "v_pk_add_f32 v[198:199], v[198:199], v[214:215]\n"
    "v_pk_add_f32 v[200:201], v[200:201], v[216:217]\n"
    "v_pk_add_f32 v[202:203], v[202:203], v[218:219]\n"
    "v_pk_add_f32 v[204:205], v[204:205], v[220:221]\n"
    "v_pk_add_f32 v[206:207], v[206:207], v[222:223]\n"
    "v_pk_add_f32 v[160:161], v[160:161], v[192:193]\n"
    "v_pk_add_f32 v[162:163], v[162:163], v[194:195]\n"
    "v_pk_add_f32 v[164:165], v[164:165], v[196:197]\n"
    "v_pk_add_f32 v[166:167], v[166:167], v[198:199]\n"
    "v_pk_add_f32 v[168:169], v[168:169], v[200:201]\n"
    "v_pk_add_f32 v[170:171], v[170:171], v[202:203]\n"
    "v_pk_add_f32 v[172:173], v[172:173], v[204:205]\n"
    "v_pk_add_f32 v[174:175], v[174:175], v[206:207]\n"
    "v_pk_fma_f32 v[0:1], v[160:161], v[104:105], v[0:1]\n"
    "v_pk_fma_f32 v[2:3], v[162:163], v[106:107], v[2:3]\n"
    "v_pk_fma_f32 v[4:5], v[164:165], v[108:109], v[4:5]\n"
    "v_pk_fma_f32 v[6:7], v[166:167], v[110:111], v[6:7]\n"
    "v_pk_fma_f32 v[8:9], v[168:169], v[112:113], v[8:9]\n"
    "v_pk_fma_f32 v[10:11], v[170:171], v[114:115], v[10:11]\n"
    "v_pk_fma_f32 v[12:13], v[172:173], v[116:117], v[12:13]\n"
    "v_pk_fma_f32 v[14:15], v[174:175], v[118:119], v[14:15]\n"
    "v_cvt_pk_bf16_f32 v16, v0, v1\n"
    "v_cvt_pk_bf16_f32 v17, v2, v3\n"
    "v_cvt_pk_bf16_f32 v18, v4, v5\n"
    "v_cvt_pk_bf16_f32 v19, v6, v7\n"
    "v_cvt_pk_bf16_f32 v20, v8, v9\n"
    "v_cvt_pk_bf16_f32 v21, v10, v11\n"
    "v_cvt_pk_bf16_f32 v22, v12, v13\n"
    "v_cvt_pk_bf16_f32 v23, v14, v15\n"
    "s_nop 1\n"
    "v_permlane16_swap_b32 v16, v18\n"
    "v_permlane16_swap_b32 v17, v19\n"
    "v_permlane16_swap_b32 v20, v22\n"
    "v_permlane16_swap_b32 v21, v23\n"
    "buffer_store_dwordx4 v[16:19], v126, s[68:71], s78 offen offset:0\n"
    "buffer_store_dwordx4 v[20:23], v126, s[68:71], s78 offen offset:64\n"
    "s_waitcnt vmcnt(0)\n"
    "s_branch F_end\n"
    "F_fb:\n"
    "s_waitcnt vmcnt(0) lgkmcnt(0)\n"
    "s_barrier\n"
    "s_ff1_i32_b32 s54, s82\n"
    "s_min_u32 s54, s54, 13\n"
    "s_branch F_B\n"
    "F_end:\n"
    :: "s"((unsigned)(size_t)p0), "s"((unsigned)((size_t)p0 >> 32)), "s"((unsigned)(size_t)p1), "s"((unsigned)((size_t)p1 >> 32)),
       "s"((unsigned)(size_t)ws), "s"((unsigned)((size_t)ws >> 32)),
       "s"(w), "s"(blockIdx.x), "s"(l),
       "s"((unsigned)(size_t)p2), "s"((unsigned)((size_t)p2 >> 32)), "s"((unsigned)(size_t)p3), "s"((unsigned)((size_t)p3 >> 32))
    : "v0","v1","v2","v3","v4","v5","v6","v7","v8","v9","v10","v11","v12","v13","v14","v15","v16","v17","v18","v19","v20","v21","v22","v23","v24","v25","v26","v27","v28","v29","v30","v31","v32","v33","v34","v35","v36","v37","v38","v39","v40","v41","v42","v43","v44","v45","v46","v47","v48","v49","v50","v51","v52","v53","v54","v55","v56","v57","v58","v59","v60","v61","v62","v63","v64","v65","v66","v67","v68","v69","v70","v71","v72","v73","v74","v75","v76","v77","v78","v79","v80","v81","v82","v83","v84","v85","v86","v87","v88","v89","v90","v91","v92","v93","v94","v95","v96","v97","v98","v99","v100","v101","v102","v103","v104","v105","v106","v107","v108","v109","v110","v111","v112","v113","v114","v115","v116","v117","v118","v119","v120","v121","v122","v123","v124","v125","v126","v127","v128","v129","v130","v131","v132","v133","v134","v135","v136","v137","v138","v139","v140","v141","v142","v143","v144","v145","v146","v147","v148","v149","v150","v151","v152","v153","v154","v155","v156","v157","v158","v159","v160","v161","v162","v163","v164","v165","v166","v167","v168","v169","v170","v171","v172","v173","v174","v175","v176","v177","v178","v179","v180","v181","v182","v183","v184","v185","v186","v187","v188","v189","v190","v191","v192","v193","v194","v195","v196","v197","v198","v199","v200","v201","v202","v203","v204","v205","v206","v207","v208","v209","v210","v211","v212","v213","v214","v215","v216","v217","v218","v219","v220","v221","v222","v223","v224","v225","v226","v227","v228","v229","v230","v231","v232","v233","v234","v235","v236","v237","v238","v239","v240","v241","v242","v243","v244","v245","v246","v247","v248","v249","v250","v251","v252","v253","v254","v255","a0","a1","a2","a3","a4","a5","a6","a7","a8","a9","a10","a11","a12","a13","a14","a15","a16","a17","a18","a19","a20","a21","a22","a23","a24","a25","a26","a27","a28","a29","a30","a31","a32","a33","a34","a35","a36","a37","a38","a39","a40","a41","a42","a43","a44","a45","a46","a47","a48","a49","a50","a51","a52","a53","a54","a55","a56","a57","a58","a59","a60","a61","a62","a63","a64","a65","a66","a67","a68","a69","a70","a71","a72","a73","a74","a75","a76","a77","a78","a79","a80","a81","a82","a83","a84","a85","a86","a87","a88","a89","a90","a91","a92","a93","a94","a95","a96","a97","a98","a99","a100","a101","a102","a103","a104","a105","a106","a107","a108","a109","a110","a111","a112","a113","a114","a115","a116","a117","a118","a119","a120","a121","a122","a123","a124","a125","a126","a127","a128","a129","a130","a131","a132","a133","a134","a135","a136","a137","a138","a139","a140","a141","a142","a143","a144","a145","a146","a147","a148","a149","a150","a151","a152","a153","a154","a155","a156","a157","a158","a159","a160","a161","a162","a163","a164","a165","a166","a167","a168","a169","a170","a171","a172","a173","a174","a175","a176","a177","a178","a179","a180","a181","a182","a183","a184","a185","a186","a187","a188","a189","a190","a191","a192","a193","a194","a195","a196","a197","a198","a199","a200","a201","a202","a203","a204","a205","a206","a207","a208","a209","a210","a211","a212","a213","a214","a215","a216","a217","a218","a219","a220","a221","a222","a223","a224","a225","a226","a227","a228","a229","a230","a231","a232","a233","a234","a235","a236","a237","a238","a239","a240","a241","a242","a243","a244","a245","a246","a247","a248","a249","a250","a251","a252","a253","a254","a255","s40","s41","s42","s43","s44","s45","s46","s47","s48","s49","s50","s51","s52","s53","s54","s55","s56","s57","s58","s59","s60","s61","s62","s63","s64","s65","s66","s67","s68","s69","s70","s71","s72","s73","s74","s75","s76","s77","s78","s79","s80","s81","s82","s83","s84","s85","s86","s87","s88","s89","s90","s91","s92","s93","s94","s95","s96","s97","s98","s99","vcc","m0","scc","memory");
}

extern "C" void kernel_launch(void* const* d_in, const int* in_sizes, int n_in, void* d_out, int out_size, void* d_ws, size_t ws_size, hipStream_t stream) {
  const u16* x = (const u16*)d_in[0]; const u16* kc = (const u16*)d_in[1]; const u16* vc = (const u16*)d_in[2];
  const u16* wq = (const u16*)d_in[3]; const u16* wo = (const u16*)d_in[4];
  const u16* wg = (const u16*)d_in[6]; const u16* wu = (const u16*)d_in[7]; const u16* wd = (const u16*)d_in[8];
  const u16* c = (const u16*)d_in[9]; const u16* wmod = (const u16*)d_in[10];
  u16* out = (u16*)d_out;
  char* ws = (char*)d_ws;
  float* tab = (float*)ws;
  float* mod = (float*)(ws + 8192);
  u16* nrm = (u16*)(ws + (1 << 16));
  k0<<<NGB + 512, 256, 0, stream>>>(c, wmod, x, tab, mod, nrm, (const char*)wq);
  k1<<<256, 256, 0, stream>>>(wq, kc, vc, wo, ws, (const char*)wg, (const char*)wu, (const char*)wd, x);
  kf<<<224, 256, 0, stream>>>(wg, wu, ws, wd, out);
}
```
